```python
import torch

def _softmax(x, axis=-1):
    return torch.softmax(x, dim=axis)

def _gelu(x):
    return torch.nn.functional.gelu(x, approximate='tanh')

def _silu(x):
    return torch.nn.functional.silu(x)

def _sigmoid(x):
    return torch.sigmoid(x)

def _layernorm(x, axis=-1, eps=1e-05):
    m = x.mean(dim=axis, keepdim=True)
    v = x.var(dim=axis, keepdim=True, unbiased=False)
    return (x - m) / torch.sqrt(v + eps)

def _scatter_add(tgt, idx, src, axis=0):
    return tgt.clone().index_add_(axis, idx, src)

def _segment_sum(src, seg, n):
    out = torch.zeros((n,) + src.shape[1:], dtype=src.dtype)
    return out.index_add_(0, seg, src)

def _pad(x, pw):
    flat = tuple((v for pair in reversed(pw) for v in pair))
    return torch.nn.functional.pad(x, flat)
kernel_name = 'sequential_preLN__mha_block__mixer_gmlp__prompt'

def setup_inputs():

    _w = lambda a: (lambda t: t.to(torch.bfloat16) if t.is_floating_point() else t)(torch.as_tensor(a))

    def _A_setup():
        B, S, D, H = (1, 2048, 576, 32)
        return {'x': _w((lambda *s: torch.randn(*s, dtype=torch.bfloat16) * 0.05)(B, S, D)), 'wqkv': _w((lambda *s: torch.randn(*s, dtype=torch.bfloat16) * 0.05)(D, 3 * D)), 'wo': _w((lambda *s: torch.randn(*s, dtype=torch.bfloat16) * 0.05)(D, D))}
    _A_in = _A_setup()

    def _B_setup():
        B, S, D, F = (1, 2048, 576, 4608)
        Fh = F // 2
        return {'x': _w((lambda *s: torch.randn(*s, dtype=torch.bfloat16) * 0.05)(B, S, D)), 'w_in': _w((lambda *s: torch.randn(*s, dtype=torch.bfloat16) * 0.8)(D, 2 * Fh)), 'w_sp': _w(0.01 * (lambda *s: torch.randn(*s, dtype=torch.bfloat16) * 0.8)(S, S)), 'b_sp': _w(1.0 + 0.01 * (lambda *s: torch.randn(*s, dtype=torch.bfloat16) * 0.05)(S)), 'w_out': _w((lambda *s: torch.randn(*s, dtype=torch.bfloat16) * 0.05)(Fh, D))}
    _B_in = _B_setup()
    return {'A_x': _A_in['x'], 'A_wqkv': _A_in['wqkv'], 'A_wo': _A_in['wo'], 'B_x': _B_in['x'], 'B_w_in': _B_in['w_in'], 'B_w_sp': _B_in['w_sp'], 'B_b_sp': _B_in['b_sp'], 'B_w_out': _B_in['w_out']}

def reference(A_x, A_wqkv, A_wo, B_x, B_w_in, B_w_sp, B_b_sp, B_w_out):

    def _A_ref(x, wqkv, wo):
        B, S, D = x.shape
        H = 32
        Dh = D // H
        qkv = (lambda x, *d: x.permute(*d))((x @ wqkv).reshape(B, S, 3, H, Dh), 2, 0, 3, 1, 4)
        q, k, v = (qkv[0], qkv[1], qkv[2])
        scores = torch.einsum('bhqd,bhkd->bhqk', q, k) * Dh ** (-0.5)
        p = _softmax(scores, axis=-1)
        o = torch.einsum('bhqk,bhkd->bhqd', p, v)
        return (lambda x, *d: x.permute(*d))(o, 0, 2, 1, 3).reshape(B, S, D) @ wo

    def _B_ref(x, w_in, w_sp, b_sp, w_out):
        B, S, D = x.shape
        F = w_in.shape[-1]
        Fh = F // 2
        z = _gelu(torch.einsum('bsd,df->bsf', x, w_in))
        z1 = z[..., :Fh].clone()
        z2 = _layernorm(z[..., Fh:], axis=-1)
        g = torch.einsum('ts,bsf->btf', w_sp, z2) + b_sp[None, :, None]
        return torch.einsum('bsf,fd->bsd', z1 * g, w_out)
    x = A_x
    h = x + _A_ref(_layernorm(x, axis=-1), A_wqkv, A_wo)
    h2 = h + _B_ref(_layernorm(h, axis=-1), B_w_in, B_w_sp, B_b_sp, B_w_out)
    return h2
```

```cpp
#include <hip/hip_runtime.h>
#include <stdint.h>

typedef unsigned short u16;
typedef short bf16x8 __attribute__((ext_vector_type(8)));
typedef short s16x4 __attribute__((ext_vector_type(4)));
typedef float f32x2 __attribute__((ext_vector_type(2)));
typedef float f32x4 __attribute__((ext_vector_type(4)));
typedef float f32x16 __attribute__((ext_vector_type(16)));
typedef unsigned u32x2 __attribute__((ext_vector_type(2)));
typedef unsigned u32x4 __attribute__((ext_vector_type(4)));
typedef __bf16 bf16x2_t __attribute__((ext_vector_type(2)));
typedef __attribute__((address_space(3))) s16x4* lds_s16x4_ptr;
#define DI __device__ __forceinline__

constexpr int S_ = 2048, D_ = 576, H_ = 32, DH = 18, FH = 2304, F2 = 4608;
constexpr int QK_TILE = 1152;
constexpr int V_OFF = 1152;
constexpr int KV_TILE = 1152 + 4 * 304;
constexpr int KV_HEAD = 64 * KV_TILE;
constexpr int Q_HEAD = 64 * QK_TILE;
constexpr size_t WS_Q = 0;
constexpr size_t WS_KV = WS_Q + (size_t)H_ * Q_HEAD;
constexpr size_t WS_AO = WS_KV + (size_t)H_ * KV_HEAD + 8192;
constexpr size_t WS_H = WS_AO + (size_t)S_ * D_ * 2;
constexpr size_t WS_Z1 = WS_H + (size_t)S_ * D_ * 2;
constexpr size_t WS_Z2T = WS_Z1 + (size_t)S_ * FH * 2;
constexpr size_t WS_Z2N = WS_Z2T + (size_t)S_ * FH * 2;
constexpr size_t WS_ST = WS_Z2N + (size_t)S_ * FH * 2;
constexpr size_t WS_U = WS_ST + (size_t)S_ * 16 * 2 * 4;
constexpr size_t WS_END = WS_U + (size_t)S_ * FH * 2;

DI unsigned cvtpk(float lo, float hi) { f32x2 v = {lo, hi}; bf16x2_t b = __builtin_convertvector(v, bf16x2_t); return __builtin_bit_cast(unsigned, b); }
DI float bflo(unsigned u) { return __uint_as_float(u << 16); }
DI float bfhi(unsigned u) { return __uint_as_float(u & 0xffff0000u); }
DI unsigned rfl(unsigned x) { return __builtin_amdgcn_readfirstlane(x); }
DI float dot2(unsigned a, unsigned b, float c) {
  return __builtin_amdgcn_fdot2_f32_bf16(__builtin_bit_cast(bf16x2_t, a), __builtin_bit_cast(bf16x2_t, b), c, false);
}
DI float xor16(float v) { return __shfl_xor(v, 16); }
DI float xor32(float v) { return __shfl_xor(v, 32); }
#define DMAQ(sb, imm, voff, srd, soff) asm volatile("s_add_u32 m0, %0, %1\n\ts_nop 0\n\tbuffer_load_dwordx4 %2, %3, %4 offen lds" :: "s"(sb), "i"(imm), "v"(voff), "s"(srd), "s"(soff) : "scc")
#define WAITV(n) asm volatile("s_waitcnt vmcnt(%0)" :: "i"(n) : "memory")
#define WAITL0() asm volatile("s_waitcnt lgkmcnt(0)" ::: "memory")
#define BAR() asm volatile("s_barrier" ::: "memory")
#define SB() __builtin_amdgcn_sched_barrier(0)
#define MFMA16(a, b, c) __builtin_amdgcn_mfma_f32_16x16x32_bf16((a), (b), (c), 0, 0, 0)
#define MFMA32(a, b, c) __builtin_amdgcn_mfma_f32_32x32x16_bf16((a), (b), (c), 0, 0, 0)
DI __amdgpu_buffer_rsrc_t mksrd(const void* p, unsigned bytes) { return __builtin_amdgcn_make_buffer_rsrc((void*)p, (short)0, (int)bytes, 0x00020000); }
DI s16x4 trread(const char* p) { return __builtin_amdgcn_ds_read_tr16_b64_v4i16((lds_s16x4_ptr)(p)); }

template <int CW_, int MI_, int NT_, int NP_, int NST_, bool BKN_, bool LN_, int KT_>
struct Cfg {
  static constexpr int CW = CW_, MI = MI_, NT = NT_, NP = NP_, NST = NST_, KT = KT_;
  static constexpr bool BKN = BKN_, LN = LN_;
  static constexpr int BM = CW * MI * 16, BN = NT * 16;
  static constexpr int NA = BM / 8, NB = BN / 8, NI = NA + NB;
  static constexpr int ABYTES = BM * 128, BBYTES = BN * 128, STAGE = ABYTES + BBYTES;
  static constexpr int RING = NST * STAGE;
  static constexpr int STAT_OFF = RING;
  static constexpr int LDS = RING + BN * 4;
  static constexpr int THREADS = (CW + NP) * 64;
};

template <class C, int P>
DI void producer(char* smem, unsigned ldsb, __amdgpu_buffer_rsrc_t srdA, __amdgpu_buffer_rsrc_t srdB, int lda, int ldb, int lane) {
  constexpr int Q0 = (C::NI * P) / C::NP, Q1 = (C::NI * (P + 1)) / C::NP, NQ = Q1 - Q0;
  unsigned vo[NQ];
#pragma unroll
  for (int j = 0; j < NQ; ++j) {
    const int q = Q0 + j;
    if (q < C::NA) {
      const int row = 8 * q + (lane >> 3);
      vo[j] = row * (lda * 2) + (((lane & 7) ^ ((row >> 1) & 7)) << 4);
    } else if (!C::BKN) {
      const int row = 8 * (q - C::NA) + (lane >> 3);
      vo[j] = row * (ldb * 2) + (((lane & 7) ^ ((row >> 1) & 7)) << 4);
    } else {
      constexpr int CPR = C::BN / 8;
      const int c = 64 * (q - C::NA) + lane, rho = c / CPR, ch = c - rho * CPR;
      const int k = (rho & ~12) | ((rho & 4) << 1) | ((rho & 8) >> 1);
      vo[j] = k * (ldb * 2) + ch * 16;
    }
  }
  auto issue = [&](int kt) {
    const unsigned sb = ldsb + (kt % C::NST) * C::STAGE;
    const unsigned soA = kt * 128, soB = C::BKN ? kt * 64 * ldb * 2 : kt * 128;
#pragma unroll
    for (int j = 0; j < NQ; ++j) {
      if (Q0 + j < C::NA) DMAQ(sb, (Q0 + j) * 1024, vo[j], srdA, soA); else DMAQ(sb, (Q0 + j) * 1024, vo[j], srdB, soB);
    }
  };
  constexpr int NJ = C::LN ? (C::NT - P + C::NP - 1) / C::NP : 0;
  float cs[NJ > 0 ? NJ : 1];
#pragma unroll
  for (int j = 0; j < NJ; ++j) cs[j] = 0.f;
  const int g = lane >> 4;
  const int tb = (16 * (g >> 1) + 4 * (g & 1) + ((lane & 15) >> 2)) * (C::BN * 2) + (lane & 3) * 8;
  auto colsum = [&](int kt) {
    if constexpr (C::LN) {
      const char* sb = smem + (kt % C::NST) * C::STAGE + C::ABYTES + tb;
#pragma unroll
      for (int jj = 0; jj < NJ; ++jj) {
        const int j = P + jj * C::NP;
#pragma unroll
        for (int kt2 = 0; kt2 < 4; ++kt2) {
          const u32x2 v = __builtin_bit_cast(u32x2, trread(sb + (kt2 >> 1) * (32 * C::BN * 2) + (kt2 & 1) * (8 * C::BN * 2) + j * 32));
          cs[jj] = dot2(v[0], 0x3F803F80u, cs[jj]); cs[jj] = dot2(v[1], 0x3F803F80u, cs[jj]);
        }
      }
      WAITL0();
    }
  };
  constexpr int PF = C::NST - 1;
#pragma unroll
  for (int s = 0; s < PF; ++s) issue(s);
  for (int kt = 0; kt < C::KT - PF; ++kt) { WAITV((PF - 1) * NQ); BAR(); issue(kt + PF); colsum(kt); }
#pragma unroll
  for (int r = PF - 1; r >= 0; --r) { WAITV(r * NQ); BAR(); colsum(C::KT - 1 - r); }
  if constexpr (C::LN) {
    float* st = (float*)(smem + C::STAT_OFF);
#pragma unroll
    for (int jj = 0; jj < NJ; ++jj) {
      float v = cs[jj]; v += xor16(v); v += xor32(v);
      if (lane < 16) st[16 * (P + jj * C::NP) + lane] = v;
    }
    WAITL0();
  }
  BAR();
}

template <class C>
DI void run_producer(char* smem, unsigned ldsb, const u16* Ab, unsigned abytes, const u16* Bb, unsigned bbytes, int lda, int ldb, int p, int lane) {
  __amdgpu_buffer_rsrc_t srdA = mksrd(Ab, abytes), srdB = mksrd(Bb, bbytes);
  if (p == 0) producer<C, 0>(smem, ldsb, srdA, srdB, lda, ldb, lane);
  if constexpr (C::NP > 1) { if (p == 1) producer<C, 1>(smem, ldsb, srdA, srdB, lda, ldb, lane); }
  if constexpr (C::NP > 2) { if (p == 2) producer<C, 2>(smem, ldsb, srdA, srdB, lda, ldb, lane); }
  if constexpr (C::NP > 3) { if (p == 3) producer<C, 3>(smem, ldsb, srdA, srdB, lda, ldb, lane); }
}

template <class C>
DI void consumer(char* smem, int w, int lane, f32x4 (&acc)[C::MI][C::NT], float (&rs)[C::MI], float (&rq)[C::MI]) {
  constexpr int MI = C::MI, NT = C::NT;
  const int g = lane >> 4, fr = lane & 15, f = fr >> 1;
  const int la0 = fr * 128 + ((g ^ f) << 4), la1 = fr * 128 + (((4 + g) ^ f) << 4);
  const int tb = (16 * (g >> 1) + 4 * (g & 1) + (fr >> 2)) * (C::BN * 2) + (lane & 3) * 8;
#pragma unroll
  for (int i = 0; i < MI; ++i) {
    rs[i] = 0.f; rq[i] = 0.f;
#pragma unroll
    for (int j = 0; j < NT; ++j) acc[i][j] = f32x4{0.f, 0.f, 0.f, 0.f};
  }
  bf16x8 a0[MI], b0[NT], a1[MI], b1[NT];
  auto rd = [&](bf16x8* a, bf16x8* bb, int kt, int ks) {
    const char* sb = smem + (kt % C::NST) * C::STAGE;
    const int lo = ks ? la1 : la0;
#pragma unroll
    for (int i = 0; i < MI; ++i) a[i] = *(const bf16x8*)(sb + (w * MI + i) * 2048 + lo);
#pragma unroll
    for (int j = 0; j < NT; ++j) {
      if constexpr (C::BKN) {
        const char* p = sb + C::ABYTES + tb + ks * (32 * C::BN * 2) + j * 32;
        const s16x4 lo4 = trread(p), hi4 = trread(p + 8 * C::BN * 2);
        bb[j] = __builtin_shufflevector(lo4, hi4, 0, 1, 2, 3, 4, 5, 6, 7);
      } else {
        bb[j] = *(const bf16x8*)(sb + C::ABYTES + j * 2048 + lo);
      }
    }
  };
  auto mm = [&](bf16x8* a, bf16x8* bb) {
#pragma unroll
    for (int j = 0; j < NT; ++j)
#pragma unroll
      for (int i = 0; i < MI; ++i) acc[i][j] = MFMA16(bb[j], a[i], acc[i][j]);
    if constexpr (C::LN) {
#pragma unroll
      for (int i = 0; i < MI; ++i) {
        const u32x4 v = __builtin_bit_cast(u32x4, a[i]);
#pragma unroll
        for (int e = 0; e < 4; ++e) { rs[i] = dot2(v[e], 0x3F803F80u, rs[i]); rq[i] = dot2(v[e], v[e], rq[i]); }
      }
    }
  };
  BAR();
  rd(a0, b0, 0, 0);
  for (int kt = 0; kt < C::KT - 1; ++kt) {
    SB();
    rd(a1, b1, kt, 1);
    mm(a0, b0);
    SB();
    WAITL0(); BAR();
    SB();
    rd(a0, b0, kt + 1, 0);
    mm(a1, b1);
  }
  SB();
  rd(a1, b1, C::KT - 1, 1);
  mm(a0, b0);
  mm(a1, b1);
  if constexpr (C::LN) {
#pragma unroll
    for (int i = 0; i < MI; ++i) { rs[i] += xor16(rs[i]); rs[i] += xor32(rs[i]); rq[i] += xor16(rq[i]); rq[i] += xor32(rq[i]); }
  }
  WAITL0(); BAR();
}

using C1 = Cfg<4, 2, 9, 4, 4, true, true, 9>;
__global__ __launch_bounds__(C1::THREADS, 2) void k_qkv(const u16* __restrict__ x, const u16* __restrict__ wqkv, char* __restrict__ ws) {
  extern __shared__ __attribute__((aligned(16))) char smem[];
  const int tid = threadIdx.x, lane = tid & 63, w = rfl(tid >> 6);
  const int b = blockIdx.x, xcd = b & 7, idx = b >> 3;
  const int tm = (xcd & 3) * 4 + (idx & 3), tn = (xcd >> 2) * 6 + (idx >> 2);
  if (w >= C1::CW) {
    run_producer<C1>(smem, (unsigned)(size_t)smem, x + (size_t)tm * 128 * D_, 128 * D_ * 2, wqkv + tn * 144, (unsigned)((D_ * 3 * D_ - tn * 144) * 2), D_, 3 * D_, w - C1::CW, lane);
    return;
  }
  f32x4 acc[2][9]; float rs[2], rq[2];
  consumer<C1>(smem, w, lane, acc, rs, rq);
  const int g = lane >> 4, fr = lane & 15;
  const float* cs = (const float*)(smem + C1::STAT_OFF);
  const int kind = tn >> 2, head0 = (tn & 3) * 8;
  const int tile32 = tm * 4 + w;
  float mu[2], rstd[2];
#pragma unroll
  for (int i = 0; i < 2; ++i) {
    mu[i] = rs[i] * (1.f / D_);
    const float var = fmaxf(rq[i] * (1.f / D_) - mu[i] * mu[i], 0.f);
    rstd[i] = rsqrtf(var + 1e-5f);
  }
  if (kind < 2) {
    const float sc = kind == 0 ? 0.23570226039551584f * 1.4426950408889634f : 1.f;
    char* base = ws + (kind == 0 ? WS_Q + (size_t)tile32 * QK_TILE : WS_KV + (size_t)tile32 * KV_TILE);
    const int hstride = kind == 0 ? Q_HEAD : KV_HEAD;
#pragma unroll
    for (int j = 0; j < 9; ++j) {
      const f32x4 c4 = *(const f32x4*)(cs + 16 * j + 4 * g);
#pragma unroll
      for (int rr = 0; rr < 2; ++rr) {
        const int c = 16 * j + 4 * g + 2 * rr, hl = c / 18, p = (c - hl * 18) >> 1;
        char* dst = base + (size_t)(head0 + hl) * hstride;
#pragma unroll
        for (int i = 0; i < 2; ++i) {
          const int tok = 16 * i + fr;
          const int off = p < 8 ? (tok + 32 * (p >> 2)) * 16 + (p & 3) * 4 : 1024 + tok * 4;
          const float v0 = (acc[i][j][2 * rr] - mu[i] * c4[2 * rr]) * (rstd[i] * sc);
          const float v1 = (acc[i][j][2 * rr + 1] - mu[i] * c4[2 * rr + 1]) * (rstd[i] * sc);
          *(unsigned*)(dst + off) = cvtpk(v0, v1);
        }
      }
    }
  } else {
    char* reg = smem + w * (32 * 288);
#pragma unroll
    for (int j = 0; j < 9; ++j) {
      const f32x4 c4 = *(const f32x4*)(cs + 16 * j + 4 * g);
#pragma unroll
      for (int i = 0; i < 2; ++i) {
        u32x2 v;
        v[0] = cvtpk((acc[i][j][0] - mu[i] * c4[0]) * rstd[i], (acc[i][j][1] - mu[i] * c4[1]) * rstd[i]);
        v[1] = cvtpk((acc[i][j][2] - mu[i] * c4[2]) * rstd[i], (acc[i][j][3] - mu[i] * c4[3]) * rstd[i]);
        *(u32x2*)(reg + (16 * i + fr) * 288 + (16 * j + 4 * g) * 2) = v;
      }
    }
    WAITL0();
    char* base = ws + WS_KV + (size_t)tile32 * KV_TILE + V_OFF;
    const int s = g >> 1, h = g & 1;
    const char* rp = reg + (16 * s + 4 * h + (fr >> 2)) * 288 + (lane & 3) * 8;
#pragma unroll
    for (int j = 0; j < 9; ++j) {
      const s16x4 lo4 = trread(rp + j * 32), hi4 = trread(rp + 8 * 288 + j * 32);
      const bf16x8 v = __builtin_shufflevector(lo4, hi4, 0, 1, 2, 3, 4, 5, 6, 7);
      const int c = 16 * j + fr, hl = c / 18, d = c - hl * 18;
      *(bf16x8*)(base + (size_t)(head0 + hl) * KV_HEAD + s * 608 + h * 304 + d * 16) = v;
    }
    if (lane < 32) {
      const u32x4 one = {0x3F803F80u, 0x3F803F80u, 0x3F803F80u, 0x3F803F80u};
      *(u32x4*)(base + (size_t)(head0 + (lane >> 2)) * KV_HEAD + (lane & 3) * 304 + 18 * 16) = one;
    }
  }
}

constexpr int ATT_LDS = 152 * 1024;
__global__ __launch_bounds__(512, 2) void k_attn(char* __restrict__ ws) {
  extern __shared__ __attribute__((aligned(16))) char smem[];
  const int tid = threadIdx.x, lane = tid & 63, w = rfl(tid >> 6);
  const int b = blockIdx.x, xcd = b & 7, idx = b >> 3;
  const int head = xcd * 4 + (idx & 3), qb = idx >> 2;
  const int q31 = lane & 31, h = lane >> 5;
  const char* qp = ws + WS_Q + (size_t)head * Q_HEAD + (size_t)(qb * 8 + w) * QK_TILE;
  const bf16x8 qf1 = *(const bf16x8*)(qp + lane * 16);
  const unsigned q2 = *(const unsigned*)(qp + 1024 + q31 * 4);
  {
    __amdgpu_buffer_rsrc_t srd = mksrd(ws + WS_KV + (size_t)head * KV_HEAD, KV_HEAD);
    const unsigned ldsb = (unsigned)(size_t)smem + w * 1024, vo = lane * 16 + w * 1024;
#pragma unroll
    for (int j = 0; j < 19; ++j) DMAQ(ldsb, j * 8192, vo, srd, (unsigned)(j * 8192));
  }
  u32x4 q2v = {h == 0 ? q2 : 0u, 0u, 0u, 0u};
  const bf16x8 qf2 = __builtin_bit_cast(bf16x8, q2v);
  const int dcl = q31 < 19 ? q31 : (q31 >= 20 && q31 < 28 ? 12 : 16);
  const int koff = lane * 16, k2off = 1024 + q31 * 4, voff = V_OFF + h * 304 + dcl * 16;
  f32x16 o;
#pragma unroll
  for (int r = 0; r < 16; ++r) o[r] = 0.f;
  auto scores = [&](int t, const f32x16& c) {
    const char* tp = smem + t * KV_TILE;
    const bf16x8 k1 = *(const bf16x8*)(tp + koff);
    u32x4 k2v = {*(const unsigned*)(tp + k2off), 0u, 0u, 0u};
    f32x16 s = MFMA32(k1, qf1, c);
    return MFMA32(__builtin_bit_cast(bf16x8, k2v), qf2, s);
  };
  auto pv = [&](int t, const f32x16& p) {
    const char* tp = smem + t * KV_TILE + voff;
    const bf16x8 v0 = *(const bf16x8*)(tp), v1 = *(const bf16x8*)(tp + 608);
    u32x4 p0, p1;
#pragma unroll
    for (int e = 0; e < 4; ++e) { p0[e] = cvtpk(p[2 * e], p[2 * e + 1]); p1[e] = cvtpk(p[8 + 2 * e], p[8 + 2 * e + 1]); }
    o = MFMA32(v0, __builtin_bit_cast(bf16x8, p0), o);
    o = MFMA32(v1, __builtin_bit_cast(bf16x8, p1), o);
  };
  f32x16 zero;
#pragma unroll
  for (int r = 0; r < 16; ++r) zero[r] = 0.f;
  WAITV(16); BAR();
  f32x16 negm;
  {
    const f32x16 s = scores(0, zero);
    float m = s[0];
#pragma unroll
    for (int r = 1; r < 16; ++r) m = fmaxf(m, s[r]);
    m = fmaxf(m, xor32(m));
#pragma unroll
    for (int r = 0; r < 16; ++r) negm[r] = -m;
  }
#define CHUNK(c, nv) { if (c > 0) { WAITV(nv); BAR(); } \
    for (int t = 8 * c; t < 8 * c + 8; ++t) { f32x16 s = scores(t, negm); _Pragma("unroll") for (int r = 0; r < 16; ++r) s[r] = __builtin_amdgcn_exp2f(s[r]); pv(t, s); } }
  CHUNK(0, 16) CHUNK(1, 14) CHUNK(2, 12) CHUNK(3, 9) CHUNK(4, 7) CHUNK(5, 5) CHUNK(6, 2) CHUNK(7, 0)
#undef CHUNK
  float l = __shfl(o[10], q31);
  bool ok = l < 1.8446744e19f;
#pragma unroll
  for (int r = 0; r < 12; ++r) ok = ok && (fabsf(o[r]) <= 3.0e38f);
  if (!__all(ok)) {
    float m = -3.0e38f;
#pragma unroll
    for (int r = 0; r < 16; ++r) o[r] = 0.f;
    for (int t = 0; t < 64; ++t) {
      f32x16 s = scores(t, zero);
      float tmax = s[0];
#pragma unroll
      for (int r = 1; r < 16; ++r) tmax = fmaxf(tmax, s[r]);
      tmax = fmaxf(tmax, xor32(tmax));
      const float mn = fmaxf(m, tmax), alpha = __builtin_amdgcn_exp2f(m - mn);
      m = mn;
#pragma unroll
      for (int r = 0; r < 16; ++r) { o[r] *= alpha; s[r] = __builtin_amdgcn_exp2f(s[r] - mn); }
      pv(t, s);
    }
    l = __shfl(o[10], q31);
  }
  const float inv = 1.f / l;
  u16* op = (u16*)(ws + WS_AO) + (size_t)((qb * 8 + w) * 32 + q31) * D_ + head * DH + 4 * h;
  *(unsigned*)(op + 0) = cvtpk(o[0] * inv, o[1] * inv);
  *(unsigned*)(op + 2) = cvtpk(o[2] * inv, o[3] * inv);
  *(unsigned*)(op + 8) = cvtpk(o[4] * inv, o[5] * inv);
  *(unsigned*)(op + 10) = cvtpk(o[6] * inv, o[7] * inv);
  if (h == 0) *(unsigned*)(op + 16) = cvtpk(o[8] * inv, o[9] * inv);
}

template <int KT>
__global__ __launch_bounds__(384, 2) void k_res(const u16* __restrict__ A, const u16* __restrict__ W, const u16* __restrict__ res, u16* __restrict__ out) {
  using C = Cfg<4, 1, 5, 2, 8, true, false, KT>;
  extern __shared__ __attribute__((aligned(16))) char smem[];
  const int tid = threadIdx.x, lane = tid & 63, w = rfl(tid >> 6);
  const int b = blockIdx.x, xcd = b & 7, idx = b >> 3;
  const int tm = (xcd & 3) * 8 + (idx & 7), tn = (xcd >> 2) * 4 + (idx >> 3);
  constexpr int K = KT * 64;
  if (w >= C::CW) {
    run_producer<C>(smem, (unsigned)(size_t)smem, A + (size_t)tm * 64 * K, 64 * K * 2, W + tn * 72, (unsigned)((K * D_ - tn * 72) * 2), K, D_, w - C::CW, lane);
    return;
  }
  f32x4 acc[1][5]; float rs[1], rq[1];
  consumer<C>(smem, w, lane, acc, rs, rq);
  const int g = lane >> 4, fr = lane & 15;
  const size_t ro = (size_t)(tm * 64 + w * 16 + fr) * D_ + tn * 72 + 4 * g;
#pragma unroll
  for (int j = 0; j < 5; ++j) {
    if (j < 4 || g < 2) {
      const u32x2 r = *(const u32x2*)(res + ro + 16 * j);
      u32x2 v;
      v[0] = cvtpk(bflo(r[0]) + acc[0][j][0], bfhi(r[0]) + acc[0][j][1]);
      v[1] = cvtpk(bflo(r[1]) + acc[0][j][2], bfhi(r[1]) + acc[0][j][3]);
      *(u32x2*)(out + ro + 16 * j) = v;
    }
  }
}

using C4 = Cfg<8, 2, 9, 4, 3, true, true, 9>;
DI float gelu_tanh(float x) {
  const float c1 = -2.f * 0.7978845608028654f * 1.4426950408889634f, c2 = c1 * 0.044715f;
  const float e = __builtin_amdgcn_exp2f(x * fmaf(x * x, c2, c1));
  return x * __builtin_amdgcn_rcpf(1.f + e);
}
__global__ __launch_bounds__(C4::THREADS, 3) void k_win(const u16* __restrict__ win, char* __restrict__ ws) {
  extern __shared__ __attribute__((aligned(16))) char smem[];
  const int tid = threadIdx.x, lane = tid & 63, w = rfl(tid >> 6);
  const int b = blockIdx.x, xcd = b & 7, idx = b >> 3;
  const int tm = (xcd & 1) * 4 + (idx & 3), tn = (xcd >> 1) * 8 + (idx >> 2);
  const u16* hsrc = (const u16*)(ws + WS_H);
  if (w >= C4::CW) {
    run_producer<C4>(smem, (unsigned)(size_t)smem, hsrc + (size_t)tm * 256 * D_, 256 * D_ * 2, win + tn * 144, (unsigned)((D_ * F2 - tn * 144) * 2), D_, F2, w - C4::CW, lane);
    return;
  }
  f32x4 acc[2][9]; float rs[2], rq[2];
  consumer<C4>(smem, w, lane, acc, rs, rq);
  const int g = lane >> 4, fr = lane & 15;
  const float* cs = (const float*)(smem + C4::STAT_OFF);
  char* reg = smem + w * (32 * 304);
  const int tok0 = tm * 256 + w * 32;
#pragma unroll
  for (int i = 0; i < 2; ++i) {
    const float mu = rs[i] * (1.f / D_);
    const float rstd = rsqrtf(fmaxf(rq[i] * (1.f / D_) - mu * mu, 0.f) + 1e-5f);
    float s1 = 0.f, s2 = 0.f;
#pragma unroll
    for (int j = 0; j < 9; ++j) {
      const f32x4 c4 = *(const f32x4*)(cs + 16 * j + 4 * g);
      float z[4];
#pragma unroll
      for (int r = 0; r < 4; ++r) { z[r] = gelu_tanh((acc[i][j][r] - mu * c4[r]) * rstd); s1 += z[r]; s2 = fmaf(z[r], z[r], s2); }
      u32x2 v = {cvtpk(z[0], z[1]), cvtpk(z[2], z[3])};
      *(u32x2*)(reg + (16 * i + fr) * 304 + (16 * j + 4 * g) * 2) = v;
    }
    if (tn >= 16) {
      s1 += xor16(s1); s1 += xor32(s1); s2 += xor16(s2); s2 += xor32(s2);
      if (g == 0) *(f32x2*)(ws + WS_ST + ((size_t)(tok0 + 16 * i + fr) * 16 + (tn - 16)) * 8) = f32x2{s1, s2};
    }
  }
  WAITL0();
  if (tn < 16) {
    u16* zb = (u16*)(ws + WS_Z1) + (size_t)tok0 * FH + tn * 144;
#pragma unroll
    for (int i = 0; i < 9; ++i) {
      const int c = i * 64 + lane, row = c / 18, ch = c - row * 18;
      *(u32x4*)(zb + (size_t)row * FH + ch * 8) = *(const u32x4*)(reg + row * 304 + ch * 16);
    }
  } else {
    u16* zb = (u16*)(ws + WS_Z2T) + (size_t)((tn - 16) * 144 + fr) * S_ + tok0 + 8 * g;
    const char* rp = reg + (8 * g + (fr >> 2)) * 304 + (lane & 3) * 8;
#pragma unroll
    for (int j = 0; j < 9; ++j) {
      const s16x4 lo4 = trread(rp + j * 32), hi4 = trread(rp + 4 * 304 + j * 32);
      *(bf16x8*)(zb + (size_t)j * 16 * S_) = __builtin_shufflevector(lo4, hi4, 0, 1, 2, 3, 4, 5, 6, 7);
    }
  }
}

__global__ __launch_bounds__(256) void k_norm(char* __restrict__ ws) {
  __shared__ f32x2 st[256];
  const int tid = threadIdx.x, tb = blockIdx.x & 7, fb = blockIdx.x >> 3;
  {
    const f32x4* p = (const f32x4*)(ws + WS_ST + (size_t)(tb * 256 + tid) * 128);
    float s1 = 0.f, s2 = 0.f;
#pragma unroll
    for (int i = 0; i < 8; ++i) { const f32x4 v = p[i]; s1 += v[0] + v[2]; s2 += v[1] + v[3]; }
    const float mu = s1 * (1.f / FH);
    st[tid] = f32x2{mu, rsqrtf(fmaxf(s2 * (1.f / FH) - mu * mu, 0.f) + 1e-5f)};
  }
  __syncthreads();
  const int c = tid & 31, r0 = tid >> 5;
  f32x2 m[8];
#pragma unroll
  for (int e = 0; e < 8; ++e) m[e] = st[c * 8 + e];
  const u16* src = (const u16*)(ws + WS_Z2T) + (size_t)(fb * 72 + r0) * S_ + tb * 256 + c * 8;
  u16* dst = (u16*)(ws + WS_Z2N) + (size_t)(fb * 72 + r0) * S_ + tb * 256 + c * 8;
  u32x4 v[9];
#pragma unroll
  for (int i = 0; i < 9; ++i) v[i] = *(const u32x4*)(src + (size_t)i * 8 * S_);
#pragma unroll
  for (int i = 0; i < 9; ++i) {
    u32x4 o;
#pragma unroll
    for (int e = 0; e < 4; ++e) o[e] = cvtpk((bflo(v[i][e]) - m[2 * e][0]) * m[2 * e][1], (bfhi(v[i][e]) - m[2 * e + 1][0]) * m[2 * e + 1][1]);
    *(u32x4*)(dst + (size_t)i * 8 * S_) = o;
  }
}

using C6 = Cfg<4, 2, 9, 4, 4, false, false, 32>;
__global__ __launch_bounds__(C6::THREADS, 2) void k_sp(const u16* __restrict__ wsp, const u16* __restrict__ bsp, char* __restrict__ ws) {
  extern __shared__ __attribute__((aligned(16))) char smem[];
  const int tid = threadIdx.x, lane = tid & 63, w = rfl(tid >> 6);
  const int b = blockIdx.x, xcd = b & 7, idx = b >> 3;
  const int tm = (xcd & 1) * 8 + (idx & 7), tn = (xcd >> 1) * 4 + (idx >> 3);
  if (w >= C6::CW) {
    run_producer<C6>(smem, (unsigned)(size_t)smem, wsp + (size_t)tm * 128 * S_, 128 * S_ * 2, (const u16*)(ws + WS_Z2N) + (size_t)tn * 144 * S_, 144 * S_ * 2, S_, S_, w - C6::CW, lane);
    return;
  }
  const int g = lane >> 4, fr = lane & 15;
  const int tok0 = tm * 128 + w * 32;
  const u16* zb = (const u16*)(ws + WS_Z1) + (size_t)tok0 * FH + tn * 144;
  f32x4 acc[2][9]; float rs[2], rq[2];
  consumer<C6>(smem, w, lane, acc, rs, rq);
  u32x4 z[9];
#pragma unroll
  for (int i = 0; i < 9; ++i) { const int c = i * 64 + lane, row = c / 18, ch = c - row * 18; z[i] = *(const u32x4*)(zb + (size_t)row * FH + ch * 8); }
  char* reg = smem + w * (32 * 592);
#pragma unroll
  for (int i = 0; i < 2; ++i) {
    const float bias = bflo(bsp[tok0 + 16 * i + fr]);
#pragma unroll
    for (int j = 0; j < 9; ++j) *(f32x4*)(reg + (16 * i + fr) * 592 + (16 * j + 4 * g) * 4) = acc[i][j] + bias;
  }
  WAITL0();
  u16* ub = (u16*)(ws + WS_U) + (size_t)tok0 * FH + tn * 144;
#pragma unroll
  for (int i = 0; i < 9; ++i) {
    const int c = i * 64 + lane, row = c / 18, ch = c - row * 18;
    const f32x4 g0 = *(const f32x4*)(reg + row * 592 + ch * 32), g1 = *(const f32x4*)(reg + row * 592 + ch * 32 + 16);
    u32x4 o;
    o[0] = cvtpk(bflo(z[i][0]) * g0[0], bfhi(z[i][0]) * g0[1]);
    o[1] = cvtpk(bflo(z[i][1]) * g0[2], bfhi(z[i][1]) * g0[3]);
    o[2] = cvtpk(bflo(z[i][2]) * g1[0], bfhi(z[i][2]) * g1[1]);
    o[3] = cvtpk(bflo(z[i][3]) * g1[2], bfhi(z[i][3]) * g1[3]);
    *(u32x4*)(ub + (size_t)row * FH + ch * 8) = o;
  }
}

extern "C" void kernel_launch(void* const* d_in, const int* in_sizes, int n_in, void* d_out, int out_size, void* d_ws, size_t ws_size, hipStream_t stream) {
  const u16* x = (const u16*)d_in[0];
  const u16* wqkv = (const u16*)d_in[1];
  const u16* wo = (const u16*)d_in[2];
  const u16* win = (const u16*)d_in[4];
  const u16* wsp = (const u16*)d_in[5];
  const u16* bsp = (const u16*)d_in[6];
  const u16* wout = (const u16*)d_in[7];
  char* ws = (char*)d_ws;
  using CR9 = Cfg<4, 1, 5, 2, 8, true, false, 9>;
  (void)hipFuncSetAttribute((const void*)k_qkv, hipFuncAttributeMaxDynamicSharedMemorySize, C1::LDS);
  (void)hipFuncSetAttribute((const void*)k_attn, hipFuncAttributeMaxDynamicSharedMemorySize, ATT_LDS);
  (void)hipFuncSetAttribute((const void*)k_res<9>, hipFuncAttributeMaxDynamicSharedMemorySize, CR9::LDS);
  (void)hipFuncSetAttribute((const void*)k_res<36>, hipFuncAttributeMaxDynamicSharedMemorySize, CR9::LDS);
  (void)hipFuncSetAttribute((const void*)k_win, hipFuncAttributeMaxDynamicSharedMemorySize, C4::LDS);
  (void)hipFuncSetAttribute((const void*)k_sp, hipFuncAttributeMaxDynamicSharedMemorySize, C6::LDS);
  hipLaunchKernelGGL(k_qkv, dim3(192), dim3(C1::THREADS), C1::LDS, stream, x, wqkv, ws);
  hipLaunchKernelGGL(k_attn, dim3(256), dim3(512), ATT_LDS, stream, ws);
  hipLaunchKernelGGL(k_res<9>, dim3(256), dim3(CR9::THREADS), CR9::LDS, stream, (const u16*)(ws + WS_AO), wo, x, (u16*)(ws + WS_H));
  hipLaunchKernelGGL(k_win, dim3(256), dim3(C4::THREADS), C4::LDS, stream, win, ws);
  hipLaunchKernelGGL(k_norm, dim3(256), dim3(256), 0, stream, ws);
  hipLaunchKernelGGL(k_sp, dim3(256), dim3(C6::THREADS), C6::LDS, stream, wsp, bsp, ws);
  hipLaunchKernelGGL(k_res<36>, dim3(256), dim3(CR9::THREADS), CR9::LDS, stream, (const u16*)(ws + WS_U), wout, (const u16*)(ws + WS_H), (u16*)d_out);
}
```

```cpp
#include <hip/hip_runtime.h>
#include <stdint.h>

typedef unsigned short u16;
typedef short bf16x8 __attribute__((ext_vector_type(8)));
typedef short s16x4 __attribute__((ext_vector_type(4)));
typedef float f32x2 __attribute__((ext_vector_type(2)));
typedef float f32x4 __attribute__((ext_vector_type(4)));
typedef float f32x16 __attribute__((ext_vector_type(16)));
typedef unsigned u32x2 __attribute__((ext_vector_type(2)));
typedef unsigned u32x4 __attribute__((ext_vector_type(4)));
typedef __bf16 bf16x2_t __attribute__((ext_vector_type(2)));
typedef __attribute__((address_space(3))) s16x4* lds_s16x4_ptr;
#define DI __device__ __forceinline__

constexpr int S_ = 2048, D_ = 576, H_ = 32, DH = 18, FH = 2304, F2 = 4608;
constexpr int QK_TILE = 1152;
constexpr int V_OFF = 1152;
constexpr int KV_TILE = 1152 + 4 * 304;
constexpr int KV_HEAD = 64 * KV_TILE;
constexpr int Q_HEAD = 64 * QK_TILE;
constexpr int WIN_TB = 64 * 288, WR_TB = 64 * 160;
constexpr size_t WS_Q = 0;
constexpr size_t WS_KV = WS_Q + (size_t)H_ * Q_HEAD;
constexpr size_t WS_AO = WS_KV + (size_t)H_ * KV_HEAD + 8192;
constexpr size_t WS_H = WS_AO + (size_t)S_ * D_ * 2;
constexpr size_t WS_Z1 = WS_H + (size_t)S_ * D_ * 2;
constexpr size_t WS_Z2T = WS_Z1 + (size_t)S_ * FH * 2;
constexpr size_t WS_Z2N = WS_Z2T + (size_t)S_ * FH * 2;
constexpr size_t WS_ST = WS_Z2N + (size_t)S_ * FH * 2;
constexpr size_t WS_CNT = WS_ST + (size_t)S_ * 16 * 2 * 4;
constexpr size_t WS_HST = WS_ST + (size_t)S_ * 32 * 2 * 4;
constexpr size_t WS_U = WS_HST + (size_t)S_ * 8 * 2 * 4;
constexpr size_t WS_WIN = WS_U + (size_t)S_ * FH * 2;
constexpr size_t WS_WOUT = WS_WIN + (size_t)32 * 9 * WIN_TB;
constexpr size_t WS_WO = WS_WOUT + (size_t)8 * 36 * WR_TB;
constexpr size_t WS_END = WS_WO + (size_t)8 * 9 * WR_TB;

DI unsigned cvtpk(float lo, float hi) { f32x2 v = {lo, hi}; bf16x2_t b = __builtin_convertvector(v, bf16x2_t); return __builtin_bit_cast(unsigned, b); }
DI float bflo(unsigned u) { return __uint_as_float(u << 16); }
DI float bfhi(unsigned u) { return __uint_as_float(u & 0xffff0000u); }
DI unsigned rfl(unsigned x) { return __builtin_amdgcn_readfirstlane(x); }
DI float xor16(float v) { return __shfl_xor(v, 16); }
DI float xor32(float v) { return __shfl_xor(v, 32); }
#define DMAQ(sb, imm, voff, srd, soff) asm volatile("s_add_u32 m0, %0, %1\n\ts_nop 0\n\tbuffer_load_dwordx4 %2, %3, %4 offen lds" :: "s"(sb), "i"(imm), "v"(voff), "s"(srd), "s"(soff) : "scc")
#define WAITV(n) asm volatile("s_waitcnt vmcnt(%0)" :: "i"(n) : "memory")
#define WAITL0() asm volatile("s_waitcnt lgkmcnt(0)" ::: "memory")
#define BAR() asm volatile("s_barrier" ::: "memory")
#define SB() __builtin_amdgcn_sched_barrier(0)
#define MFMA16(a, b, c) __builtin_amdgcn_mfma_f32_16x16x32_bf16((a), (b), (c), 0, 0, 0)
#define MFMA32(a, b, c) __builtin_amdgcn_mfma_f32_32x32x16_bf16((a), (b), (c), 0, 0, 0)
#define VREG(x) asm volatile("" : "+v"(x))
DI __amdgpu_buffer_rsrc_t mksrd(const void* p, unsigned bytes) { return __builtin_amdgcn_make_buffer_rsrc((void*)p, (short)0, (int)bytes, 0x00020000); }
DI s16x4 trread(const char* p) { return __builtin_amdgcn_ds_read_tr16_b64_v4i16((lds_s16x4_ptr)(p)); }
DI bf16x8 cat8(s16x4 lo, s16x4 hi) { return __builtin_shufflevector(lo, hi, 0, 1, 2, 3, 4, 5, 6, 7); }
DI bf16x8 ones8() { const u32x4 o = {0x3F803F80u, 0x3F803F80u, 0x3F803F80u, 0x3F803F80u}; return __builtin_bit_cast(bf16x8, o); }
#define ST_AUX 16
DI void st16(__amdgpu_buffer_rsrc_t srd, unsigned off, u32x4 v) { __builtin_amdgcn_raw_buffer_store_b128(v, srd, off, 0, ST_AUX); }
#define TOUCH(dummy, ptr) asm volatile("global_load_dword %0, %1, off" : "+v"(dummy) : "v"(ptr))
#define STAMP(kid, slot)
#define STAMP_END(kid)

enum { B_NK = 0, B_KN = 1, B_TILED = 2 };
template <int BM_, int NT_, int NP_, int NST_, int BMODE_, int KT_, bool COLSUM_>
struct Cfg {
  static constexpr int BM = BM_, NT = NT_, NP = NP_, NST = NST_, KT = KT_, BMODE = BMODE_;
  static constexpr bool BKN = BMODE_ != B_NK, COLSUM = COLSUM_;
  static constexpr int BN = NT * 16;
  static constexpr int NA = BM / 8, NB = BN / 8, NI = NA + NB;
  static constexpr int ABYTES = BM * 128, BBYTES = BN * 128, STAGE = ABYTES + BBYTES;
  static constexpr int RING = NST * STAGE;
  static constexpr int CS_OFF = RING;
  static constexpr int RS_OFF = RING + BN * 4;
  static constexpr int LDS = RS_OFF + BM * 8;
};

template <class C, int P, int TAILN, int TCPR>
DI void producer(char* smem, unsigned ldsb, __amdgpu_buffer_rsrc_t srdA, __amdgpu_buffer_rsrc_t srdB, int lda, int ldb, int lane, __amdgpu_buffer_rsrc_t srdT, int ldt) {
  constexpr int Q0 = (C::NI * P) / C::NP, Q1 = (C::NI * (P + 1)) / C::NP, NQ = Q1 - Q0;
  unsigned vo[NQ];
#pragma unroll
  for (int j = 0; j < NQ; ++j) {
    const int q = Q0 + j;
    if (q < C::NA) {
      const int row = 8 * q + (lane >> 3);
      vo[j] = row * (lda * 2) + (((lane & 7) ^ ((row >> 1) & 7)) << 4);
    } else if (C::BMODE == B_NK) {
      const int row = 8 * (q - C::NA) + (lane >> 3);
      vo[j] = row * (ldb * 2) + (((lane & 7) ^ ((row >> 1) & 7)) << 4);
    } else if (C::BMODE == B_KN) {
      constexpr int CPR = C::BN / 8;
      const int c = 64 * (q - C::NA) + lane, rho = c / CPR, ch = c - rho * CPR;
      const int k = (rho & ~12) | ((rho & 4) << 1) | ((rho & 8) >> 1);
      vo[j] = k * (ldb * 2) + ch * 16;
    } else {
      vo[j] = (q - C::NA) * 1024 + lane * 16;
    }
  }
  auto issue = [&](int kt) {
    const unsigned sb = ldsb + (kt % C::NST) * C::STAGE;
    const unsigned soA = kt * 128, soB = C::BMODE == B_NK ? kt * 128 : (C::BMODE == B_KN ? kt * 64 * ldb * 2 : kt * C::BBYTES);
#pragma unroll
    for (int j = 0; j < NQ; ++j) {
      if (Q0 + j < C::NA) DMAQ(sb, (Q0 + j) * 1024, vo[j], srdA, soA); else DMAQ(sb, (Q0 + j) * 1024, vo[j], srdB, soB);
    }
  };
  constexpr int NJ = C::COLSUM ? (C::NT - P + C::NP - 1) / C::NP : 0;
  f32x4 cs[NJ > 0 ? NJ : 1];
#pragma unroll
  for (int j = 0; j < NJ; ++j) cs[j] = f32x4{0.f, 0.f, 0.f, 0.f};
  const int g = lane >> 4;
  const int tb = (16 * (g >> 1) + 4 * (g & 1) + ((lane & 15) >> 2)) * (C::BN * 2) + (lane & 3) * 8;
  auto colsum = [&](int kt) {
    if constexpr (C::COLSUM) {
      const char* sb = smem + (kt % C::NST) * C::STAGE + C::ABYTES + tb;
#pragma unroll
      for (int jj = 0; jj < NJ; ++jj) {
        const int j = P + jj * C::NP;
#pragma unroll
        for (int ks = 0; ks < 2; ++ks) {
          const char* p = sb + ks * (32 * C::BN * 2) + j * 32;
          cs[jj] = MFMA16(cat8(trread(p), trread(p + 8 * C::BN * 2)), ones8(), cs[jj]);
        }
      }
      WAITL0();
    }
  };
  constexpr int PF = C::NST - 1;
#pragma unroll
  for (int s = 0; s < PF; ++s) issue(s);
  for (int kt = 0; kt < C::KT - PF; ++kt) { WAITV((PF - 1) * NQ); BAR(); issue(kt + PF); colsum(kt); }
  constexpr int TH = TAILN / 2, T0 = (TH * P) / C::NP, T1 = (TH * (P + 1)) / C::NP, NTL = T1 - T0;
  auto tail = [&](int half) {
    const unsigned sb = ldsb + ((C::KT + half) % C::NST) * C::STAGE;
#pragma unroll
    for (int j = 0; j < NTL; ++j) {
      const int c = 64 * (half * TH + T0 + j) + lane, row = c / (TCPR > 0 ? TCPR : 1), ch = c - row * TCPR;
      const unsigned vo2 = row * ldt + ch * 16;
      DMAQ(sb, (T0 + j) * 1024, vo2, srdT, 0u);
    }
  };
#pragma unroll
  for (int r = PF - 1; r >= 0; --r) {
    WAITV(r * NQ + (TAILN > 0 ? (r < PF - 1 ? NTL : 0) + (r < PF - 2 ? NTL : 0) : 0)); BAR();
    if (TAILN > 0 && r == PF - 1) tail(0);
    if (TAILN > 0 && r == PF - 2) tail(1);
    colsum(C::KT - 1 - r);
  }
  if (TAILN > 0) WAITV(0);
  if constexpr (C::COLSUM) {
    float* st = (float*)(smem + C::CS_OFF);
#pragma unroll
    for (int jj = 0; jj < NJ; ++jj) {
      if ((lane & 15) == 0) *(f32x4*)(st + 16 * (P + jj * C::NP) + 4 * g) = cs[jj];
    }
    WAITL0();
  }
  BAR();
}

template <class C, int TAILN = 0, int TCPR = 0>
DI void run_producer(char* smem, const void* Ab, unsigned abytes, const void* Bb, unsigned bbytes, int lda, int ldb, int p, int lane,
                     const void* Tb = nullptr, unsigned tbytes = 0, int ldt = 0) {
  __amdgpu_buffer_rsrc_t srdA = mksrd(Ab, abytes), srdB = mksrd(Bb, bbytes), srdT = mksrd(TAILN > 0 ? Tb : Ab, TAILN > 0 ? tbytes : abytes);
  const unsigned ldsb = (unsigned)(size_t)smem;
  if (p == 0) producer<C, 0, TAILN, TCPR>(smem, ldsb, srdA, srdB, lda, ldb, lane, srdT, ldt);
  if constexpr (C::NP > 1) { if (p == 1) producer<C, 1, TAILN, TCPR>(smem, ldsb, srdA, srdB, lda, ldb, lane, srdT, ldt); }
  if constexpr (C::NP > 2) { if (p == 2) producer<C, 2, TAILN, TCPR>(smem, ldsb, srdA, srdB, lda, ldb, lane, srdT, ldt); }
  if constexpr (C::NP > 3) { if (p == 3) producer<C, 3, TAILN, TCPR>(smem, ldsb, srdA, srdB, lda, ldb, lane, srdT, ldt); }
}

struct NoHook { DI void operator()() const {} };
template <class C, int MI, int NTW, int J0, bool ROLL, bool ROWSTAT, class Hook>
DI void consumer(char* smem, int row0, int lane, f32x4 (&acc)[MI][NTW], float (&rs)[MI], float (&rq)[MI], char* ws_dbg, int kid, int hook_kt, Hook hook) {
  const int g = lane >> 4, fr = lane & 15, f = fr >> 1;
  const int la0 = row0 * 128 + fr * 128 + ((g ^ f) << 4), la1 = row0 * 128 + fr * 128 + (((4 + g) ^ f) << 4);
  const int lb0 = C::ABYTES + J0 * 2048 + fr * 128 + ((g ^ f) << 4), lb1 = C::ABYTES + J0 * 2048 + fr * 128 + (((4 + g) ^ f) << 4);
  const int tb = C::ABYTES + J0 * 32 + (16 * (g >> 1) + 4 * (g & 1) + (fr >> 2)) * (C::BN * 2) + (lane & 3) * 8;
  f32x4 ars[MI], arq[MI];
#pragma unroll
  for (int i = 0; i < MI; ++i) {
    ars[i] = f32x4{0.f, 0.f, 0.f, 0.f}; arq[i] = f32x4{0.f, 0.f, 0.f, 0.f};
#pragma unroll
    for (int j = 0; j < NTW; ++j) acc[i][j] = f32x4{0.f, 0.f, 0.f, 0.f};
  }
  bf16x8 a0[MI], b0[NTW], a1[MI], b1[ROLL ? 1 : NTW];
  auto rda = [&](bf16x8* a, int kt, int ks) {
    const char* sb = smem + (kt % C::NST) * C::STAGE + (ks ? la1 : la0);
#pragma unroll
    for (int i = 0; i < MI; ++i) a[i] = *(const bf16x8*)(sb + i * 2048);
  };
  auto rdb1 = [&](int j, int kt, int ks) -> bf16x8 {
    const char* sb = smem + (kt % C::NST) * C::STAGE;
    if constexpr (C::BKN) {
      const char* p = sb + tb + ks * (32 * C::BN * 2) + j * 32;
      return cat8(trread(p), trread(p + 8 * C::BN * 2));
    } else {
      return *(const bf16x8*)(sb + (ks ? lb1 : lb0) + j * 2048);
    }
  };
  auto stats = [&](bf16x8* a) {
    if constexpr (ROWSTAT) {
#pragma unroll
      for (int i = 0; i < MI; ++i) { ars[i] = MFMA16(ones8(), a[i], ars[i]); arq[i] = MFMA16(a[i], a[i], arq[i]); }
    }
  };
  STAMP(kid, 0);
  BAR();
  STAMP(kid, 1);
  if constexpr (!ROLL) {
    constexpr int NM = MI * NTW + (ROWSTAT ? 2 * MI : 0), ND = MI + NTW * (C::BKN ? 2 : 1), RPG = (ND + (NM * 2 / 3) - 1) / (NM * 2 / 3);
#define SCHED(id) _Pragma("unroll") for (int q = 0; q < NM; ++q) { __builtin_amdgcn_sched_group_barrier(0x008, 1, id); \
    if (q * RPG < ND) __builtin_amdgcn_sched_group_barrier(0x080, RPG, id); }
    auto rd = [&](bf16x8* a, bf16x8* bb, int kt, int ks) {
      rda(a, kt, ks);
#pragma unroll
      for (int j = 0; j < NTW; ++j) bb[j] = rdb1(j, kt, ks);
    };
    auto mm = [&](bf16x8* a, bf16x8* bb) {
#pragma unroll
      for (int j = 0; j < NTW; ++j)
#pragma unroll
        for (int i = 0; i < MI; ++i) acc[i][j] = MFMA16(bb[j], a[i], acc[i][j]);
      stats(a);
    };
    auto body = [&](int kt) {
      SB();
      rd(a1, b1, kt, 1);
      mm(a0, b0);
      SCHED(0)
      SB();
      WAITL0(); BAR();
      SB();
      rd(a0, b0, kt + 1, 0);
      mm(a1, b1);
      SCHED(1)
    };
    rd(a0, b0, 0, 0);
    if (hook_kt > 0) {
      for (int kt = 0; kt < hook_kt; ++kt) body(kt);
      SB(); hook(); SB();
      for (int kt = hook_kt; kt < C::KT - 1; ++kt) body(kt);
    } else {
      for (int kt = 0; kt < C::KT - 1; ++kt) body(kt);
    }
    SB();
    rd(a1, b1, C::KT - 1, 1);
    mm(a0, b0);
    mm(a1, b1);
#undef SCHED
  } else {
    auto sub = [&](bf16x8* a, int kt, int ks, bool more) {
#pragma unroll
      for (int j = 0; j < NTW; ++j) {
#pragma unroll
        for (int i = 0; i < MI; ++i) acc[i][j] = MFMA16(b0[j], a[i], acc[i][j]);
        if (more) b0[j] = rdb1(j, kt, ks);
        SB();
      }
      stats(a);
    };
    rda(a0, 0, 0);
#pragma unroll
    for (int j = 0; j < NTW; ++j) b0[j] = rdb1(j, 0, 0);
    for (int kt = 0; kt < C::KT - 1; ++kt) {
      rda(a1, kt, 1);
      sub(a0, kt, 1, true);
      WAITL0(); BAR();
      rda(a0, kt + 1, 0);
      sub(a1, kt + 1, 0, true);
    }
    rda(a1, C::KT - 1, 1);
    sub(a0, C::KT - 1, 1, true);
    sub(a1, 0, 0, false);
  }
  if constexpr (ROWSTAT) {
#pragma unroll
    for (int i = 0; i < MI; ++i) {
      rs[i] = ars[i][0];
      const float d = (fr & 2) ? ((fr & 1) ? arq[i][3] : arq[i][2]) : ((fr & 1) ? arq[i][1] : arq[i][0]);
      rq[i] = __shfl(d, fr + 16 * (fr >> 2));
    }
  }
  SB();
  WAITL0(); BAR();
  SB();
  STAMP(kid, 2);
}

using C1 = Cfg<128, 9, 4, 4, B_KN, 9, true>;
DI void retile_weights(const u16* __restrict__ win, const u16* __restrict__ wout, const u16* __restrict__ wo, char* __restrict__ ws, int gw, int lane) {
  constexpr int U1 = 32 * 9 * 18, U2 = U1 + 8 * 36 * 10, U3 = U2 + 8 * 9 * 10;
  auto src_of = [&](int u) -> const u16* {
    if (u < U1) {
      const int tile = u / 18, q = u - tile * 18, tn = tile / 9, kt = tile - tn * 9;
      const int c = 64 * q + lane, rho = c / 18, ch = c - rho * 18;
      const int k = (rho & ~12) | ((rho & 4) << 1) | ((rho & 8) >> 1);
      return win + (size_t)(kt * 64 + k) * F2 + tn * 144 + ch * 8;
    }
    const bool o = u < U2; const int v = u - (o ? U1 : U2), KT = o ? 36 : 9;
    const int tile = v / 10, q = v - tile * 10, tn = tile / KT, kt = tile - tn * KT;
    const int c = 64 * q + lane, rho = c / 10, ch = c - rho * 10;
    const int k = (rho & ~12) | ((rho & 4) << 1) | ((rho & 8) >> 1);
    int col = tn * 72 + ch * 8; if (col > D_ - 8) col = D_ - 8;
    return (o ? wout : wo) + (size_t)(kt * 64 + k) * D_ + col;
  };
  char* dst0 = ws + WS_WIN + lane * 16;
  for (int u0 = gw; u0 < U3; u0 += 512 * 6) {
    u32x4 v[6];
#pragma unroll
    for (int i = 0; i < 6; ++i) { const int u = u0 + 512 * i; if (u < U3) v[i] = *(const u32x4*)src_of(u); }
#pragma unroll
    for (int i = 0; i < 6; ++i) { const int u = u0 + 512 * i; if (u < U3) *(u32x4*)(dst0 + (size_t)u * 1024) = v[i]; }
  }
}
__global__ __launch_bounds__(512, 2) void k_qkv(const u16* __restrict__ x, const u16* __restrict__ wqkv, const u16* __restrict__ win, const u16* __restrict__ wout,
                                                const u16* __restrict__ wo, char* __restrict__ ws) {
  extern __shared__ __attribute__((aligned(16))) char smem[];
  const int tid = threadIdx.x, w = rfl(tid >> 6);
  const int b = blockIdx.x, xcd = b & 7, idx = b >> 3;
  char* ws_dbg = ws;
  if (b == 255 && tid < 8) *(unsigned*)(ws + WS_CNT + tid * 128) = 0u;
  if (idx >= 24) { STAMP(0, 0); STAMP(0, 1); STAMP(0, 2); retile_weights(win, wout, wo, ws, ((idx - 24) * 8 + xcd) * 8 + w, tid & 63); STAMP_END(0); return; }
  const int tm = (xcd & 3) * 4 + (idx & 3), tn = (xcd >> 2) * 6 + (idx >> 2);
  if (w >= 4) {
    run_producer<C1>(smem, x + (size_t)tm * 128 * D_, 128 * D_ * 2, wqkv + tn * 144, (unsigned)((D_ * 3 * D_ - tn * 144) * 2), D_, 3 * D_, w - 4, tid & 63);
    return;
  }
  f32x4 acc[2][9]; float rs[2], rq[2];
  consumer<C1, 2, 9, 0, false, true>(smem, w * 32, tid & 63, acc, rs, rq, ws, 0, 0, NoHook());
  int tz = threadIdx.x; VREG(tz);
  const int lane = tz & 63, g = lane >> 4, fr = lane & 15;
  const float* cs = (const float*)(smem + C1::CS_OFF);
  const int kind = tn >> 2, head0 = (tn & 3) * 8;
  const int tile32 = tm * 4 + w;
  const float sc = kind == 0 ? 0.23570226039551584f * 1.4426950408889634f : 1.f;
  float mu[2], rstd[2];
#pragma unroll
  for (int i = 0; i < 2; ++i) {
    mu[i] = rs[i] * (1.f / D_);
    rstd[i] = rsqrtf(fmaxf(rq[i] * (1.f / D_) - mu[i] * mu[i], 0.f) + 1e-5f) * sc;
  }
  constexpr int RP = 336;
  char* reg = smem + w * (32 * RP);
  f32x4 c4a[9];
#pragma unroll
  for (int j = 0; j < 9; ++j) c4a[j] = *(const f32x4*)(cs + 16 * j + 4 * g);
#pragma unroll
  for (int j = 0; j < 9; ++j) {
    const f32x4 c4 = c4a[j];
#pragma unroll
    for (int rr = 0; rr < 2; ++rr) {
      const int c = 16 * j + 4 * g + 2 * rr, off = c * 2 + (c / 18) * 4;
#pragma unroll
      for (int i = 0; i < 2; ++i)
        *(unsigned*)(reg + (16 * i + fr) * RP + off) = cvtpk((acc[i][j][2 * rr] - mu[i] * c4[2 * rr]) * rstd[i], (acc[i][j][2 * rr + 1] - mu[i] * c4[2 * rr + 1]) * rstd[i]);
    }
  }
  WAITL0();
  if (kind < 2) {
    char* base = ws + (kind == 0 ? WS_Q + (size_t)tile32 * QK_TILE + (size_t)head0 * Q_HEAD : WS_KV + (size_t)tile32 * KV_TILE + (size_t)head0 * KV_HEAD);
    const int hstride = kind == 0 ? Q_HEAD : KV_HEAD;
    const char* rp = reg + (lane & 31) * RP + (lane >> 5) * 16;
#pragma unroll
    for (int hl = 0; hl < 8; ++hl) {
      const u32x2 lo = *(const u32x2*)(rp + hl * 40), hi = *(const u32x2*)(rp + hl * 40 + 8);
      *(u32x4*)(base + (size_t)hl * hstride + lane * 16) = u32x4{lo[0], lo[1], hi[0], hi[1]};
    }
#pragma unroll
    for (int q = 0; q < 4; ++q) {
      const int hl = 2 * q + (lane >> 5);
      *(unsigned*)(base + (size_t)hl * hstride + 1024 + (lane & 31) * 4) = *(const unsigned*)(reg + (lane & 31) * RP + hl * 40 + 32);
    }
  } else {
    char* base = ws + WS_KV + (size_t)tile32 * KV_TILE + V_OFF + (size_t)head0 * KV_HEAD;
    const int s = g >> 1, h = g & 1;
    const char* rp = reg + (16 * s + 4 * h + (fr >> 2)) * RP + (lane & 3) * 8;
#pragma unroll
    for (int j = 0; j < 10; ++j) {
      const bf16x8 v = cat8(trread(rp + j * 32), trread(rp + 8 * RP + j * 32));
      const int c = 16 * j + fr, hl = c / 20, d = c - hl * 20;
      if (d < 18) *(bf16x8*)(base + (size_t)hl * KV_HEAD + s * 608 + h * 304 + d * 16) = v;
    }
    if (lane < 32) {
      *(bf16x8*)(base + (size_t)(lane >> 2) * KV_HEAD + (lane & 3) * 304 + 18 * 16) = ones8();
    }
  }
  STAMP_END(0);
}

constexpr int ATT_LDS = 152 * 1024;
__global__ __launch_bounds__(512, 2) void k_attn(char* __restrict__ ws, const u16* __restrict__ wsp) {
  extern __shared__ __attribute__((aligned(16))) char smem[];
  const int tid = threadIdx.x, lane = tid & 63, w = rfl(tid >> 6);
  const int b = blockIdx.x, xcd = b & 7, idx = b >> 3;
  const int head = xcd * 4 + (idx & 3), qb = idx >> 2;
  char* ws_dbg = ws; STAMP(1, 0);
  const int q31 = lane & 31, h = lane >> 5;
  const char* qp = ws + WS_Q + (size_t)head * Q_HEAD + (size_t)(qb * 8 + w) * QK_TILE;
  bf16x8 qf1; unsigned q2;
  asm volatile("global_load_dwordx4 %0, %1, off" : "=v"(qf1) : "v"(qp + lane * 16));
  asm volatile("global_load_dword %0, %1, off" : "=v"(q2) : "v"(qp + 1024 + q31 * 4));
  {
    __amdgpu_buffer_rsrc_t srd = mksrd(ws + WS_KV + (size_t)head * KV_HEAD, KV_HEAD);
    const unsigned ldsb = (unsigned)(size_t)smem + w * 1024, vo = lane * 16 + w * 1024;
#pragma unroll
    for (int j = 0; j < 19; ++j) DMAQ(ldsb, j * 8192, vo, srd, (unsigned)(j * 8192));
  }
  asm volatile("s_waitcnt vmcnt(16)" : "+v"(qf1), "+v"(q2) :: "memory");
  BAR();
  u32x4 q2v = {h == 0 ? q2 : 0u, 0u, 0u, 0u};
  const bf16x8 qf2 = __builtin_bit_cast(bf16x8, q2v);
  const int dcl = q31 < 19 ? q31 : (q31 >= 20 && q31 < 28 ? 12 : 16);
  const int koff = lane * 16, k2off = 1024 + q31 * 4, voff = V_OFF + h * 304 + dcl * 16;
  f32x16 o;
#pragma unroll
  for (int r = 0; r < 16; ++r) o[r] = 0.f;
  auto scores = [&](int t, const f32x16& c) {
    const char* tp = smem + t * KV_TILE;
    const bf16x8 k1 = *(const bf16x8*)(tp + koff);
    u32x4 k2v = {*(const unsigned*)(tp + k2off), 0u, 0u, 0u};
    f32x16 s = MFMA32(k1, qf1, c);
    return MFMA32(__builtin_bit_cast(bf16x8, k2v), qf2, s);
  };
  f32x16 zero;
#pragma unroll
  for (int r = 0; r < 16; ++r) zero[r] = 0.f;
  STAMP(1, 1);
  f32x16 negm;
  {
    const f32x16 s = scores(0, zero);
    float m = s[0];
#pragma unroll
    for (int r = 1; r < 16; ++r) m = fmaxf(m, s[r]);
    m = fmaxf(m, xor32(m));
#pragma unroll
    for (int r = 0; r < 16; ++r) negm[r] = -m;
  }
  {
    bf16x8 k1, v0, v1; u32x4 k2 = {0u, 0u, 0u, 0u};
    f32x16 sA = scores(0, negm), sB;
    k1 = *(const bf16x8*)(smem + KV_TILE + koff); k2[0] = *(const unsigned*)(smem + KV_TILE + k2off);
    v0 = *(const bf16x8*)(smem + voff); v1 = *(const bf16x8*)(smem + voff + 608);
    auto step = [&](int t, f32x16& sc, f32x16& sn) {
      const char* tp = smem + t * KV_TILE;
      u32x4 p0, p1;
      sn = MFMA32(k1, qf1, negm);
      k1 = *(const bf16x8*)(tp + 2 * KV_TILE + koff);
#pragma unroll
      for (int e = 0; e < 4; ++e) sc[e] = __builtin_amdgcn_exp2f(sc[e]);
      SB();
      sn = MFMA32(__builtin_bit_cast(bf16x8, k2), qf2, sn);
      k2[0] = *(const unsigned*)(tp + 2 * KV_TILE + k2off);
#pragma unroll
      for (int e = 4; e < 8; ++e) sc[e] = __builtin_amdgcn_exp2f(sc[e]);
#pragma unroll
      for (int e = 0; e < 4; ++e) p0[e] = cvtpk(sc[2 * e], sc[2 * e + 1]);
      SB();
      o = MFMA32(v0, __builtin_bit_cast(bf16x8, p0), o);
      v0 = *(const bf16x8*)(tp + KV_TILE + voff);
#pragma unroll
      for (int e = 8; e < 16; ++e) sc[e] = __builtin_amdgcn_exp2f(sc[e]);
#pragma unroll
      for (int e = 0; e < 4; ++e) p1[e] = cvtpk(sc[8 + 2 * e], sc[8 + 2 * e + 1]);
      SB();
      o = MFMA32(v1, __builtin_bit_cast(bf16x8, p1), o);
      v1 = *(const bf16x8*)(tp + KV_TILE + voff + 608);
      SB();
    };
    unsigned dummy = 0;
    auto warm = [&]() { const unsigned T = b * 512 + tid; if (T < 65536u) TOUCH(dummy, (const char*)wsp + (size_t)T * 128); else TOUCH(dummy, (const char*)wsp); };
#define CHUNK(c, nv) { if (c > 0) { WAITV(nv); BAR(); } if (c == 3) warm(); \
    for (int t = 8 * c; t < 8 * c + 8; t += 2) { step(t, sA, sB); step(t + 1, sB, sA); } }
    CHUNK(0, 16) CHUNK(1, 13) CHUNK(2, 11) CHUNK(3, 9) CHUNK(4, 7) CHUNK(5, 5) CHUNK(6, 3) CHUNK(7, 1)
#undef CHUNK
    asm volatile("s_waitcnt vmcnt(0)" : "+v"(dummy) :: "memory");
  }
  STAMP(1, 2);
  float l = __shfl(o[10], q31);
  bool ok = l < 1.8446744e19f;
#pragma unroll
  for (int r = 0; r < 12; ++r) ok = ok && (fabsf(o[r]) <= 3.0e38f);
  if (!__all(ok)) {
    float m = -3.0e38f;
#pragma unroll
    for (int r = 0; r < 16; ++r) o[r] = 0.f;
    for (int t = 0; t < 64; ++t) {
      f32x16 s = scores(t, zero);
      float tmax = s[0];
#pragma unroll
      for (int r = 1; r < 16; ++r) tmax = fmaxf(tmax, s[r]);
      tmax = fmaxf(tmax, xor32(tmax));
      const float mn = fmaxf(m, tmax), alpha = __builtin_amdgcn_exp2f(m - mn);
      m = mn;
#pragma unroll
      for (int r = 0; r < 16; ++r) { o[r] *= alpha; s[r] = __builtin_amdgcn_exp2f(s[r] - mn); }
      const char* tp = smem + t * KV_TILE + voff;
      const bf16x8 v0 = *(const bf16x8*)(tp), v1 = *(const bf16x8*)(tp + 608);
      u32x4 p0, p1;
#pragma unroll
      for (int e = 0; e < 4; ++e) { p0[e] = cvtpk(s[2 * e], s[2 * e + 1]); p1[e] = cvtpk(s[8 + 2 * e], s[8 + 2 * e + 1]); }
      o = MFMA32(v0, __builtin_bit_cast(bf16x8, p0), o);
      o = MFMA32(v1, __builtin_bit_cast(bf16x8, p1), o);
    }
    l = __shfl(o[10], q31);
  }
  const float inv = 1.f / l;
  u16* op = (u16*)(ws + WS_AO) + (size_t)((qb * 8 + w) * 32 + q31) * D_ + head * DH + 4 * h;
  *(unsigned*)(op + 0) = cvtpk(o[0] * inv, o[1] * inv);
  *(unsigned*)(op + 2) = cvtpk(o[2] * inv, o[3] * inv);
  *(unsigned*)(op + 8) = cvtpk(o[4] * inv, o[5] * inv);
  *(unsigned*)(op + 10) = cvtpk(o[6] * inv, o[7] * inv);
  if (h == 0) *(unsigned*)(op + 16) = cvtpk(o[8] * inv, o[9] * inv);
  STAMP_END(1);
}

template <int KT> using CR = Cfg<64, 5, 4, 8, B_TILED, KT, false>;
template <int KT>
__global__ __launch_bounds__(512, 2) void k_res(const u16* __restrict__ A, const char* __restrict__ Wt, const u16* __restrict__ res, u16* __restrict__ out, char* __restrict__ ws) {
  using C = CR<KT>;
  extern __shared__ __attribute__((aligned(16))) char smem[];
  const int tid = threadIdx.x, lane = tid & 63, w = rfl(tid >> 6);
  const int b = blockIdx.x, xcd = b & 7, idx = b >> 3;
  const int tm = (xcd & 3) * 8 + (idx & 7), tn = (xcd >> 2) * 4 + (idx >> 3);
  constexpr int K = KT * 64;
  char* ws_dbg = ws;
  if (w >= 4) {
    run_producer<C>(smem, A + (size_t)tm * 64 * K, 64 * K * 2, Wt + (size_t)tn * KT * WR_TB, KT * WR_TB, K, 0, w - 4, lane);
    return;
  }
  const int g = lane >> 4, fr = lane & 15;
  const int tok = tm * 64 + w * 16 + fr;
  const size_t ro = (size_t)tok * D_ + tn * 72 + 4 * g;
  u32x2 r[5];
#pragma unroll
  for (int j = 0; j < 5; ++j) r[j] = *(const u32x2*)(res + ro + ((j < 4 || g < 2) ? 16 * j : 0));
  f32x4 acc[1][5]; float rs[1], rq[1];
  consumer<C, 1, 5, 0, false, false>(smem, w * 16, lane, acc, rs, rq, ws, KT == 9 ? 2 : 6, 0, NoHook());
  float s1 = 0.f, s2 = 0.f;
#pragma unroll
  for (int j = 0; j < 5; ++j) {
    if (j < 4 || g < 2) {
      u32x2 v;
      v[0] = cvtpk(bflo(r[j][0]) + acc[0][j][0], bfhi(r[j][0]) + acc[0][j][1]);
      v[1] = cvtpk(bflo(r[j][1]) + acc[0][j][2], bfhi(r[j][1]) + acc[0][j][3]);
      *(u32x2*)(out + ro + 16 * j) = v;
      if constexpr (KT == 9) {
        const float a0 = bflo(v[0]), a1 = bfhi(v[0]), a2 = bflo(v[1]), a3 = bfhi(v[1]);
        s1 += (a0 + a1) + (a2 + a3);
        s2 = fmaf(a0, a0, fmaf(a1, a1, fmaf(a2, a2, fmaf(a3, a3, s2))));
      }
    }
  }
  if constexpr (KT == 9) {
    s1 += xor16(s1); s1 += xor32(s1); s2 += xor16(s2); s2 += xor32(s2);
    if (g == 0) *(f32x2*)(ws + WS_HST + ((size_t)tok * 8 + tn) * 8) = f32x2{s1, s2};
  }
  STAMP_END(KT == 9 ? 2 : 6);
}

using C4 = Cfg<256, 9, 3, 3, B_TILED, 9, true>;
template <int NTW, int J0>
DI void win_wave(char* smem, char* ws, int wm, int tm, int tn, int lane0) {
  char* ws_dbg = ws;
  f32x4 acc[4][NTW]; float rs[4], rq[4];
  consumer<C4, 4, NTW, J0, true, false>(smem, wm * 64, lane0, acc, rs, rq, ws, 3, 0, NoHook());
  int tz = threadIdx.x; VREG(tz);
  const int lane = tz & 63, g = lane >> 4, fr = lane & 15;
  const float* cs = (const float*)(smem + C4::CS_OFF) + 16 * J0;
  const f32x2* rst = (const f32x2*)(smem + C4::RS_OFF) + wm * 64;
  constexpr int RP = NTW * 32 + 16;
  constexpr int WN = J0 ? 1 : 0;
  char* reg = smem + (wm * 2 + WN) * (64 * 176);
  const int tok0 = tm * 256 + wm * 64;
  const float c1 = -2.f * 0.7978845608028654f * 1.4426950408889634f, c2 = c1 * 0.044715f;
  f32x4 c4a[NTW]; f32x2 sta[4];
#pragma unroll
  for (int j = 0; j < NTW; ++j) c4a[j] = *(const f32x4*)(cs + 16 * j + 4 * g);
#pragma unroll
  for (int i = 0; i < 4; ++i) sta[i] = rst[16 * i + fr];
  u32x2 zp[4][NTW];
  f32x2 sum[4];
#pragma unroll
  for (int i = 0; i < 4; ++i) {
    const f32x2 st = sta[i];
    const float k1s = c1 * st[1], k3s = c2 * st[1] * st[1] * st[1], irss = __builtin_amdgcn_rcpf(st[1]);
    const f32x2 nmu = {-st[0], -st[0]}, k1 = {k1s, k1s}, k3 = {k3s, k3s}, irs = {irss, irss};
    f32x2 s1 = {0.f, 0.f}, s2 = {0.f, 0.f};
#pragma unroll
    for (int j = 0; j < NTW; ++j) {
#pragma unroll
      for (int hh = 0; hh < 2; ++hh) {
        const f32x2 a2 = {acc[i][j][2 * hh], acc[i][j][2 * hh + 1]}, c2v = {c4a[j][2 * hh], c4a[j][2 * hh + 1]};
        const f32x2 t = nmu * c2v + a2;
        const f32x2 y = t * (t * t * k3 + k1);
        const f32x2 e = {__builtin_amdgcn_exp2f(y[0]), __builtin_amdgcn_exp2f(y[1])};
        const f32x2 d = e * irs + irs;
        const f32x2 z = t * f32x2{__builtin_amdgcn_rcpf(d[0]), __builtin_amdgcn_rcpf(d[1])};
        s1 += z; s2 += z * z;
        zp[i][j][hh] = cvtpk(z[0], z[1]);
      }
    }
    float a1 = s1[0] + s1[1], a2 = s2[0] + s2[1];
    if (tn >= 16) { a1 += xor16(a1); a1 += xor32(a1); a2 += xor16(a2); a2 += xor32(a2); }
    sum[i] = f32x2{a1, a2};
  }
  if (tn < 16) {
#pragma unroll
    for (int i = 0; i < 4; ++i)
#pragma unroll
      for (int j = 0; j < NTW; ++j) *(u32x2*)(reg + (16 * i + fr) * RP + (16 * j + 4 * g) * 2) = zp[i][j];
    WAITL0();
    const __amdgpu_buffer_rsrc_t so = mksrd((u16*)(ws + WS_Z1) + (size_t)tok0 * FH + tn * 144 + 16 * J0, 64 * FH * 2);
    constexpr int CPR = NTW * 2;
#pragma unroll
    for (int i = 0; i < CPR; ++i) {
      const int c = i * 64 + lane, row = c / CPR, ch = c - row * CPR;
      st16(so, row * (FH * 2) + ch * 16, *(const u32x4*)(reg + row * RP + ch * 16));
    }
  } else {
    f32x2* part = (f32x2*)(smem + 96 * 1024);
    if (g == 0) {
#pragma unroll
      for (int i = 0; i < 4; ++i) part[WN * 256 + wm * 64 + 16 * i + fr] = sum[i];
    }
    WAITL0(); BAR();
    const __amdgpu_buffer_rsrc_t srdS = mksrd(ws + WS_ST + (size_t)tm * (16 * 256 * 8), 16 * 256 * 8);
    if (wm == 0 && WN == 0) {
#pragma unroll
      for (int i = 0; i < 4; ++i) {
        const f32x2 v = part[64 * i + lane] + part[256 + 64 * i + lane];
        __builtin_amdgcn_raw_buffer_store_b64(__builtin_bit_cast(u32x2, v), srdS, ((tn - 16) * 256 + 64 * i + lane) * 8, 0, 16);
      }
      asm volatile("s_waitcnt vmcnt(0)" ::: "memory");
      unsigned* cnt = (unsigned*)(ws + WS_CNT + tm * 128);
      if (lane == 0) {
        __hip_atomic_fetch_add(cnt, 1u, __ATOMIC_RELAXED, __HIP_MEMORY_SCOPE_AGENT);
        for (unsigned spins = 0; spins < (1u << 24); ++spins) {
          if (__hip_atomic_load(cnt, __ATOMIC_RELAXED, __HIP_MEMORY_SCOPE_AGENT) >= 16u) break;
          __builtin_amdgcn_s_sleep(1);
        }
      }
      asm volatile("" ::: "memory");
    }
    BAR();
    f32x4 t4 = {0.f, 0.f, 0.f, 0.f};
#pragma unroll
    for (int q = 0; q < 8; ++q)
      t4 += __builtin_bit_cast(f32x4, __builtin_amdgcn_raw_buffer_load_b128(srdS, (((lane >> 5) + 2 * q) * 256 + wm * 64 + 2 * (lane & 31)) * 8, 0, 16));
#pragma unroll
    for (int e = 0; e < 4; ++e) t4[e] += xor32(t4[e]);
    f32x2* st2 = (f32x2*)(smem + 104 * 1024) + (wm * 2 + WN) * 64;
    if (lane < 32) {
      const float m0 = t4[0] * (1.f / FH), m1 = t4[2] * (1.f / FH);
      const float r0 = rsqrtf(fmaxf(t4[1] * (1.f / FH) - m0 * m0, 0.f) + 1e-5f), r1 = rsqrtf(fmaxf(t4[3] * (1.f / FH) - m1 * m1, 0.f) + 1e-5f);
      *(f32x4*)(st2 + 2 * lane) = f32x4{r0, -m0 * r0, r1, -m1 * r1};
    }
    WAITL0();
#pragma unroll
    for (int i = 0; i < 4; ++i) {
      const f32x2 st = st2[16 * i + fr];
#pragma unroll
      for (int j = 0; j < NTW; ++j) {
        u32x2 v;
        v[0] = cvtpk(fmaf(bflo(zp[i][j][0]), st[0], st[1]), fmaf(bfhi(zp[i][j][0]), st[0], st[1]));
        v[1] = cvtpk(fmaf(bflo(zp[i][j][1]), st[0], st[1]), fmaf(bfhi(zp[i][j][1]), st[0], st[1]));
        *(u32x2*)(reg + (16 * i + fr) * RP + (16 * j + 4 * g) * 2) = v;
      }
    }
    WAITL0();
    const __amdgpu_buffer_rsrc_t so = mksrd((u16*)(ws + WS_Z2N) + (size_t)((tn - 16) * 144 + 16 * J0) * S_ + tok0, NTW * 16 * S_ * 2);
    const char* rp = reg + (8 * g + (fr >> 2)) * RP + (lane & 3) * 8;
#pragma unroll
    for (int hh = 0; hh < 2; ++hh)
#pragma unroll
      for (int j = 0; j < NTW; ++j)
        st16(so, (j * 16 + fr) * (S_ * 2) + (8 * g + 32 * hh) * 2, __builtin_bit_cast(u32x4, cat8(trread(rp + hh * 32 * RP + j * 32), trread(rp + (hh * 32 + 4) * RP + j * 32))));
  }
  STAMP_END(3);
}
__global__ __launch_bounds__(768, 3) void k_win(char* __restrict__ ws) {
  extern __shared__ __attribute__((aligned(16))) char smem[];
  const int tid = threadIdx.x, w = rfl(tid >> 6);
  const int b = blockIdx.x, xcd = b & 7, idx = b >> 3;
  const int tm = (xcd & 1) * 4 + (idx & 3), tn = (xcd >> 1) * 8 + (idx >> 2);
  const u16* hsrc = (const u16*)(ws + WS_H);
  if (w == 11) {
    const int lane = tid & 63;
    f32x2* rst = (f32x2*)(smem + C4::RS_OFF);
#pragma unroll
    for (int i = 0; i < 4; ++i) {
      const f32x4* p = (const f32x4*)(ws + WS_HST + (size_t)(tm * 256 + 64 * i + lane) * 64);
      const f32x4 a = p[0], bb = p[1], c = p[2], d = p[3];
      const float s1 = (a[0] + a[2]) + (bb[0] + bb[2]) + (c[0] + c[2]) + (d[0] + d[2]), s2 = (a[1] + a[3]) + (bb[1] + bb[3]) + (c[1] + c[3]) + (d[1] + d[3]);
      const float mu = s1 * (1.f / D_);
      rst[64 * i + lane] = f32x2{mu, rsqrtf(fmaxf(s2 * (1.f / D_) - mu * mu, 0.f) + 1e-5f)};
    }
    WAITL0(); BAR();
    return;
  }
  if (w >= 8) {
    run_producer<C4>(smem, hsrc + (size_t)tm * 256 * D_, 256 * D_ * 2, ws + WS_WIN + (size_t)tn * 9 * WIN_TB, 9 * WIN_TB, D_, 0, w - 8, tid & 63);
    return;
  }
  if (w < 4) win_wave<5, 0>(smem, ws, w, tm, tn, tid & 63); else win_wave<4, 5>(smem, ws, w - 4, tm, tn, tid & 63);
}

using C6 = Cfg<128, 9, 4, 4, B_NK, 32, false>;
__global__ __launch_bounds__(512, 2) void k_sp(const u16* __restrict__ wsp, const u16* __restrict__ bsp, char* __restrict__ ws) {
  extern __shared__ __attribute__((aligned(16))) char smem[];
  const int tid = threadIdx.x, lane = tid & 63, w = rfl(tid >> 6);
  const int b = blockIdx.x, xcd = b & 7, idx = b >> 3;
  const int tm = (xcd & 1) * 8 + (idx & 7), tn = (xcd >> 1) * 4 + (idx >> 3);
  char* ws_dbg = ws;
  if (w >= 4) {
    run_producer<C6, 36, 18>(smem, wsp + (size_t)tm * 128 * S_, 128 * S_ * 2, (const u16*)(ws + WS_Z2N) + (size_t)tn * 144 * S_, 144 * S_ * 2, S_, S_, w - 4, lane,
                             (const u16*)(ws + WS_Z1) + (size_t)tm * 128 * FH + tn * 144, (unsigned)((128 * FH - tn * 144) * 2), FH * 2);
    return;
  }
  const int g = lane >> 4, fr = lane & 15;
  const int tok0 = tm * 128 + w * 32;
  f32x4 acc[2][9]; float rs[2], rq[2];
  const unsigned bias0 = bsp[tok0 + fr], bias1 = bsp[tok0 + 16 + fr];
  consumer<C6, 2, 9, 0, false, false>(smem, w * 32, lane, acc, rs, rq, ws, 5, 0, NoHook());
  char* reg = smem + ((C6::KT + (w >> 1)) % C6::NST) * C6::STAGE + (w & 1) * (32 * 288);
#pragma unroll
  for (int i = 0; i < 2; ++i) {
    const float bi = bflo(i ? bias1 : bias0);
#pragma unroll
    for (int j = 0; j < 9; ++j) {
      u32x2* p = (u32x2*)(reg + (16 * i + fr) * 288 + (16 * j + 4 * g) * 2);
      const u32x2 z = *p;
      u32x2 v;
      v[0] = cvtpk(bflo(z[0]) * (acc[i][j][0] + bi), bfhi(z[0]) * (acc[i][j][1] + bi));
      v[1] = cvtpk(bflo(z[1]) * (acc[i][j][2] + bi), bfhi(z[1]) * (acc[i][j][3] + bi));
      *p = v;
    }
  }
  WAITL0();
  const __amdgpu_buffer_rsrc_t so = mksrd((u16*)(ws + WS_U) + (size_t)tok0 * FH + tn * 144, 32 * FH * 2);
#pragma unroll
  for (int i = 0; i < 9; ++i) {
    const int c = i * 64 + lane, row = c / 18, ch = c - row * 18;
    st16(so, row * (FH * 2) + ch * 16, *(const u32x4*)(reg + row * 288 + ch * 16));
  }
  STAMP_END(5);
}

extern "C" void kernel_launch(void* const* d_in, const int* in_sizes, int n_in, void* d_out, int out_size, void* d_ws, size_t ws_size, hipStream_t stream) {
  const u16* x = (const u16*)d_in[0];
  const u16* wqkv = (const u16*)d_in[1];
  const u16* wo = (const u16*)d_in[2];
  const u16* win = (const u16*)d_in[4];
  const u16* wsp = (const u16*)d_in[5];
  const u16* bsp = (const u16*)d_in[6];
  const u16* wout = (const u16*)d_in[7];
  char* ws = (char*)d_ws;
  (void)hipFuncSetAttribute((const void*)k_qkv, hipFuncAttributeMaxDynamicSharedMemorySize, C1::LDS);
  (void)hipFuncSetAttribute((const void*)k_attn, hipFuncAttributeMaxDynamicSharedMemorySize, ATT_LDS);
  (void)hipFuncSetAttribute((const void*)k_res<9>, hipFuncAttributeMaxDynamicSharedMemorySize, CR<9>::LDS);
  (void)hipFuncSetAttribute((const void*)k_res<36>, hipFuncAttributeMaxDynamicSharedMemorySize, CR<36>::LDS);
  (void)hipFuncSetAttribute((const void*)k_win, hipFuncAttributeMaxDynamicSharedMemorySize, C4::LDS);
  (void)hipFuncSetAttribute((const void*)k_sp, hipFuncAttributeMaxDynamicSharedMemorySize, C6::LDS);
  hipLaunchKernelGGL(k_qkv, dim3(256), dim3(512), C1::LDS, stream, x, wqkv, win, wout, wo, ws);
  hipLaunchKernelGGL(k_attn, dim3(256), dim3(512), ATT_LDS, stream, ws, wsp);
  hipLaunchKernelGGL(k_res<9>, dim3(256), dim3(512), CR<9>::LDS, stream, (const u16*)(ws + WS_AO), (const char*)(ws + WS_WO), x, (u16*)(ws + WS_H), ws);
  hipLaunchKernelGGL(k_win, dim3(256), dim3(768), C4::LDS, stream, ws);
  hipLaunchKernelGGL(k_sp, dim3(256), dim3(512), C6::LDS, stream, wsp, bsp, ws);
  hipLaunchKernelGGL(k_res<36>, dim3(256), dim3(512), CR<36>::LDS, stream, (const u16*)(ws + WS_U), (const char*)(ws + WS_WOUT), (const u16*)(ws + WS_H), (u16*)d_out, ws);
}
```

```cpp
#include <hip/hip_runtime.h>
#include <stdint.h>

typedef unsigned short u16;
typedef short bf16x8 __attribute__((ext_vector_type(8)));
typedef short s16x4 __attribute__((ext_vector_type(4)));
typedef float f32x2 __attribute__((ext_vector_type(2)));
typedef float f32x4 __attribute__((ext_vector_type(4)));
typedef float f32x16 __attribute__((ext_vector_type(16)));
typedef unsigned u32x2 __attribute__((ext_vector_type(2)));
typedef unsigned u32x4 __attribute__((ext_vector_type(4)));
typedef __bf16 bf16x2_t __attribute__((ext_vector_type(2)));
typedef __attribute__((address_space(3))) s16x4* lds_s16x4_ptr;
#define DI __device__ __forceinline__

constexpr int S_ = 2048, D_ = 576, H_ = 32, DH = 18, FH = 2304, F2 = 4608;
constexpr int QK_TILE = 1152;
constexpr int V_OFF = 1152;
constexpr int KV_TILE = 1152 + 4 * 304;
constexpr int KV_HEAD = 64 * KV_TILE;
constexpr int Q_HEAD = 64 * QK_TILE;
constexpr int WIN_TB = 64 * 288, WR_TB = 64 * 160;
constexpr size_t WS_Q = 0;
constexpr size_t WS_KV = WS_Q + (size_t)H_ * Q_HEAD;
constexpr size_t WS_AO = WS_KV + (size_t)H_ * KV_HEAD + 8192;
constexpr size_t WS_H = WS_AO + (size_t)S_ * D_ * 2;
constexpr size_t WS_Z1 = WS_H + (size_t)S_ * D_ * 2;
constexpr size_t WS_Z2T = WS_Z1 + (size_t)S_ * FH * 2;
constexpr size_t WS_Z2N = WS_Z2T + (size_t)S_ * FH * 2;
constexpr size_t WS_ST = WS_Z2N + (size_t)S_ * FH * 2;
constexpr size_t WS_CNT = WS_ST + (size_t)S_ * 16 * 2 * 4;
constexpr size_t WS_HST = WS_ST + (size_t)S_ * 32 * 2 * 4;
constexpr size_t WS_U = WS_HST + (size_t)S_ * 8 * 2 * 4;
constexpr size_t WS_WIN = WS_U + (size_t)S_ * FH * 2;
constexpr size_t WS_WOUT = WS_WIN + (size_t)32 * 9 * WIN_TB;
constexpr size_t WS_WO = WS_WOUT + (size_t)8 * 36 * WR_TB;
constexpr size_t WS_CS = WS_WO + (size_t)8 * 9 * WR_TB;
constexpr size_t WS_END = WS_CS + (size_t)F2 * 4;

DI unsigned cvtpk(float lo, float hi) { f32x2 v = {lo, hi}; bf16x2_t b = __builtin_convertvector(v, bf16x2_t); return __builtin_bit_cast(unsigned, b); }
DI float bflo(unsigned u) { return __uint_as_float(u << 16); }
DI float bfhi(unsigned u) { return __uint_as_float(u & 0xffff0000u); }
DI unsigned rfl(unsigned x) { return __builtin_amdgcn_readfirstlane(x); }
DI float xor16(float v) { return __shfl_xor(v, 16); }
DI float xor32(float v) { return __shfl_xor(v, 32); }
#define DMAQ(sb, imm, voff, srd, soff) asm volatile("s_add_u32 m0, %0, %1\n\ts_nop 0\n\tbuffer_load_dwordx4 %2, %3, %4 offen lds" :: "s"(sb), "i"(imm), "v"(voff), "s"(srd), "s"(soff) : "scc")
#define DMAQ_SC1(sb, imm, voff, srd, soff) asm volatile("s_add_u32 m0, %0, %1\n\ts_nop 0\n\tbuffer_load_dwordx4 %2, %3, %4 offen sc1 lds" :: "s"(sb), "i"(imm), "v"(voff), "s"(srd), "s"(soff) : "scc")
#define WAITV(n) asm volatile("s_waitcnt vmcnt(%0)" :: "i"(n) : "memory")
#define WAITL0() asm volatile("s_waitcnt lgkmcnt(0)" ::: "memory")
#define BAR() asm volatile("s_barrier" ::: "memory")
#define SB() __builtin_amdgcn_sched_barrier(0)
#define MFMA16(a, b, c) __builtin_amdgcn_mfma_f32_16x16x32_bf16((a), (b), (c), 0, 0, 0)
#define MFMA32(a, b, c) __builtin_amdgcn_mfma_f32_32x32x16_bf16((a), (b), (c), 0, 0, 0)
#define VREG(x) asm volatile("" : "+v"(x))
DI __amdgpu_buffer_rsrc_t mksrd(const void* p, unsigned bytes) { return __builtin_amdgcn_make_buffer_rsrc((void*)p, (short)0, (int)bytes, 0x00020000); }
DI s16x4 trread(const char* p) { return __builtin_amdgcn_ds_read_tr16_b64_v4i16((lds_s16x4_ptr)(p)); }
DI bf16x8 cat8(s16x4 lo, s16x4 hi) { return __builtin_shufflevector(lo, hi, 0, 1, 2, 3, 4, 5, 6, 7); }
DI bf16x8 ones8() { const u32x4 o = {0x3F803F80u, 0x3F803F80u, 0x3F803F80u, 0x3F803F80u}; return __builtin_bit_cast(bf16x8, o); }
#define ST_AUX 16
DI void st16(__amdgpu_buffer_rsrc_t srd, unsigned off, u32x4 v) { __builtin_amdgcn_raw_buffer_store_b128(v, srd, off, 0, ST_AUX); }
#define TOUCH(dummy, ptr) asm volatile("global_load_dword %0, %1, off" : "+v"(dummy) : "v"(ptr))
#define STAMP(kid, slot)
#define STAMPW(kid, slot)
#define STAMP_END(kid)

enum { B_NK = 0, B_KN = 1, B_TILED = 2 };
template <int BM_, int NT_, int NP_, int NST_, int BMODE_, int KT_, bool COLSUM_>
struct Cfg {
  static constexpr int BM = BM_, NT = NT_, NP = NP_, NST = NST_, KT = KT_, BMODE = BMODE_;
  static constexpr bool BKN = BMODE_ != B_NK, COLSUM = COLSUM_;
  static constexpr int BN = NT * 16;
  static constexpr int NA = BM / 8, NB = BN / 8, NI = NA + NB;
  static constexpr int ABYTES = BM * 128, BBYTES = BN * 128, STAGE = ABYTES + BBYTES;
  static constexpr int RING = NST * STAGE;
  static constexpr int CS_OFF = RING;
  static constexpr int RS_OFF = RING + BN * 4;
  static constexpr int LDS = RS_OFF + BM * 8;
};

template <class C, int TAILN = 0, int TCPR = 1>
DI void run_producer(char* smem, const void* Ab, unsigned abytes, const void* Bb, unsigned bbytes, int lda, int ldb, int p, int lane, char* ws_dbg, int kid,
                     const void* Tb = nullptr, unsigned tbytes = 0, int ldt = 0) {
  if (p == 0) STAMPW(kid, 4);
  constexpr int HA = C::NA / 2, HB = C::NB / 2, NQ = HA + HB;
  static_assert(C::NA % 4 == 0 && C::NB % 2 == 0, "piece split");
  const __amdgpu_buffer_rsrc_t srdA = mksrd(Ab, abytes), srdB = mksrd(Bb, bbytes);
  const unsigned ldsb = (unsigned)(size_t)smem;
  const unsigned mA = ldsb + p * (HA * 1024), mB = ldsb + C::ABYTES + p * (HB * 1024);
  const unsigned sw0 = ((lane & 7) ^ (lane >> 4)) << 4, sw1 = ((lane & 7) ^ (4 + (lane >> 4))) << 4;
  const unsigned va0 = (p * HA * 8 + (lane >> 3)) * (lda * 2) + sw0, va1 = (p * HA * 8 + (lane >> 3)) * (lda * 2) + sw1;
  unsigned vb[HB];
#pragma unroll
  for (int j = 0; j < HB; ++j) {
    if (C::BMODE == B_NK) {
      const int q = p * HB + j;
      vb[j] = (8 * q + (lane >> 3)) * (ldb * 2) + ((q & 1) ? sw1 : sw0);
    } else if (C::BMODE == B_KN) {
      constexpr int CPR = C::BN / 8;
      const int c = 64 * (p * HB + j) + lane, rho = c / CPR, ch = c - rho * CPR;
      const int k = (rho & ~12) | ((rho & 4) << 1) | ((rho & 8) >> 1);
      vb[j] = k * (ldb * 2) + ch * 16;
    } else {
      vb[j] = (p * HB + j) * 1024 + lane * 16;
    }
  }
  auto issue = [&](int kt) {
    const unsigned so = (kt % C::NST) * C::STAGE, sa = mA + so, sb = mB + so;
    const unsigned soA = kt * 128, soB = C::BMODE == B_NK ? kt * 128 : (C::BMODE == B_KN ? kt * 64 * ldb * 2 : kt * C::BBYTES);
#pragma unroll
    for (int j = 0; j < HA; ++j) { const unsigned v = ((j & 1) ? va1 : va0) + j * 8 * (lda * 2); DMAQ(sa, j * 1024, v, srdA, soA); }
#pragma unroll
    for (int j = 0; j < HB; ++j) DMAQ(sb, j * 1024, vb[j], srdB, soB);
  };
  constexpr int PF = C::NST - 1;
#pragma unroll
  for (int s = 0; s < PF; ++s) issue(s);
  if (p == 0) STAMPW(kid, 5);
  constexpr int NJ = C::COLSUM ? (C::NT + 1) / 2 : 0;
  f32x4 cs[NJ > 0 ? NJ : 1];
#pragma unroll
  for (int j = 0; j < NJ; ++j) cs[j] = f32x4{0.f, 0.f, 0.f, 0.f};
  const int g = lane >> 4;
  const int tb = C::ABYTES + (16 * (g >> 1) + 4 * (g & 1) + ((lane & 15) >> 2)) * (C::BN * 2) + (lane & 3) * 8 + p * 32;
  auto colsum = [&](int kt) {
    if constexpr (C::COLSUM) {
      const char* sb = smem + (kt % C::NST) * C::STAGE + tb;
#pragma unroll
      for (int jj = 0; jj < NJ; ++jj) {
#pragma unroll
        for (int ks = 0; ks < 2; ++ks) {
          const char* q = sb + ks * (32 * C::BN * 2) + jj * 64;
          cs[jj] = MFMA16(cat8(trread(q), trread(q + 8 * C::BN * 2)), ones8(), cs[jj]);
        }
      }
      WAITL0();
    }
  };
  for (int kt = 0; kt < C::KT - PF; ++kt) { WAITV((PF - 1) * NQ); BAR(); issue(kt + PF); colsum(kt); }
  constexpr int NTL = TAILN / 4;
  auto tail = [&](int half) {
    const unsigned sb = ldsb + ((C::KT + half) % C::NST) * C::STAGE + p * (NTL * 1024);
#pragma unroll
    for (int j = 0; j < NTL; ++j) {
      const int c = 64 * (half * 2 * NTL + p * NTL + j) + lane, row = c / TCPR, ch = c - row * TCPR;
      const unsigned vo2 = row * ldt + ch * 16;
      DMAQ(sb, j * 1024, vo2, mksrd(Tb, tbytes), 0u);
    }
  };
#pragma unroll
  for (int r = PF - 1; r >= 0; --r) {
    WAITV(r * NQ + (TAILN > 0 ? (r < PF - 1 ? NTL : 0) + (r < PF - 2 ? NTL : 0) : 0)); BAR();
    if (TAILN > 0 && r == PF - 1) tail(0);
    if (TAILN > 0 && r == PF - 2) tail(1);
    colsum(C::KT - 1 - r);
  }
  if (TAILN > 0) WAITV(0);
  if constexpr (C::COLSUM) {
    float* st = (float*)(smem + C::CS_OFF);
#pragma unroll
    for (int jj = 0; jj < NJ; ++jj) {
      if ((lane & 15) == 0 && p + 2 * jj < C::NT) *(f32x4*)(st + 16 * (p + 2 * jj) + 4 * g) = cs[jj];
    }
    WAITL0();
  }
  BAR();
}

struct NoHook { DI void operator()() const {} };
template <class C, int MI, int NTW, int J0, bool ROLL, bool ROWSTAT, class Hook>
DI void consumer(char* smem, int row0, int lane, f32x4 (&acc)[MI][NTW], float (&rs)[MI], float (&rq)[MI], char* ws_dbg, int kid, int hook_kt, Hook hook) {
  const int g = lane >> 4, fr = lane & 15, f = fr >> 1;
  const int la0 = row0 * 128 + fr * 128 + ((g ^ f) << 4), la1 = row0 * 128 + fr * 128 + (((4 + g) ^ f) << 4);
  const int lb0 = C::ABYTES + J0 * 2048 + fr * 128 + ((g ^ f) << 4), lb1 = C::ABYTES + J0 * 2048 + fr * 128 + (((4 + g) ^ f) << 4);
  const int tb = C::ABYTES + J0 * 32 + (16 * (g >> 1) + 4 * (g & 1) + (fr >> 2)) * (C::BN * 2) + (lane & 3) * 8;
  f32x4 ars[MI], arq[MI];
#pragma unroll
  for (int i = 0; i < MI; ++i) {
    ars[i] = f32x4{0.f, 0.f, 0.f, 0.f}; arq[i] = f32x4{0.f, 0.f, 0.f, 0.f};
#pragma unroll
    for (int j = 0; j < NTW; ++j) acc[i][j] = f32x4{0.f, 0.f, 0.f, 0.f};
  }
  bf16x8 a0[MI], b0[NTW], a1[MI], b1[ROLL ? 1 : NTW];
  auto rda = [&](bf16x8* a, int kt, int ks) {
    const char* sb = smem + (kt % C::NST) * C::STAGE + (ks ? la1 : la0);
#pragma unroll
    for (int i = 0; i < MI; ++i) a[i] = *(const bf16x8*)(sb + i * 2048);
  };
  auto rdb1 = [&](int j, int kt, int ks) -> bf16x8 {
    const char* sb = smem + (kt % C::NST) * C::STAGE;
    if constexpr (C::BKN) {
      const char* p = sb + tb + ks * (32 * C::BN * 2) + j * 32;
      return cat8(trread(p), trread(p + 8 * C::BN * 2));
    } else {
      return *(const bf16x8*)(sb + (ks ? lb1 : lb0) + j * 2048);
    }
  };
  auto stats = [&](bf16x8* a) {
    if constexpr (ROWSTAT) {
#pragma unroll
      for (int i = 0; i < MI; ++i) { ars[i] = MFMA16(ones8(), a[i], ars[i]); arq[i] = MFMA16(a[i], a[i], arq[i]); }
    }
  };
  STAMP(kid, 0);
  BAR();
  STAMP(kid, 1);
  if constexpr (!ROLL) {
    constexpr int NM = MI * NTW + (ROWSTAT ? 2 * MI : 0), ND = MI + NTW * (C::BKN ? 2 : 1), RPG = (ND + (NM * 2 / 3) - 1) / (NM * 2 / 3);
#define SCHED(id) _Pragma("unroll") for (int q = 0; q < NM; ++q) { __builtin_amdgcn_sched_group_barrier(0x008, 1, id); \
    if (q * RPG < ND) __builtin_amdgcn_sched_group_barrier(0x080, RPG, id); }
    auto rd = [&](bf16x8* a, bf16x8* bb, int kt, int ks) {
      rda(a, kt, ks);
#pragma unroll
      for (int j = 0; j < NTW; ++j) bb[j] = rdb1(j, kt, ks);
    };
    auto mm = [&](bf16x8* a, bf16x8* bb) {
#pragma unroll
      for (int j = 0; j < NTW; ++j)
#pragma unroll
        for (int i = 0; i < MI; ++i) acc[i][j] = MFMA16(bb[j], a[i], acc[i][j]);
      stats(a);
    };
    auto body = [&](int kt) {
      SB();
      rd(a1, b1, kt, 1);
      mm(a0, b0);
      SCHED(0)
      SB();
      WAITL0(); BAR();
      SB();
      rd(a0, b0, kt + 1, 0);
      mm(a1, b1);
      SCHED(1)
    };
    rd(a0, b0, 0, 0);
    if (hook_kt > 0) {
      for (int kt = 0; kt < hook_kt; ++kt) body(kt);
      SB(); hook(); SB();
      for (int kt = hook_kt; kt < C::KT - 1; ++kt) body(kt);
    } else {
      for (int kt = 0; kt < C::KT - 1; ++kt) body(kt);
    }
    SB();
    rd(a1, b1, C::KT - 1, 1);
    mm(a0, b0);
    mm(a1, b1);
#undef SCHED
  } else {
    auto sub = [&](bf16x8* a, int kt, int ks, bool more) {
#pragma unroll
      for (int j = 0; j < NTW; ++j) {
#pragma unroll
        for (int i = 0; i < MI; ++i) acc[i][j] = MFMA16(b0[j], a[i], acc[i][j]);
        if (more) b0[j] = rdb1(j, kt, ks);
        SB();
      }
      stats(a);
    };
    rda(a0, 0, 0);
#pragma unroll
    for (int j = 0; j < NTW; ++j) b0[j] = rdb1(j, 0, 0);
    for (int kt = 0; kt < C::KT - 1; ++kt) {
      rda(a1, kt, 1);
      sub(a0, kt, 1, true);
      WAITL0(); BAR();
      rda(a0, kt + 1, 0);
      sub(a1, kt + 1, 0, true);
    }
    rda(a1, C::KT - 1, 1);
    sub(a0, C::KT - 1, 1, true);
    sub(a1, 0, 0, false);
  }
  if constexpr (ROWSTAT) {
#pragma unroll
    for (int i = 0; i < MI; ++i) {
      rs[i] = ars[i][0];
      const float d = (fr & 2) ? ((fr & 1) ? arq[i][3] : arq[i][2]) : ((fr & 1) ? arq[i][1] : arq[i][0]);
      rq[i] = __shfl(d, fr + 16 * (fr >> 2));
    }
  }
  SB();
  WAITL0(); BAR();
  SB();
  STAMP(kid, 2);
}

using C1 = Cfg<128, 9, 4, 4, B_KN, 9, true>;
DI void retile_weights(const u16* __restrict__ win, const u16* __restrict__ wout, const u16* __restrict__ wo, char* __restrict__ ws, int gw, int lane) {
  constexpr int U1 = 32 * 9 * 18, U2 = U1 + 8 * 36 * 10, U3 = U2 + 8 * 9 * 10;
  auto src_of = [&](int u) -> const u16* {
    if (u < U1) {
      const int tile = u / 18, q = u - tile * 18, tn = tile / 9, kt = tile - tn * 9;
      const int c = 64 * q + lane, rho = c / 18, ch = c - rho * 18;
      const int k = (rho & ~12) | ((rho & 4) << 1) | ((rho & 8) >> 1);
      return win + (size_t)(kt * 64 + k) * F2 + tn * 144 + ch * 8;
    }
    const bool o = u < U2; const int v = u - (o ? U1 : U2), KT = o ? 36 : 9;
    const int tile = v / 10, q = v - tile * 10, tn = tile / KT, kt = tile - tn * KT;
    const int c = 64 * q + lane, rho = c / 10, ch = c - rho * 10;
    const int k = (rho & ~12) | ((rho & 4) << 1) | ((rho & 8) >> 1);
    int col = tn * 72 + ch * 8; if (col > D_ - 8) col = D_ - 8;
    return (o ? wout : wo) + (size_t)(kt * 64 + k) * D_ + col;
  };
  char* dst0 = ws + WS_WIN + lane * 16;
  constexpr int NU = (U3 + 511) / 512;
  u32x4 v[NU];
#pragma unroll
  for (int i = 0; i < NU; ++i) { const int u = gw + 512 * i; v[i] = *(const u32x4*)src_of(u < U3 ? u : gw); }
#pragma unroll
  for (int i = 0; i < NU; ++i) { const int u = gw + 512 * i; if (u < U3) *(u32x4*)(dst0 + (size_t)u * 1024) = v[i]; }
}
__global__ __launch_bounds__(512, 2) void k_qkv(const u16* __restrict__ x, const u16* __restrict__ wqkv, const u16* __restrict__ win, const u16* __restrict__ wout,
                                                const u16* __restrict__ wo, char* __restrict__ ws) {
  extern __shared__ __attribute__((aligned(16))) char smem[];
  const int tid = threadIdx.x, w = rfl(tid >> 6);
  const int b = blockIdx.x, xcd = b & 7, idx = b >> 3;
  char* ws_dbg = ws;
  if (b == 255 && tid < 32) *(unsigned*)(ws + WS_CNT + tid * 128) = 0u;
  if (idx >= 24) {
    STAMP(0, 0); STAMP(0, 1); STAMP(0, 2);
    const int pw = (idx - 24) * 8 + xcd;
    f32x4 sa = {0.f, 0.f, 0.f, 0.f}, sb = {0.f, 0.f, 0.f, 0.f};
    const int cc = tid % 9, rg = tid / 9;
    u32x4 cv[11];
    if (rg < 56) {
#pragma unroll
      for (int i = 0; i < 11; ++i) { const int r = rg + 56 * i; cv[i] = *(const u32x4*)(win + (size_t)(r < D_ ? r : rg) * F2 + pw * 72 + cc * 8); }
    }
    retile_weights(win, wout, wo, ws, pw * 8 + w, tid & 63);
    float* red = (float*)smem;
    if (rg < 56) {
#pragma unroll
      for (int i = 0; i < 11; ++i) if (rg + 56 * i < D_) {
        sa += f32x4{bflo(cv[i][0]), bfhi(cv[i][0]), bflo(cv[i][1]), bfhi(cv[i][1])};
        sb += f32x4{bflo(cv[i][2]), bfhi(cv[i][2]), bflo(cv[i][3]), bfhi(cv[i][3])};
      }
      *(f32x4*)(red + rg * 72 + cc * 8) = sa; *(f32x4*)(red + rg * 72 + cc * 8 + 4) = sb;
    }
    __syncthreads();
    if (tid < 72) {
      float t = 0.f;
      for (int r = 0; r < 56; ++r) t += red[r * 72 + tid];
      ((float*)(ws + WS_CS))[pw * 72 + tid] = t;
    }
    STAMP_END(0);
    return;
  }
  const int tm = (xcd & 3) * 4 + (idx & 3), tn = (xcd >> 2) * 6 + (idx >> 2);
  if (w >= 6) return;
  if (__builtin_expect(w >= 4, 1)) {
    run_producer<C1>(smem, x + (size_t)tm * 128 * D_, 128 * D_ * 2, wqkv + tn * 144, (unsigned)((D_ * 3 * D_ - tn * 144) * 2), D_, 3 * D_, w - 4, tid & 63, ws, 0);
    return;
  }
  f32x4 acc[2][9]; float rs[2], rq[2];
  consumer<C1, 2, 9, 0, false, true>(smem, w * 32, tid & 63, acc, rs, rq, ws, 0, 0, NoHook());
  int tz = threadIdx.x; VREG(tz);
  const int lane = tz & 63, g = lane >> 4, fr = lane & 15;
  const float* cs = (const float*)(smem + C1::CS_OFF);
  const int kind = tn >> 2, head0 = (tn & 3) * 8;
  const int tile32 = tm * 4 + w;
  const float sc = kind == 0 ? 0.23570226039551584f * 1.4426950408889634f : 1.f;
  float mu[2], rstd[2];
#pragma unroll
  for (int i = 0; i < 2; ++i) {
    mu[i] = rs[i] * (1.f / D_);
    rstd[i] = rsqrtf(fmaxf(rq[i] * (1.f / D_) - mu[i] * mu[i], 0.f) + 1e-5f) * sc;
  }
  constexpr int RP = 336;
  char* reg = smem + w * (32 * RP);
  f32x4 c4a[9];
#pragma unroll
  for (int j = 0; j < 9; ++j) c4a[j] = *(const f32x4*)(cs + 16 * j + 4 * g);
#pragma unroll
  for (int j = 0; j < 9; ++j) {
    const f32x4 c4 = c4a[j];
#pragma unroll
    for (int rr = 0; rr < 2; ++rr) {
      const int c = 16 * j + 4 * g + 2 * rr, off = c * 2 + (c / 18) * 4;
#pragma unroll
      for (int i = 0; i < 2; ++i)
        *(unsigned*)(reg + (16 * i + fr) * RP + off) = cvtpk((acc[i][j][2 * rr] - mu[i] * c4[2 * rr]) * rstd[i], (acc[i][j][2 * rr + 1] - mu[i] * c4[2 * rr + 1]) * rstd[i]);
    }
  }
  WAITL0();
  if (kind < 2) {
    char* base = ws + (kind == 0 ? WS_Q + (size_t)tile32 * QK_TILE + (size_t)head0 * Q_HEAD : WS_KV + (size_t)tile32 * KV_TILE + (size_t)head0 * KV_HEAD);
    const int hstride = kind == 0 ? Q_HEAD : KV_HEAD;
    const char* rp = reg + (lane & 31) * RP + (lane >> 5) * 16;
#pragma unroll
    for (int hl = 0; hl < 8; ++hl) {
      const u32x2 lo = *(const u32x2*)(rp + hl * 40), hi = *(const u32x2*)(rp + hl * 40 + 8);
      *(u32x4*)(base + (size_t)hl * hstride + lane * 16) = u32x4{lo[0], lo[1], hi[0], hi[1]};
    }
#pragma unroll
    for (int q = 0; q < 4; ++q) {
      const int hl = 2 * q + (lane >> 5);
      *(unsigned*)(base + (size_t)hl * hstride + 1024 + (lane & 31) * 4) = *(const unsigned*)(reg + (lane & 31) * RP + hl * 40 + 32);
    }
  } else {
    char* base = ws + WS_KV + (size_t)tile32 * KV_TILE + V_OFF + (size_t)head0 * KV_HEAD;
    const int s = g >> 1, h = g & 1;
    const char* rp = reg + (16 * s + 4 * h + (fr >> 2)) * RP + (lane & 3) * 8;
#pragma unroll
    for (int j = 0; j < 10; ++j) {
      const bf16x8 v = cat8(trread(rp + j * 32), trread(rp + 8 * RP + j * 32));
      const int c = 16 * j + fr, hl = c / 20, d = c - hl * 20;
      if (d < 18) *(bf16x8*)(base + (size_t)hl * KV_HEAD + s * 608 + h * 304 + d * 16) = v;
    }
    if (lane < 32) {
      *(bf16x8*)(base + (size_t)(lane >> 2) * KV_HEAD + (lane & 3) * 304 + 18 * 16) = ones8();
    }
  }
  STAMP_END(0);
}

constexpr int ATT_LDS = 152 * 1024;
__global__ __launch_bounds__(512, 2) void k_attn(char* __restrict__ ws, const u16* __restrict__ wsp) {
  extern __shared__ __attribute__((aligned(16))) char smem[];
  const int tid = threadIdx.x, lane = tid & 63, w = rfl(tid >> 6);
  const int b = blockIdx.x, xcd = b & 7, idx = b >> 3;
  const int head = xcd * 4 + (idx & 3), qb = idx >> 2;
  char* ws_dbg = ws; STAMP(1, 0);
  const int q31 = lane & 31, h = lane >> 5;
  const char* qp = ws + WS_Q + (size_t)head * Q_HEAD + (size_t)(qb * 8 + w) * QK_TILE;
  bf16x8 qf1; unsigned q2;
  asm volatile("global_load_dwordx4 %0, %1, off" : "=v"(qf1) : "v"(qp + lane * 16));
  asm volatile("global_load_dword %0, %1, off" : "=v"(q2) : "v"(qp + 1024 + q31 * 4));
  {
    __amdgpu_buffer_rsrc_t srd = mksrd(ws + WS_KV + (size_t)head * KV_HEAD, KV_HEAD);
    const unsigned ldsb = (unsigned)(size_t)smem + w * 1024, vo = lane * 16 + w * 1024;
#pragma unroll
    for (int j = 0; j < 19; ++j) DMAQ(ldsb, j * 8192, vo, srd, (unsigned)(j * 8192));
  }
  asm volatile("s_waitcnt vmcnt(16)" : "+v"(qf1), "+v"(q2) :: "memory");
  BAR();
  u32x4 q2v = {h == 0 ? q2 : 0u, 0u, 0u, 0u};
  const bf16x8 qf2 = __builtin_bit_cast(bf16x8, q2v);
  const int dcl = q31 < 19 ? q31 : (q31 >= 20 && q31 < 28 ? 12 : 16);
  const int koff = lane * 16, k2off = 1024 + q31 * 4, voff = V_OFF + h * 304 + dcl * 16;
  f32x16 o;
#pragma unroll
  for (int r = 0; r < 16; ++r) o[r] = 0.f;
  auto scores = [&](int t, const f32x16& c) {
    const char* tp = smem + t * KV_TILE;
    const bf16x8 k1 = *(const bf16x8*)(tp + koff);
    u32x4 k2v = {*(const unsigned*)(tp + k2off), 0u, 0u, 0u};
    f32x16 s = MFMA32(k1, qf1, c);
    return MFMA32(__builtin_bit_cast(bf16x8, k2v), qf2, s);
  };
  f32x16 zero;
#pragma unroll
  for (int r = 0; r < 16; ++r) zero[r] = 0.f;
  STAMP(1, 1);
  f32x16 negm;
  {
    const f32x16 s = scores(0, zero);
    float m = s[0];
#pragma unroll
    for (int r = 1; r < 16; ++r) m = fmaxf(m, s[r]);
    m = fmaxf(m, xor32(m));
#pragma unroll
    for (int r = 0; r < 16; ++r) negm[r] = -m;
  }
  {
    bf16x8 k1, v0, v1; u32x4 k2 = {0u, 0u, 0u, 0u};
    f32x16 sA = scores(0, negm), sB;
    k1 = *(const bf16x8*)(smem + KV_TILE + koff); k2[0] = *(const unsigned*)(smem + KV_TILE + k2off);
    v0 = *(const bf16x8*)(smem + voff); v1 = *(const bf16x8*)(smem + voff + 608);
    auto step = [&](int t, f32x16& sc, f32x16& sn) {
      const char* tp = smem + t * KV_TILE;
      u32x4 p0, p1;
      sn = MFMA32(k1, qf1, negm);
      k1 = *(const bf16x8*)(tp + 2 * KV_TILE + koff);
#pragma unroll
      for (int e = 0; e < 4; ++e) sc[e] = __builtin_amdgcn_exp2f(sc[e]);
      SB();
      sn = MFMA32(__builtin_bit_cast(bf16x8, k2), qf2, sn);
      k2[0] = *(const unsigned*)(tp + 2 * KV_TILE + k2off);
#pragma unroll
      for (int e = 4; e < 8; ++e) sc[e] = __builtin_amdgcn_exp2f(sc[e]);
#pragma unroll
      for (int e = 0; e < 4; ++e) p0[e] = cvtpk(sc[2 * e], sc[2 * e + 1]);
      SB();
      o = MFMA32(v0, __builtin_bit_cast(bf16x8, p0), o);
      v0 = *(const bf16x8*)(tp + KV_TILE + voff);
#pragma unroll
      for (int e = 8; e < 16; ++e) sc[e] = __builtin_amdgcn_exp2f(sc[e]);
#pragma unroll
      for (int e = 0; e < 4; ++e) p1[e] = cvtpk(sc[8 + 2 * e], sc[8 + 2 * e + 1]);
      SB();
      o = MFMA32(v1, __builtin_bit_cast(bf16x8, p1), o);
      v1 = *(const bf16x8*)(tp + KV_TILE + voff + 608);
      SB();
    };
    unsigned dummy = 0;
    auto warm = [&]() { const unsigned T = b * 512 + tid; if (T < 65536u) TOUCH(dummy, (const char*)wsp + (size_t)T * 128); else TOUCH(dummy, (const char*)wsp); };
#define CHUNK(c, nv) { if (c > 0) { WAITV(nv); BAR(); } if (c == 3) warm(); \
    for (int t = 8 * c; t < 8 * c + 8; t += 2) { step(t, sA, sB); step(t + 1, sB, sA); } }
    CHUNK(0, 16) CHUNK(1, 13) CHUNK(2, 11) CHUNK(3, 9) CHUNK(4, 7) CHUNK(5, 5) CHUNK(6, 3) CHUNK(7, 1)
#undef CHUNK
    asm volatile("s_waitcnt vmcnt(0)" : "+v"(dummy) :: "memory");
  }
  STAMP(1, 2);
  float l = __shfl(o[10], q31);
  bool ok = l < 1.8446744e19f;
#pragma unroll
  for (int r = 0; r < 12; ++r) ok = ok && (fabsf(o[r]) <= 3.0e38f);
  if (!__all(ok)) {
    float m = -3.0e38f;
#pragma unroll
    for (int r = 0; r < 16; ++r) o[r] = 0.f;
    for (int t = 0; t < 64; ++t) {
      f32x16 s = scores(t, zero);
      float tmax = s[0];
#pragma unroll
      for (int r = 1; r < 16; ++r) tmax = fmaxf(tmax, s[r]);
      tmax = fmaxf(tmax, xor32(tmax));
      const float mn = fmaxf(m, tmax), alpha = __builtin_amdgcn_exp2f(m - mn);
      m = mn;
#pragma unroll
      for (int r = 0; r < 16; ++r) { o[r] *= alpha; s[r] = __builtin_amdgcn_exp2f(s[r] - mn); }
      const char* tp = smem + t * KV_TILE + voff;
      const bf16x8 v0 = *(const bf16x8*)(tp), v1 = *(const bf16x8*)(tp + 608);
      u32x4 p0, p1;
#pragma unroll
      for (int e = 0; e < 4; ++e) { p0[e] = cvtpk(s[2 * e], s[2 * e + 1]); p1[e] = cvtpk(s[8 + 2 * e], s[8 + 2 * e + 1]); }
      o = MFMA32(v0, __builtin_bit_cast(bf16x8, p0), o);
      o = MFMA32(v1, __builtin_bit_cast(bf16x8, p1), o);
    }
    l = __shfl(o[10], q31);
  }
  const float inv = 1.f / l;
  u16* op = (u16*)(ws + WS_AO) + (size_t)((qb * 8 + w) * 32 + q31) * D_ + head * DH + 4 * h;
  *(unsigned*)(op + 0) = cvtpk(o[0] * inv, o[1] * inv);
  *(unsigned*)(op + 2) = cvtpk(o[2] * inv, o[3] * inv);
  *(unsigned*)(op + 8) = cvtpk(o[4] * inv, o[5] * inv);
  *(unsigned*)(op + 10) = cvtpk(o[6] * inv, o[7] * inv);
  if (h == 0) *(unsigned*)(op + 16) = cvtpk(o[8] * inv, o[9] * inv);
  STAMP_END(1);
}

template <int KT> using CR = Cfg<64, 5, 4, 8, B_TILED, KT, false>;
template <int KT>
__global__ __launch_bounds__(384, 2) void k_res(const u16* __restrict__ A, const char* __restrict__ Wt, const u16* __restrict__ res, u16* __restrict__ out, char* __restrict__ ws) {
  using C = CR<KT>;
  extern __shared__ __attribute__((aligned(16))) char smem[];
  const int tid = threadIdx.x, lane = tid & 63, w = rfl(tid >> 6);
  const int b = blockIdx.x, xcd = b & 7, idx = b >> 3;
  const int tm = (xcd & 3) * 8 + (idx & 7), tn = (xcd >> 2) * 4 + (idx >> 3);
  constexpr int K = KT * 64;
  char* ws_dbg = ws;
  if (__builtin_expect(w >= 4, 1)) {
    run_producer<C>(smem, A + (size_t)tm * 64 * K, 64 * K * 2, Wt + (size_t)tn * KT * WR_TB, KT * WR_TB, K, 0, w - 4, lane, ws, KT == 9 ? 2 : 6);
    return;
  }
  const int g = lane >> 4, fr = lane & 15;
  const int tok = tm * 64 + w * 16 + fr;
  const size_t ro = (size_t)tok * D_ + tn * 72 + 4 * g;
  u32x2 r[5];
#pragma unroll
  for (int j = 0; j < 5; ++j) r[j] = *(const u32x2*)(res + ro + ((j < 4 || g < 2) ? 16 * j : 0));
  f32x4 acc[1][5]; float rs[1], rq[1];
  consumer<C, 1, 5, 0, false, false>(smem, w * 16, lane, acc, rs, rq, ws, KT == 9 ? 2 : 6, 0, NoHook());
  float s1 = 0.f, s2 = 0.f;
#pragma unroll
  for (int j = 0; j < 5; ++j) {
    if (j < 4 || g < 2) {
      u32x2 v;
      v[0] = cvtpk(bflo(r[j][0]) + acc[0][j][0], bfhi(r[j][0]) + acc[0][j][1]);
      v[1] = cvtpk(bflo(r[j][1]) + acc[0][j][2], bfhi(r[j][1]) + acc[0][j][3]);
      *(u32x2*)(out + ro + 16 * j) = v;
      if constexpr (KT == 9) {
        const float a0 = bflo(v[0]), a1 = bfhi(v[0]), a2 = bflo(v[1]), a3 = bfhi(v[1]);
        s1 += (a0 + a1) + (a2 + a3);
        s2 = fmaf(a0, a0, fmaf(a1, a1, fmaf(a2, a2, fmaf(a3, a3, s2))));
      }
    }
  }
  if constexpr (KT == 9) {
    s1 += xor16(s1); s1 += xor32(s1); s2 += xor16(s2); s2 += xor32(s2);
    if (g == 0) *(f32x2*)(ws + WS_HST + ((size_t)tok * 8 + tn) * 8) = f32x2{s1, s2};
  }
  STAMP_END(KT == 9 ? 2 : 6);
}

constexpr int MST = 36864, MNST = 4;
constexpr int M_CSA = MNST * MST, M_CSB = M_CSA + 576, M_RS = M_CSB + 576, M_S2 = M_RS + 1024, M_LDS = M_S2 + 1024;
DI void spin_until(const unsigned* cnt, unsigned target) {
  for (unsigned spins = 0; spins < (1u << 24); ++spins) {
    if (__hip_atomic_load(cnt, __ATOMIC_RELAXED, __HIP_MEMORY_SCOPE_AGENT) >= target) break;
    __builtin_amdgcn_s_sleep(1);
  }
}
__global__ __launch_bounds__(448, 2) void k_mix(const u16* __restrict__ wsp, const u16* __restrict__ bsp, char* __restrict__ ws) {
  extern __shared__ __attribute__((aligned(16))) char smem[];
  const int tid = threadIdx.x, w = rfl(tid >> 6);
  const int b = blockIdx.x, xcd = b & 7, idx = b >> 3;
  const int tm = (xcd & 1) * 8 + (idx & 7), tn = (xcd >> 1) * 4 + (idx >> 3);
  char* ws_dbg = ws;
  unsigned* cnt1 = (unsigned*)(ws + WS_CNT + tm * 128);
  unsigned* cnt2 = (unsigned*)(ws + WS_CNT + (16 + tn) * 128);
  if (__builtin_expect(w == 4 || w == 5, 1)) {
    const int lane = tid & 63, p = w - 4;
    if (p == 0) STAMPW(3, 4);
    const unsigned ldsb = (unsigned)(size_t)smem;
    const unsigned mA = ldsb + p * 8192, mB = ldsb + 16384 + p * 9216;
    const unsigned sw0 = ((lane & 7) ^ (lane >> 4)) << 4, sw1 = ((lane & 7) ^ (4 + (lane >> 4))) << 4;
    const int g = lane >> 4;
    constexpr int NQ = 17;
    {
      const __amdgpu_buffer_rsrc_t srdA = mksrd((const u16*)(ws + WS_H) + (size_t)tm * 128 * D_, 128 * D_ * 2), srdB = mksrd(ws + WS_WIN, 32 * 9 * WIN_TB);
      const unsigned va0 = (p * 64 + (lane >> 3)) * (D_ * 2) + sw0, va1 = (p * 64 + (lane >> 3)) * (D_ * 2) + sw1, vb = p * 9216 + lane * 16;
      auto issue = [&](int s) {
        const int ph = s >= 9, kt = s - 9 * ph;
        const unsigned so = (s & 3) * MST, sa = mA + so, sb = mB + so;
        const unsigned soA = kt * 128, soB = ((ph ? tn : tn + 16) * 9 + kt) * WIN_TB;
#pragma unroll
        for (int j = 0; j < 8; ++j) { const unsigned v = ((j & 1) ? va1 : va0) + j * 8 * (D_ * 2); DMAQ(sa, j * 1024, v, srdA, soA); }
#pragma unroll
        for (int j = 0; j < 9; ++j) { const unsigned v = vb + j * 1024; DMAQ(sb, j * 1024, v, srdB, soB); }
      };
      issue(0); issue(1); issue(2);
      if (p == 0) STAMPW(3, 5);
      for (int s = 0; s < 15; ++s) { WAITV(2 * NQ); BAR(); issue(s + 3); }
      WAITV(2 * NQ); BAR(); WAITV(NQ); BAR(); WAITV(0); BAR();
    }
    BAR();
    BAR();
    if (p == 0) STAMPW(5, 0);
    {
      const __amdgpu_buffer_rsrc_t srdA = mksrd(wsp + (size_t)tm * 128 * S_, 128 * S_ * 2), srdB = mksrd((const u16*)(ws + WS_Z2N) + (size_t)tn * 144 * S_, 144 * S_ * 2);
      const unsigned va0 = (p * 64 + (lane >> 3)) * (S_ * 2) + sw0, va1 = (p * 64 + (lane >> 3)) * (S_ * 2) + sw1;
      const unsigned vb0 = (p * 72 + (lane >> 3)) * (S_ * 2) + (p ? sw1 : sw0), vb1 = (p * 72 + (lane >> 3)) * (S_ * 2) + (p ? sw0 : sw1);
      auto issueA = [&](int kt) {
        const unsigned sa = mA + (kt & 3) * MST, sk = kt * 128;
#pragma unroll
        for (int j = 0; j < 8; ++j) { const unsigned v = ((j & 1) ? va1 : va0) + j * 8 * (S_ * 2); DMAQ(sa, j * 1024, v, srdA, sk); }
      };
      auto issueB = [&](int kt) {
        const unsigned sb = mB + (kt & 3) * MST, sk = kt * 128;
#pragma unroll
        for (int j = 0; j < 9; ++j) { const unsigned v = ((j & 1) ? vb1 : vb0) + j * 8 * (S_ * 2); DMAQ_SC1(sb, j * 1024, v, srdB, sk); }
      };
      auto issue = [&](int kt) { issueA(kt); issueB(kt); };
      issueA(0); issueA(1); issueA(2);
      if (lane == 0) spin_until(cnt2, 16u);
      asm volatile("" ::: "memory");
      if (p == 0) STAMPW(5, 1);
      if (p == 0) STAMPW(5, 2);
      issueB(0); issueB(1); issueB(2);
      if (p == 0) STAMPW(5, 3);
      WAITV(18); BAR(); issue(3);
      WAITV(9 + NQ); BAR(); issue(4);
      for (int kt = 2; kt < 29; ++kt) { WAITV(2 * NQ); BAR(); issue(kt + 3); }
      WAITV(2 * NQ); BAR(); WAITV(NQ); BAR(); WAITV(0); BAR();
    }
    BAR();
    return;
  }
  if (w == 6) {
    const int lane = tid & 63;
    f32x2* rst = (f32x2*)(smem + M_RS);
#pragma unroll
    for (int i = 0; i < 2; ++i) {
      const f32x4* q = (const f32x4*)(ws + WS_HST + (size_t)(tm * 128 + 64 * i + lane) * 64);
      const f32x4 a = q[0], bb = q[1], c = q[2], d = q[3];
      const float s1 = (a[0] + a[2]) + (bb[0] + bb[2]) + (c[0] + c[2]) + (d[0] + d[2]), s2 = (a[1] + a[3]) + (bb[1] + bb[3]) + (c[1] + c[3]) + (d[1] + d[3]);
      const float mu = s1 * (1.f / D_);
      rst[64 * i + lane] = f32x2{mu, rsqrtf(fmaxf(s2 * (1.f / D_) - mu * mu, 0.f) + 1e-5f)};
    }
    if (lane < 36) {
      *(f32x4*)(smem + M_CSA + lane * 16) = *(const f32x4*)(ws + WS_CS + (size_t)((tn + 16) * 144 + lane * 4) * 4);
      *(f32x4*)(smem + M_CSB + lane * 16) = *(const f32x4*)(ws + WS_CS + (size_t)(tn * 144 + lane * 4) * 4);
    }
    WAITL0();
    for (int s = 0; s < 15; ++s) BAR();
    if (lane == 0) spin_until(cnt1, 16u);
    asm volatile("" ::: "memory");
    const __amdgpu_buffer_rsrc_t srdS = mksrd(ws + WS_ST + (size_t)tm * (16 * 128 * 8), 16 * 128 * 8);
    u32x4 pv[16];
#pragma unroll
    for (int q = 0; q < 16; ++q) pv[q] = __builtin_amdgcn_raw_buffer_load_b128(srdS, (q * 128 + 2 * lane) * 8, 0, 16);
    SB(); BAR(); BAR(); SB();
    f32x4 t4 = {0.f, 0.f, 0.f, 0.f};
#pragma unroll
    for (int q = 0; q < 16; ++q) t4 += __builtin_bit_cast(f32x4, pv[q]);
    {
      const float m0 = t4[0] * (1.f / FH), m1 = t4[2] * (1.f / FH);
      const float r0 = rsqrtf(fmaxf(t4[1] * (1.f / FH) - m0 * m0, 0.f) + 1e-5f), r1 = rsqrtf(fmaxf(t4[3] * (1.f / FH) - m1 * m1, 0.f) + 1e-5f);
      *(f32x4*)(smem + M_S2 + lane * 16) = f32x4{r0, -m0 * r0, r1, -m1 * r1};
    }
    WAITL0();
    BAR();
    BAR();
    return;
  }
  const int lane = tid & 63, g = lane >> 4, fr = lane & 15, f = fr >> 1;
  const int l0 = fr * 128 + ((g ^ f) << 4), l1 = fr * 128 + (((4 + g) ^ f) << 4);
  const int tb = 16384 + (16 * (g >> 1) + 4 * (g & 1) + (fr >> 2)) * 288 + (lane & 3) * 8;
  const int tok0 = tm * 128 + w * 32;
  f32x4 acc[2][9];
  bf16x8 a0[2], b0[9], a1[2], b1[9];
  auto zero_acc = [&]() {
#pragma unroll
    for (int i = 0; i < 2; ++i)
#pragma unroll
      for (int j = 0; j < 9; ++j) acc[i][j] = f32x4{0.f, 0.f, 0.f, 0.f};
  };
  auto mm = [&](bf16x8* a, bf16x8* bb) {
#pragma unroll
    for (int j = 0; j < 9; ++j)
#pragma unroll
      for (int i = 0; i < 2; ++i) acc[i][j] = MFMA16(bb[j], a[i], acc[i][j]);
  };
  auto rdKN = [&](bf16x8* a, bf16x8* bb, int s, int ks) {
    const char* sb = smem + (s & 3) * MST;
#pragma unroll
    for (int i = 0; i < 2; ++i) a[i] = *(const bf16x8*)(sb + w * 4096 + i * 2048 + (ks ? l1 : l0));
#pragma unroll
    for (int j = 0; j < 9; ++j) { const char* q = sb + tb + ks * (32 * 288) + j * 32; bb[j] = cat8(trread(q), trread(q + 8 * 288)); }
  };
  auto rdNK = [&](bf16x8* a, bf16x8* bb, int s, int ks) {
    const char* sb = smem + (s & 3) * MST + (ks ? l1 : l0);
#pragma unroll
    for (int i = 0; i < 2; ++i) a[i] = *(const bf16x8*)(sb + w * 4096 + i * 2048);
#pragma unroll
    for (int j = 0; j < 9; ++j) bb[j] = *(const bf16x8*)(sb + 16384 + j * 2048);
  };
#define SCHED(id, ND, RPG) _Pragma("unroll") for (int q = 0; q < 18; ++q) { __builtin_amdgcn_sched_group_barrier(0x008, 1, id); \
    if (q * RPG < ND) __builtin_amdgcn_sched_group_barrier(0x080, RPG, id); }
  auto gelu = [&](u32x2 (&zp)[2][9], int csoff, f32x2 (&sum)[2]) {
    const float c1 = -2.f * 0.7978845608028654f * 1.4426950408889634f, c2 = c1 * 0.044715f;
    const float* cs = (const float*)(smem + csoff);
    const f32x2* rst = (const f32x2*)(smem + M_RS) + w * 32;
    float nmu[2], k1[2], k3[2], irs[2];
    f32x2 s1[2], s2[2];
#pragma unroll
    for (int i = 0; i < 2; ++i) {
      const f32x2 st = rst[16 * i + fr];
      nmu[i] = -st[0]; k1[i] = c1 * st[1]; k3[i] = c2 * st[1] * st[1] * st[1]; irs[i] = __builtin_amdgcn_rcpf(st[1]);
      s1[i] = f32x2{0.f, 0.f}; s2[i] = f32x2{0.f, 0.f};
    }
    f32x4 c4 = *(const f32x4*)(cs + 4 * g);
#pragma unroll
    for (int j = 0; j < 9; ++j) {
      const f32x4 c4n = *(const f32x4*)(cs + 16 * (j < 8 ? j + 1 : 8) + 4 * g);
      SB();
#pragma unroll
      for (int i = 0; i < 2; ++i)
#pragma unroll
        for (int hh = 0; hh < 2; ++hh) {
          const f32x2 a2 = {acc[i][j][2 * hh], acc[i][j][2 * hh + 1]}, c2v = {c4[2 * hh], c4[2 * hh + 1]};
          const f32x2 t = f32x2{nmu[i], nmu[i]} * c2v + a2;
          const f32x2 y = t * (t * t * f32x2{k3[i], k3[i]} + f32x2{k1[i], k1[i]});
          const f32x2 e = {__builtin_amdgcn_exp2f(y[0]), __builtin_amdgcn_exp2f(y[1])};
          const f32x2 d = e * f32x2{irs[i], irs[i]} + f32x2{irs[i], irs[i]};
          const f32x2 z = t * f32x2{__builtin_amdgcn_rcpf(d[0]), __builtin_amdgcn_rcpf(d[1])};
          s1[i] += z; s2[i] += z * z;
          zp[i][j][hh] = cvtpk(z[0], z[1]);
        }
      VREG(zp[0][j]); VREG(zp[1][j]);
      c4 = c4n;
      SB();
    }
#pragma unroll
    for (int i = 0; i < 2; ++i) sum[i] = f32x2{s1[i][0] + s1[i][1], s2[i][0] + s2[i][1]};
  };
  const __amdgpu_buffer_rsrc_t srdS = mksrd(ws + WS_ST + (size_t)tm * (16 * 128 * 8), 16 * 128 * 8);
  zero_acc();
  STAMP(3, 0);
  BAR();
  STAMP(3, 1);
  rdKN(a0, b0, 0, 0);
  u32x2 z2p[2][9];
  auto stageAB = [&](int s) {
    SB(); rdKN(a1, b1, s, 1); mm(a0, b0); SCHED(0, 20, 2) SB();
    WAITL0(); BAR();
    SB(); rdKN(a0, b0, s + 1, 0); mm(a1, b1); SCHED(1, 20, 2)
  };
  for (int s = 0; s < 9; ++s) stageAB(s);
  SB();
  STAMP(5, 6);
  {
    f32x2 sum[2];
    gelu(z2p, M_CSA, sum);
#pragma unroll
    for (int i = 0; i < 2; ++i) {
      f32x2 v = sum[i];
      v[0] += xor16(v[0]); v[0] += xor32(v[0]); v[1] += xor16(v[1]); v[1] += xor32(v[1]);
      if (g == 0) __builtin_amdgcn_raw_buffer_store_b64(__builtin_bit_cast(u32x2, v), srdS, (tn * 128 + w * 32 + 16 * i + fr) * 8, 0, 16);
    }
    asm volatile("s_waitcnt vmcnt(0)" ::: "memory");
    zero_acc();
  }
  SB();
  STAMP(5, 4);
  stageAB(9);
  if (w == 0 && lane == 0) __hip_atomic_fetch_add(cnt1, 1u, __ATOMIC_RELAXED, __HIP_MEMORY_SCOPE_AGENT);
  for (int s = 10; s < 17; ++s) stageAB(s);
  SB(); rdKN(a1, b1, 17, 1); mm(a0, b0); mm(a1, b1); SB();
  WAITL0(); BAR();
  STAMP(3, 2);
  {
    const f32x2* st2 = (const f32x2*)(smem + M_S2) + w * 32;
    char* reg = smem + w * (32 * 304);
#pragma unroll
    for (int i = 0; i < 2; ++i) {
      const f32x2 st = st2[16 * i + fr];
#pragma unroll
      for (int j = 0; j < 9; ++j) {
        u32x2 v;
        v[0] = cvtpk(fmaf(bflo(z2p[i][j][0]), st[0], st[1]), fmaf(bfhi(z2p[i][j][0]), st[0], st[1]));
        v[1] = cvtpk(fmaf(bflo(z2p[i][j][1]), st[0], st[1]), fmaf(bfhi(z2p[i][j][1]), st[0], st[1]));
        *(u32x2*)(reg + (16 * i + fr) * 304 + (16 * j + 4 * g) * 2) = v;
      }
    }
    WAITL0();
    const __amdgpu_buffer_rsrc_t so = mksrd((u16*)(ws + WS_Z2N) + (size_t)(tn * 144) * S_ + tok0, 144 * S_ * 2);
    const char* rp = reg + (8 * g + (fr >> 2)) * 304 + (lane & 3) * 8;
#pragma unroll
    for (int j = 0; j < 9; ++j)
      __builtin_amdgcn_raw_buffer_store_b128(__builtin_bit_cast(u32x4, cat8(trread(rp + j * 32), trread(rp + 4 * 304 + j * 32))), so, (j * 16 + fr) * (S_ * 2) + 8 * g * 2, 0, 16);
    asm volatile("s_waitcnt vmcnt(0)" ::: "memory");
    WAITL0(); BAR();
    if (w == 0 && lane == 0) __hip_atomic_fetch_add(cnt2, 1u, __ATOMIC_RELAXED, __HIP_MEMORY_SCOPE_AGENT);
  }
  u32x2 z1p[2][9];
  { f32x2 sum[2]; gelu(z1p, M_CSB, sum); }
  const unsigned bias0 = bsp[tok0 + fr], bias1 = bsp[tok0 + 16 + fr];
  zero_acc();
  STAMP(4, 0);
  BAR();
  STAMP(4, 1);
  rdNK(a0, b0, 0, 0);
  for (int kt = 0; kt < 31; ++kt) {
    SB(); rdNK(a1, b1, kt, 1); mm(a0, b0); SCHED(0, 11, 1) SB();
    WAITL0(); BAR();
    SB(); rdNK(a0, b0, kt + 1, 0); mm(a1, b1); SCHED(1, 11, 1)
  }
  SB(); rdNK(a1, b1, 31, 1); mm(a0, b0); mm(a1, b1); SB();
  WAITL0(); BAR();
  SB();
  STAMP(4, 2);
#undef SCHED
  char* reg = smem + w * (32 * 304);
#pragma unroll
  for (int i = 0; i < 2; ++i) {
    const float bi = bflo(i ? bias1 : bias0);
#pragma unroll
    for (int j = 0; j < 9; ++j) {
      u32x2 v;
      v[0] = cvtpk(bflo(z1p[i][j][0]) * (acc[i][j][0] + bi), bfhi(z1p[i][j][0]) * (acc[i][j][1] + bi));
      v[1] = cvtpk(bflo(z1p[i][j][1]) * (acc[i][j][2] + bi), bfhi(z1p[i][j][1]) * (acc[i][j][3] + bi));
      *(u32x2*)(reg + (16 * i + fr) * 304 + (16 * j + 4 * g) * 2) = v;
    }
  }
  WAITL0();
  const __amdgpu_buffer_rsrc_t so = mksrd((u16*)(ws + WS_U) + (size_t)tok0 * FH + tn * 144, 32 * FH * 2);
#pragma unroll
  for (int i = 0; i < 9; ++i) {
    const int c = i * 64 + lane, row = c / 18, ch = c - row * 18;
    st16(so, row * (FH * 2) + ch * 16, *(const u32x4*)(reg + row * 304 + ch * 16));
  }
}

extern "C" void kernel_launch(void* const* d_in, const int* in_sizes, int n_in, void* d_out, int out_size, void* d_ws, size_t ws_size, hipStream_t stream) {
  const u16* x = (const u16*)d_in[0];
  const u16* wqkv = (const u16*)d_in[1];
  const u16* wo = (const u16*)d_in[2];
  const u16* win = (const u16*)d_in[4];
  const u16* wsp = (const u16*)d_in[5];
  const u16* bsp = (const u16*)d_in[6];
  const u16* wout = (const u16*)d_in[7];
  char* ws = (char*)d_ws;
  (void)hipFuncSetAttribute((const void*)k_qkv, hipFuncAttributeMaxDynamicSharedMemorySize, C1::LDS);
  (void)hipFuncSetAttribute((const void*)k_attn, hipFuncAttributeMaxDynamicSharedMemorySize, ATT_LDS);
  (void)hipFuncSetAttribute((const void*)k_res<9>, hipFuncAttributeMaxDynamicSharedMemorySize, CR<9>::LDS);
  (void)hipFuncSetAttribute((const void*)k_res<36>, hipFuncAttributeMaxDynamicSharedMemorySize, CR<36>::LDS);
  (void)hipFuncSetAttribute((const void*)k_mix, hipFuncAttributeMaxDynamicSharedMemorySize, M_LDS);
  hipLaunchKernelGGL(k_qkv, dim3(256), dim3(512), C1::LDS, stream, x, wqkv, win, wout, wo, ws);
  hipLaunchKernelGGL(k_attn, dim3(256), dim3(512), ATT_LDS, stream, ws, wsp);
  hipLaunchKernelGGL(k_res<9>, dim3(256), dim3(384), CR<9>::LDS, stream, (const u16*)(ws + WS_AO), (const char*)(ws + WS_WO), x, (u16*)(ws + WS_H), ws);
  hipLaunchKernelGGL(k_mix, dim3(256), dim3(448), M_LDS, stream, wsp, bsp, ws);
  hipLaunchKernelGGL(k_res<36>, dim3(256), dim3(384), CR<36>::LDS, stream, (const u16*)(ws + WS_U), (const char*)(ws + WS_WOUT), (const u16*)(ws + WS_H), (u16*)d_out, ws);
}
```

```cpp
#include <hip/hip_runtime.h>
#include <stdint.h>

typedef unsigned short u16;
typedef short bf16x8 __attribute__((ext_vector_type(8)));
typedef short s16x4 __attribute__((ext_vector_type(4)));
typedef float f32x2 __attribute__((ext_vector_type(2)));
typedef float f32x4 __attribute__((ext_vector_type(4)));
typedef float f32x16 __attribute__((ext_vector_type(16)));
typedef unsigned u32x2 __attribute__((ext_vector_type(2)));
typedef unsigned u32x4 __attribute__((ext_vector_type(4)));
typedef __bf16 bf16x2_t __attribute__((ext_vector_type(2)));
typedef __attribute__((address_space(3))) s16x4* lds_s16x4_ptr;
#define DI __device__ __forceinline__

constexpr int S_ = 2048, D_ = 576, H_ = 32, DH = 18, FH = 2304, F2 = 4608;
constexpr int QK_TILE = 1152;
constexpr int V_OFF = 1152;
constexpr int KV_TILE = 1152 + 4 * 304;
constexpr int KV_HEAD = 64 * KV_TILE;
constexpr int Q_HEAD = 64 * QK_TILE;
constexpr int WIN_TB = 64 * 288, WR_TB = 64 * 160;
constexpr size_t WS_Q = 0;
constexpr size_t WS_KV = WS_Q + (size_t)H_ * Q_HEAD;
constexpr size_t WS_AO = WS_KV + (size_t)H_ * KV_HEAD + 8192;
constexpr size_t WS_H = WS_AO + (size_t)S_ * D_ * 2;
constexpr size_t WS_Z1 = WS_H + (size_t)S_ * D_ * 2;
constexpr size_t WS_Z2T = WS_Z1 + (size_t)S_ * FH * 2;
constexpr size_t WS_Z2N = WS_Z2T + (size_t)S_ * FH * 2;
constexpr size_t WS_ST = WS_Z2N + (size_t)S_ * FH * 2;
constexpr size_t WS_CNT = WS_ST + (size_t)S_ * 16 * 2 * 4;
constexpr size_t WS_HST = WS_ST + (size_t)S_ * 32 * 2 * 4;
constexpr size_t WS_U = WS_HST + (size_t)S_ * 8 * 2 * 4;
constexpr size_t WS_WIN = WS_U + (size_t)S_ * FH * 2;
constexpr size_t WS_WOUT = WS_WIN + (size_t)32 * 9 * WIN_TB;
constexpr size_t WS_WO = WS_WOUT + (size_t)8 * 36 * WR_TB;
constexpr size_t WS_CS = WS_WO + (size_t)8 * 9 * WR_TB;
constexpr size_t WS_END = WS_CS + (size_t)F2 * 4;

DI unsigned cvtpk(float lo, float hi) { f32x2 v = {lo, hi}; bf16x2_t b = __builtin_convertvector(v, bf16x2_t); return __builtin_bit_cast(unsigned, b); }
DI float bflo(unsigned u) { return __uint_as_float(u << 16); }
DI float bfhi(unsigned u) { return __uint_as_float(u & 0xffff0000u); }
DI unsigned rfl(unsigned x) { return __builtin_amdgcn_readfirstlane(x); }
DI float xor16(float v) { return __shfl_xor(v, 16); }
DI float xor32(float v) { return __shfl_xor(v, 32); }
#define DMAQ(sb, imm, voff, srd, soff) asm volatile("s_add_u32 m0, %0, %1\n\ts_nop 0\n\tbuffer_load_dwordx4 %2, %3, %4 offen lds" :: "s"(sb), "i"(imm), "v"(voff), "s"(srd), "s"(soff) : "scc")
#define DMAQ_SC1(sb, imm, voff, srd, soff) asm volatile("s_add_u32 m0, %0, %1\n\ts_nop 0\n\tbuffer_load_dwordx4 %2, %3, %4 offen sc1 lds" :: "s"(sb), "i"(imm), "v"(voff), "s"(srd), "s"(soff) : "scc")
#define WAITV(n) asm volatile("s_waitcnt vmcnt(%0)" :: "i"(n) : "memory")
#define WAITL0() asm volatile("s_waitcnt lgkmcnt(0)" ::: "memory")
#define BAR() asm volatile("s_barrier" ::: "memory")
#define SB() __builtin_amdgcn_sched_barrier(0)
#define MFMA16(a, b, c) __builtin_amdgcn_mfma_f32_16x16x32_bf16((a), (b), (c), 0, 0, 0)
#define MFMA32(a, b, c) __builtin_amdgcn_mfma_f32_32x32x16_bf16((a), (b), (c), 0, 0, 0)
#define VREG(x) asm volatile("" : "+v"(x))
DI __amdgpu_buffer_rsrc_t mksrd(const void* p, unsigned bytes) { return __builtin_amdgcn_make_buffer_rsrc((void*)p, (short)0, (int)bytes, 0x00020000); }
DI s16x4 trread(const char* p) { return __builtin_amdgcn_ds_read_tr16_b64_v4i16((lds_s16x4_ptr)(p)); }
DI bf16x8 cat8(s16x4 lo, s16x4 hi) { return __builtin_shufflevector(lo, hi, 0, 1, 2, 3, 4, 5, 6, 7); }
DI bf16x8 ones8() { const u32x4 o = {0x3F803F80u, 0x3F803F80u, 0x3F803F80u, 0x3F803F80u}; return __builtin_bit_cast(bf16x8, o); }
#define ST_AUX 16
DI void st16(__amdgpu_buffer_rsrc_t srd, unsigned off, u32x4 v) { __builtin_amdgcn_raw_buffer_store_b128(v, srd, off, 0, ST_AUX); }
#define TOUCH(dummy, ptr) asm volatile("global_load_dword %0, %1, off" : "+v"(dummy) : "v"(ptr))
#define STAMP(kid, slot)
#define STAMPW(kid, slot)
#define STAMP_END(kid)

enum { B_NK = 0, B_KN = 1, B_TILED = 2 };
template <int BM_, int NT_, int NP_, int NST_, int BMODE_, int KT_, bool COLSUM_>
struct Cfg {
  static constexpr int BM = BM_, NT = NT_, NP = NP_, NST = NST_, KT = KT_, BMODE = BMODE_;
  static constexpr bool BKN = BMODE_ != B_NK, COLSUM = COLSUM_;
  static constexpr int BN = NT * 16;
  static constexpr int NA = BM / 8, NB = BN / 8, NI = NA + NB;
  static constexpr int ABYTES = BM * 128, BBYTES = BN * 128, STAGE = ABYTES + BBYTES;
  static constexpr int RING = NST * STAGE;
  static constexpr int CS_OFF = RING;
  static constexpr int RS_OFF = RING + BN * 4;
  static constexpr int LDS = RS_OFF + BM * 8;
};

template <class C, int TAILN = 0, int TCPR = 1>
DI void run_producer(char* smem, const void* Ab, unsigned abytes, const void* Bb, unsigned bbytes, int lda, int ldb, int p, int lane, char* ws_dbg, int kid,
                     const void* Tb = nullptr, unsigned tbytes = 0, int ldt = 0) {
  if (p == 0) STAMPW(kid, 4);
  constexpr int HA = C::NA / 2, HB = C::NB / 2, NQ = HA + HB;
  static_assert(C::NA % 4 == 0 && C::NB % 2 == 0, "piece split");
  const __amdgpu_buffer_rsrc_t srdA = mksrd(Ab, abytes), srdB = mksrd(Bb, bbytes);
  const unsigned ldsb = (unsigned)(size_t)smem;
  const unsigned mA = ldsb + p * (HA * 1024), mB = ldsb + C::ABYTES + p * (HB * 1024);
  const unsigned sw0 = ((lane & 7) ^ (lane >> 4)) << 4, sw1 = ((lane & 7) ^ (4 + (lane >> 4))) << 4;
  const unsigned va0 = (p * HA * 8 + (lane >> 3)) * (lda * 2) + sw0, va1 = (p * HA * 8 + (lane >> 3)) * (lda * 2) + sw1;
  unsigned vb[HB];
#pragma unroll
  for (int j = 0; j < HB; ++j) {
    if (C::BMODE == B_NK) {
      const int q = p * HB + j;
      vb[j] = (8 * q + (lane >> 3)) * (ldb * 2) + ((q & 1) ? sw1 : sw0);
    } else if (C::BMODE == B_KN) {
      constexpr int CPR = C::BN / 8;
      const int c = 64 * (p * HB + j) + lane, rho = c / CPR, ch = c - rho * CPR;
      const int k = (rho & ~12) | ((rho & 4) << 1) | ((rho & 8) >> 1);
      vb[j] = k * (ldb * 2) + ch * 16;
    } else {
      vb[j] = (p * HB + j) * 1024 + lane * 16;
    }
  }
  auto issue = [&](int kt) {
    const unsigned so = (kt % C::NST) * C::STAGE, sa = mA + so, sb = mB + so;
    const unsigned soA = kt * 128, soB = C::BMODE == B_NK ? kt * 128 : (C::BMODE == B_KN ? kt * 64 * ldb * 2 : kt * C::BBYTES);
#pragma unroll
    for (int j = 0; j < HA; ++j) { const unsigned v = ((j & 1) ? va1 : va0) + j * 8 * (lda * 2); DMAQ(sa, j * 1024, v, srdA, soA); }
#pragma unroll
    for (int j = 0; j < HB; ++j) DMAQ(sb, j * 1024, vb[j], srdB, soB);
  };
  constexpr int PF = C::NST - 1;
#pragma unroll
  for (int s = 0; s < PF; ++s) issue(s);
  if (p == 0) STAMPW(kid, 5);
  constexpr int NJ = C::COLSUM ? (C::NT + 1) / 2 : 0;
  f32x4 cs[NJ > 0 ? NJ : 1];
#pragma unroll
  for (int j = 0; j < NJ; ++j) cs[j] = f32x4{0.f, 0.f, 0.f, 0.f};
  const int g = lane >> 4;
  const int tb = C::ABYTES + (16 * (g >> 1) + 4 * (g & 1) + ((lane & 15) >> 2)) * (C::BN * 2) + (lane & 3) * 8 + p * 32;
  auto colsum = [&](int kt) {
    if constexpr (C::COLSUM) {
      const char* sb = smem + (kt % C::NST) * C::STAGE + tb;
#pragma unroll
      for (int jj = 0; jj < NJ; ++jj) {
#pragma unroll
        for (int ks = 0; ks < 2; ++ks) {
          const char* q = sb + ks * (32 * C::BN * 2) + jj * 64;
          cs[jj] = MFMA16(cat8(trread(q), trread(q + 8 * C::BN * 2)), ones8(), cs[jj]);
        }
      }
      WAITL0();
    }
  };
  for (int kt = 0; kt < C::KT - PF; ++kt) { WAITV((PF - 1) * NQ); BAR(); issue(kt + PF); colsum(kt); }
  constexpr int NTL = TAILN / 4;
  auto tail = [&](int half) {
    const unsigned sb = ldsb + ((C::KT + half) % C::NST) * C::STAGE + p * (NTL * 1024);
#pragma unroll
    for (int j = 0; j < NTL; ++j) {
      const int c = 64 * (half * 2 * NTL + p * NTL + j) + lane, row = c / TCPR, ch = c - row * TCPR;
      const unsigned vo2 = row * ldt + ch * 16;
      DMAQ(sb, j * 1024, vo2, mksrd(Tb, tbytes), 0u);
    }
  };
#pragma unroll
  for (int r = PF - 1; r >= 0; --r) {
    WAITV(r * NQ + (TAILN > 0 ? (r < PF - 1 ? NTL : 0) + (r < PF - 2 ? NTL : 0) : 0)); BAR();
    if (TAILN > 0 && r == PF - 1) tail(0);
    if (TAILN > 0 && r == PF - 2) tail(1);
    colsum(C::KT - 1 - r);
  }
  if (TAILN > 0) WAITV(0);
  if constexpr (C::COLSUM) {
    float* st = (float*)(smem + C::CS_OFF);
#pragma unroll
    for (int jj = 0; jj < NJ; ++jj) {
      if ((lane & 15) == 0 && p + 2 * jj < C::NT) *(f32x4*)(st + 16 * (p + 2 * jj) + 4 * g) = cs[jj];
    }
    WAITL0();
  }
  BAR();
}

struct NoHook { DI void operator()() const {} };
template <class C, int MI, int NTW, int J0, bool ROLL, bool ROWSTAT, class Hook>
DI void consumer(char* smem, int row0, int lane, f32x4 (&acc)[MI][NTW], float (&rs)[MI], float (&rq)[MI], char* ws_dbg, int kid, int hook_kt, Hook hook) {
  const int g = lane >> 4, fr = lane & 15, f = fr >> 1;
  const int la0 = row0 * 128 + fr * 128 + ((g ^ f) << 4), la1 = row0 * 128 + fr * 128 + (((4 + g) ^ f) << 4);
  const int lb0 = C::ABYTES + J0 * 2048 + fr * 128 + ((g ^ f) << 4), lb1 = C::ABYTES + J0 * 2048 + fr * 128 + (((4 + g) ^ f) << 4);
  const int tb = C::ABYTES + J0 * 32 + (16 * (g >> 1) + 4 * (g & 1) + (fr >> 2)) * (C::BN * 2) + (lane & 3) * 8;
  f32x4 ars[MI], arq[MI];
#pragma unroll
  for (int i = 0; i < MI; ++i) {
    ars[i] = f32x4{0.f, 0.f, 0.f, 0.f}; arq[i] = f32x4{0.f, 0.f, 0.f, 0.f};
#pragma unroll
    for (int j = 0; j < NTW; ++j) acc[i][j] = f32x4{0.f, 0.f, 0.f, 0.f};
  }
  bf16x8 a0[MI], b0[NTW], a1[MI], b1[ROLL ? 1 : NTW];
  auto rda = [&](bf16x8* a, int kt, int ks) {
    const char* sb = smem + (kt % C::NST) * C::STAGE + (ks ? la1 : la0);
#pragma unroll
    for (int i = 0; i < MI; ++i) a[i] = *(const bf16x8*)(sb + i * 2048);
  };
  auto rdb1 = [&](int j, int kt, int ks) -> bf16x8 {
    const char* sb = smem + (kt % C::NST) * C::STAGE;
    if constexpr (C::BKN) {
      const char* p = sb + tb + ks * (32 * C::BN * 2) + j * 32;
      return cat8(trread(p), trread(p + 8 * C::BN * 2));
    } else {
      return *(const bf16x8*)(sb + (ks ? lb1 : lb0) + j * 2048);
    }
  };
  auto stats = [&](bf16x8* a) {
    if constexpr (ROWSTAT) {
#pragma unroll
      for (int i = 0; i < MI; ++i) { ars[i] = MFMA16(ones8(), a[i], ars[i]); arq[i] = MFMA16(a[i], a[i], arq[i]); }
    }
  };
  STAMP(kid, 0);
  BAR();
  STAMP(kid, 1);
  if constexpr (!ROLL) {
    constexpr int NM = MI * NTW + (ROWSTAT ? 2 * MI : 0), ND = MI + NTW * (C::BKN ? 2 : 1), RPG = (ND + (NM * 2 / 3) - 1) / (NM * 2 / 3);
#define SCHED(id) _Pragma("unroll") for (int q = 0; q < NM; ++q) { __builtin_amdgcn_sched_group_barrier(0x008, 1, id); \
    if (q * RPG < ND) __builtin_amdgcn_sched_group_barrier(0x080, RPG, id); }
    auto rd = [&](bf16x8* a, bf16x8* bb, int kt, int ks) {
      rda(a, kt, ks);
#pragma unroll
      for (int j = 0; j < NTW; ++j) bb[j] = rdb1(j, kt, ks);
    };
    auto mm = [&](bf16x8* a, bf16x8* bb) {
#pragma unroll
      for (int j = 0; j < NTW; ++j)
#pragma unroll
        for (int i = 0; i < MI; ++i) acc[i][j] = MFMA16(bb[j], a[i], acc[i][j]);
      stats(a);
    };
    auto body = [&](int kt) {
      SB();
      rd(a1, b1, kt, 1);
      mm(a0, b0);
      SCHED(0)
      SB();
      WAITL0(); BAR();
      SB();
      rd(a0, b0, kt + 1, 0);
      mm(a1, b1);
      SCHED(1)
    };
    rd(a0, b0, 0, 0);
    if (hook_kt > 0) {
      for (int kt = 0; kt < hook_kt; ++kt) body(kt);
      SB(); hook(); SB();
      for (int kt = hook_kt; kt < C::KT - 1; ++kt) body(kt);
    } else {
      for (int kt = 0; kt < C::KT - 1; ++kt) body(kt);
    }
    SB();
    rd(a1, b1, C::KT - 1, 1);
    mm(a0, b0);
    mm(a1, b1);
#undef SCHED
  } else {
    auto sub = [&](bf16x8* a, int kt, int ks, bool more) {
#pragma unroll
      for (int j = 0; j < NTW; ++j) {
#pragma unroll
        for (int i = 0; i < MI; ++i) acc[i][j] = MFMA16(b0[j], a[i], acc[i][j]);
        if (more) b0[j] = rdb1(j, kt, ks);
        SB();
      }
      stats(a);
    };
    rda(a0, 0, 0);
#pragma unroll
    for (int j = 0; j < NTW; ++j) b0[j] = rdb1(j, 0, 0);
    for (int kt = 0; kt < C::KT - 1; ++kt) {
      rda(a1, kt, 1);
      sub(a0, kt, 1, true);
      WAITL0(); BAR();
      rda(a0, kt + 1, 0);
      sub(a1, kt + 1, 0, true);
    }
    rda(a1, C::KT - 1, 1);
    sub(a0, C::KT - 1, 1, true);
    sub(a1, 0, 0, false);
  }
  if constexpr (ROWSTAT) {
#pragma unroll
    for (int i = 0; i < MI; ++i) {
      rs[i] = ars[i][0];
      const float d = (fr & 2) ? ((fr & 1) ? arq[i][3] : arq[i][2]) : ((fr & 1) ? arq[i][1] : arq[i][0]);
      rq[i] = __shfl(d, fr + 16 * (fr >> 2));
    }
  }
  SB();
  WAITL0(); BAR();
  SB();
  STAMP(kid, 2);
}

using C1 = Cfg<128, 9, 4, 4, B_KN, 9, true>;
DI void retile_weights(const u16* __restrict__ win, const u16* __restrict__ wout, const u16* __restrict__ wo, char* __restrict__ ws, int gw, int lane) {
  constexpr int U1 = 32 * 9 * 18, U2 = U1 + 8 * 36 * 10, U3 = U2 + 8 * 9 * 10;
  auto src_of = [&](int u) -> const u16* {
    if (u < U1) {
      const int tile = u / 18, q = u - tile * 18, tn = tile / 9, kt = tile - tn * 9;
      const int c = 64 * q + lane, rho = c / 18, ch = c - rho * 18;
      const int k = (rho & ~12) | ((rho & 4) << 1) | ((rho & 8) >> 1);
      return win + (size_t)(kt * 64 + k) * F2 + tn * 144 + ch * 8;
    }
    const bool o = u < U2; const int v = u - (o ? U1 : U2), KT = o ? 36 : 9;
    const int tile = v / 10, q = v - tile * 10, tn = tile / KT, kt = tile - tn * KT;
    const int c = 64 * q + lane, rho = c / 10, ch = c - rho * 10;
    const int k = (rho & ~12) | ((rho & 4) << 1) | ((rho & 8) >> 1);
    int col = tn * 72 + ch * 8; if (col > D_ - 8) col = D_ - 8;
    return (o ? wout : wo) + (size_t)(kt * 64 + k) * D_ + col;
  };
  char* dst0 = ws + WS_WIN + lane * 16;
  constexpr int NU = (U3 + 511) / 512;
  u32x4 v[NU];
#pragma unroll
  for (int i = 0; i < NU; ++i) { const int u = gw + 512 * i; v[i] = *(const u32x4*)src_of(u < U3 ? u : gw); }
#pragma unroll
  for (int i = 0; i < NU; ++i) { const int u = gw + 512 * i; if (u < U3) *(u32x4*)(dst0 + (size_t)u * 1024) = v[i]; }
}
__global__ __launch_bounds__(512, 2) void k_qkv(const u16* __restrict__ x, const u16* __restrict__ wqkv, const u16* __restrict__ win, const u16* __restrict__ wout,
                                                const u16* __restrict__ wo, char* __restrict__ ws) {
  extern __shared__ __attribute__((aligned(16))) char smem[];
  const int tid = threadIdx.x, w = rfl(tid >> 6);
  const int b = blockIdx.x, xcd = b & 7, idx = b >> 3;
  char* ws_dbg = ws;
  if (b == 255 && tid < 32) *(unsigned*)(ws + WS_CNT + tid * 128) = 0u;
  if (idx >= 24) {
    STAMP(0, 0); STAMP(0, 1); STAMP(0, 2);
    const int pw = (idx - 24) * 8 + xcd;
    f32x4 sa = {0.f, 0.f, 0.f, 0.f}, sb = {0.f, 0.f, 0.f, 0.f};
    const int cc = tid % 9, rg = tid / 9;
    u32x4 cv[11];
    if (rg < 56) {
#pragma unroll
      for (int i = 0; i < 11; ++i) { const int r = rg + 56 * i; cv[i] = *(const u32x4*)(win + (size_t)(r < D_ ? r : rg) * F2 + pw * 72 + cc * 8); }
    }
    retile_weights(win, wout, wo, ws, pw * 8 + w, tid & 63);
    float* red = (float*)smem;
    if (rg < 56) {
#pragma unroll
      for (int i = 0; i < 11; ++i) if (rg + 56 * i < D_) {
        sa += f32x4{bflo(cv[i][0]), bfhi(cv[i][0]), bflo(cv[i][1]), bfhi(cv[i][1])};
        sb += f32x4{bflo(cv[i][2]), bfhi(cv[i][2]), bflo(cv[i][3]), bfhi(cv[i][3])};
      }
      *(f32x4*)(red + rg * 72 + cc * 8) = sa; *(f32x4*)(red + rg * 72 + cc * 8 + 4) = sb;
    }
    __syncthreads();
    if (tid < 72) {
      float t = 0.f;
      for (int r = 0; r < 56; ++r) t += red[r * 72 + tid];
      ((float*)(ws + WS_CS))[pw * 72 + tid] = t;
    }
    STAMP_END(0);
    return;
  }
  const int tm = (xcd & 3) * 4 + (idx & 3), tn = (xcd >> 2) * 6 + (idx >> 2);
  if (w >= 6) return;
  if (__builtin_expect(w >= 4, 1)) {
    run_producer<C1>(smem, x + (size_t)tm * 128 * D_, 128 * D_ * 2, wqkv + tn * 144, (unsigned)((D_ * 3 * D_ - tn * 144) * 2), D_, 3 * D_, w - 4, tid & 63, ws, 0);
    return;
  }
  f32x4 acc[2][9]; float rs[2], rq[2];
  consumer<C1, 2, 9, 0, false, true>(smem, w * 32, tid & 63, acc, rs, rq, ws, 0, 0, NoHook());
  int tz = threadIdx.x; VREG(tz);
  const int lane = tz & 63, g = lane >> 4, fr = lane & 15;
  const float* cs = (const float*)(smem + C1::CS_OFF);
  const int kind = tn >> 2, head0 = (tn & 3) * 8;
  const int tile32 = tm * 4 + w;
  const float sc = kind == 0 ? 0.23570226039551584f * 1.4426950408889634f : 1.f;
  float mu[2], rstd[2];
#pragma unroll
  for (int i = 0; i < 2; ++i) {
    mu[i] = rs[i] * (1.f / D_);
    rstd[i] = rsqrtf(fmaxf(rq[i] * (1.f / D_) - mu[i] * mu[i], 0.f) + 1e-5f) * sc;
  }
  constexpr int RP = 336;
  char* reg = smem + w * (32 * RP);
  f32x4 c4a[9];
#pragma unroll
  for (int j = 0; j < 9; ++j) c4a[j] = *(const f32x4*)(cs + 16 * j + 4 * g);
#pragma unroll
  for (int j = 0; j < 9; ++j) {
    const f32x4 c4 = c4a[j];
#pragma unroll
    for (int rr = 0; rr < 2; ++rr) {
      const int c = 16 * j + 4 * g + 2 * rr, off = c * 2 + (c / 18) * 4;
#pragma unroll
      for (int i = 0; i < 2; ++i)
        *(unsigned*)(reg + (16 * i + fr) * RP + off) = cvtpk((acc[i][j][2 * rr] - mu[i] * c4[2 * rr]) * rstd[i], (acc[i][j][2 * rr + 1] - mu[i] * c4[2 * rr + 1]) * rstd[i]);
    }
  }
  WAITL0();
  if (kind < 2) {
    char* base = ws + (kind == 0 ? WS_Q + (size_t)tile32 * QK_TILE + (size_t)head0 * Q_HEAD : WS_KV + (size_t)tile32 * KV_TILE + (size_t)head0 * KV_HEAD);
    const int hstride = kind == 0 ? Q_HEAD : KV_HEAD;
    const char* rp = reg + (lane & 31) * RP + (lane >> 5) * 16;
#pragma unroll
    for (int hl = 0; hl < 8; ++hl) {
      const u32x2 lo = *(const u32x2*)(rp + hl * 40), hi = *(const u32x2*)(rp + hl * 40 + 8);
      *(u32x4*)(base + (size_t)hl * hstride + lane * 16) = u32x4{lo[0], lo[1], hi[0], hi[1]};
    }
#pragma unroll
    for (int q = 0; q < 4; ++q) {
      const int hl = 2 * q + (lane >> 5);
      *(unsigned*)(base + (size_t)hl * hstride + 1024 + (lane & 31) * 4) = *(const unsigned*)(reg + (lane & 31) * RP + hl * 40 + 32);
    }
  } else {
    char* base = ws + WS_KV + (size_t)tile32 * KV_TILE + V_OFF + (size_t)head0 * KV_HEAD;
    const int s = g >> 1, h = g & 1;
    const char* rp = reg + (16 * s + 4 * h + (fr >> 2)) * RP + (lane & 3) * 8;
#pragma unroll
    for (int j = 0; j < 10; ++j) {
      const bf16x8 v = cat8(trread(rp + j * 32), trread(rp + 8 * RP + j * 32));
      const int c = 16 * j + fr, hl = c / 20, d = c - hl * 20;
      if (d < 18) *(bf16x8*)(base + (size_t)hl * KV_HEAD + s * 608 + h * 304 + d * 16) = v;
    }
    if (lane < 32) {
      *(bf16x8*)(base + (size_t)(lane >> 2) * KV_HEAD + (lane & 3) * 304 + 18 * 16) = ones8();
    }
  }
  STAMP_END(0);
}

constexpr int ATT_LDS = 152 * 1024;
__global__ __launch_bounds__(512, 2) void k_attn(char* __restrict__ ws, const u16* __restrict__ wsp, const char* __restrict__ outp) {
  extern __shared__ __attribute__((aligned(16))) char smem[];
  const int tid = threadIdx.x, lane = tid & 63, w = rfl(tid >> 6);
  const int b = blockIdx.x, xcd = b & 7, idx = b >> 3;
  const int head = xcd * 4 + (idx & 3), qb = idx >> 2;
  char* ws_dbg = ws; STAMP(1, 0);
  const int q31 = lane & 31, h = lane >> 5;
  const char* qp = ws + WS_Q + (size_t)head * Q_HEAD + (size_t)(qb * 8 + w) * QK_TILE;
  bf16x8 qf1; unsigned q2;
  asm volatile("global_load_dwordx4 %0, %1, off" : "=v"(qf1) : "v"(qp + lane * 16));
  asm volatile("global_load_dword %0, %1, off" : "=v"(q2) : "v"(qp + 1024 + q31 * 4));
  {
    __amdgpu_buffer_rsrc_t srd = mksrd(ws + WS_KV + (size_t)head * KV_HEAD, KV_HEAD);
    const unsigned ldsb = (unsigned)(size_t)smem + w * 1024, vo = lane * 16 + w * 1024;
#pragma unroll
    for (int j = 0; j < 19; ++j) DMAQ(ldsb, j * 8192, vo, srd, (unsigned)(j * 8192));
  }
  asm volatile("s_waitcnt vmcnt(16)" : "+v"(qf1), "+v"(q2) :: "memory");
  BAR();
  u32x4 q2v = {h == 0 ? q2 : 0u, 0u, 0u, 0u};
  const bf16x8 qf2 = __builtin_bit_cast(bf16x8, q2v);
  const int dcl = q31 < 19 ? q31 : (q31 >= 20 && q31 < 28 ? 12 : 16);
  const int koff = lane * 16, k2off = 1024 + q31 * 4, voff = V_OFF + h * 304 + dcl * 16;
  f32x16 o;
#pragma unroll
  for (int r = 0; r < 16; ++r) o[r] = 0.f;
  auto scores = [&](int t, const f32x16& c) {
    const char* tp = smem + t * KV_TILE;
    const bf16x8 k1 = *(const bf16x8*)(tp + koff);
    u32x4 k2v = {*(const unsigned*)(tp + k2off), 0u, 0u, 0u};
    f32x16 s = MFMA32(k1, qf1, c);
    return MFMA32(__builtin_bit_cast(bf16x8, k2v), qf2, s);
  };
  f32x16 zero;
#pragma unroll
  for (int r = 0; r < 16; ++r) zero[r] = 0.f;
  STAMP(1, 1);
  f32x16 negm;
  {
    const f32x16 s = scores(0, zero);
    float m = s[0];
#pragma unroll
    for (int r = 1; r < 16; ++r) m = fmaxf(m, s[r]);
    m = fmaxf(m, xor32(m));
#pragma unroll
    for (int r = 0; r < 16; ++r) negm[r] = -m;
  }
  {
    bf16x8 k1, v0, v1; u32x4 k2 = {0u, 0u, 0u, 0u};
    f32x16 sA = scores(0, negm), sB;
    k1 = *(const bf16x8*)(smem + KV_TILE + koff); k2[0] = *(const unsigned*)(smem + KV_TILE + k2off);
    v0 = *(const bf16x8*)(smem + voff); v1 = *(const bf16x8*)(smem + voff + 608);
    auto step = [&](int t, f32x16& sc, f32x16& sn) {
      const char* tp = smem + t * KV_TILE;
      u32x4 p0, p1;
      sn = MFMA32(k1, qf1, negm);
      k1 = *(const bf16x8*)(tp + 2 * KV_TILE + koff);
#pragma unroll
      for (int e = 0; e < 4; ++e) sc[e] = __builtin_amdgcn_exp2f(sc[e]);
      SB();
      sn = MFMA32(__builtin_bit_cast(bf16x8, k2), qf2, sn);
      k2[0] = *(const unsigned*)(tp + 2 * KV_TILE + k2off);
#pragma unroll
      for (int e = 4; e < 8; ++e) sc[e] = __builtin_amdgcn_exp2f(sc[e]);
#pragma unroll
      for (int e = 0; e < 4; ++e) p0[e] = cvtpk(sc[2 * e], sc[2 * e + 1]);
      SB();
      o = MFMA32(v0, __builtin_bit_cast(bf16x8, p0), o);
      v0 = *(const bf16x8*)(tp + KV_TILE + voff);
#pragma unroll
      for (int e = 8; e < 16; ++e) sc[e] = __builtin_amdgcn_exp2f(sc[e]);
#pragma unroll
      for (int e = 0; e < 4; ++e) p1[e] = cvtpk(sc[8 + 2 * e], sc[8 + 2 * e + 1]);
      SB();
      o = MFMA32(v1, __builtin_bit_cast(bf16x8, p1), o);
      v1 = *(const bf16x8*)(tp + KV_TILE + voff + 608);
      SB();
    };
    unsigned dummy = 0;
    auto warm = [&]() {
      const unsigned T = b * 512 + tid; if (T < 65536u) TOUCH(dummy, (const char*)wsp + (size_t)T * 128); else TOUCH(dummy, (const char*)wsp);
    };
#define CHUNK(c, nv) { if (c > 0) { WAITV(nv); BAR(); } if (c == 3) warm(); \
    for (int t = 8 * c; t < 8 * c + 8; t += 2) { step(t, sA, sB); step(t + 1, sB, sA); } }
    CHUNK(0, 16) CHUNK(1, 13) CHUNK(2, 11) CHUNK(3, 9) CHUNK(4, 7) CHUNK(5, 5) CHUNK(6, 3) CHUNK(7, 1)
#undef CHUNK
    asm volatile("s_waitcnt vmcnt(0)" : "+v"(dummy) :: "memory");
  }
  STAMP(1, 2);
  float l = __shfl(o[10], q31);
  bool ok = l < 1.8446744e19f;
#pragma unroll
  for (int r = 0; r < 12; ++r) ok = ok && (fabsf(o[r]) <= 3.0e38f);
  if (!__all(ok)) {
    float m = -3.0e38f;
#pragma unroll
    for (int r = 0; r < 16; ++r) o[r] = 0.f;
    for (int t = 0; t < 64; ++t) {
      f32x16 s = scores(t, zero);
      float tmax = s[0];
#pragma unroll
      for (int r = 1; r < 16; ++r) tmax = fmaxf(tmax, s[r]);
      tmax = fmaxf(tmax, xor32(tmax));
      const float mn = fmaxf(m, tmax), alpha = __builtin_amdgcn_exp2f(m - mn);
      m = mn;
#pragma unroll
      for (int r = 0; r < 16; ++r) { o[r] *= alpha; s[r] = __builtin_amdgcn_exp2f(s[r] - mn); }
      const char* tp = smem + t * KV_TILE + voff;
      const bf16x8 v0 = *(const bf16x8*)(tp), v1 = *(const bf16x8*)(tp + 608);
      u32x4 p0, p1;
#pragma unroll
      for (int e = 0; e < 4; ++e) { p0[e] = cvtpk(s[2 * e], s[2 * e + 1]); p1[e] = cvtpk(s[8 + 2 * e], s[8 + 2 * e + 1]); }
      o = MFMA32(v0, __builtin_bit_cast(bf16x8, p0), o);
      o = MFMA32(v1, __builtin_bit_cast(bf16x8, p1), o);
    }
    l = __shfl(o[10], q31);
  }
  const float inv = 1.f / l;
  u16* op = (u16*)(ws + WS_AO) + (size_t)((qb * 8 + w) * 32 + q31) * D_ + head * DH + 4 * h;
  *(unsigned*)(op + 0) = cvtpk(o[0] * inv, o[1] * inv);
  *(unsigned*)(op + 2) = cvtpk(o[2] * inv, o[3] * inv);
  *(unsigned*)(op + 8) = cvtpk(o[4] * inv, o[5] * inv);
  *(unsigned*)(op + 10) = cvtpk(o[6] * inv, o[7] * inv);
  if (h == 0) *(unsigned*)(op + 16) = cvtpk(o[8] * inv, o[9] * inv);
  STAMP_END(1);
}

template <int KT> using CR = Cfg<64, 5, 4, 8, B_TILED, KT, false>;
template <int KT>
__global__ __launch_bounds__(384, 2) void k_res(const u16* __restrict__ A, const char* __restrict__ Wt, const u16* __restrict__ res, u16* __restrict__ out, char* __restrict__ ws) {
  using C = CR<KT>;
  extern __shared__ __attribute__((aligned(16))) char smem[];
  const int tid = threadIdx.x, lane = tid & 63, w = rfl(tid >> 6);
  const int b = blockIdx.x, xcd = b & 7, idx = b >> 3;
  const int tm = (xcd & 3) * 8 + (idx & 7), tn = (xcd >> 2) * 4 + (idx >> 3);
  constexpr int K = KT * 64;
  char* ws_dbg = ws;
  if (__builtin_expect(w >= 4, 1)) {
    run_producer<C>(smem, A + (size_t)tm * 64 * K, 64 * K * 2, Wt + (size_t)tn * KT * WR_TB, KT * WR_TB, K, 0, w - 4, lane, ws, KT == 9 ? 2 : 6);
    return;
  }
  const int g = lane >> 4, fr = lane & 15;
  const int tok = tm * 64 + w * 16 + fr;
  const size_t ro = (size_t)tok * D_ + tn * 72 + 4 * g;
  u32x2 r[5];
#pragma unroll
  for (int j = 0; j < 5; ++j) r[j] = *(const u32x2*)(res + ro + ((j < 4 || g < 2) ? 16 * j : 0));
  f32x4 acc[1][5]; float rs[1], rq[1];
  consumer<C, 1, 5, 0, false, false>(smem, w * 16, lane, acc, rs, rq, ws, KT == 9 ? 2 : 6, 0, NoHook());
  float s1 = 0.f, s2 = 0.f;
#pragma unroll
  for (int j = 0; j < 5; ++j) {
    if (j < 4 || g < 2) {
      u32x2 v;
      v[0] = cvtpk(bflo(r[j][0]) + acc[0][j][0], bfhi(r[j][0]) + acc[0][j][1]);
      v[1] = cvtpk(bflo(r[j][1]) + acc[0][j][2], bfhi(r[j][1]) + acc[0][j][3]);
      *(u32x2*)(out + ro + 16 * j) = v;
      if constexpr (KT == 9) {
        const float a0 = bflo(v[0]), a1 = bfhi(v[0]), a2 = bflo(v[1]), a3 = bfhi(v[1]);
        s1 += (a0 + a1) + (a2 + a3);
        s2 = fmaf(a0, a0, fmaf(a1, a1, fmaf(a2, a2, fmaf(a3, a3, s2))));
      }
    }
  }
  if constexpr (KT == 9) {
    s1 += xor16(s1); s1 += xor32(s1); s2 += xor16(s2); s2 += xor32(s2);
    if (g == 0) *(f32x2*)(ws + WS_HST + ((size_t)tok * 8 + tn) * 8) = f32x2{s1, s2};
  }
  STAMP_END(KT == 9 ? 2 : 6);
}

constexpr int MST = 36864, MNST = 4;
constexpr int M_CSA = MNST * MST, M_CSB = M_CSA + 576, M_RS = M_CSB + 576, M_S2 = M_RS + 1024, M_LDS = M_S2 + 1024;
DI void spin_until(const unsigned* cnt, unsigned target) {
  for (unsigned spins = 0; spins < (1u << 24); ++spins) {
    if (__hip_atomic_load(cnt, __ATOMIC_RELAXED, __HIP_MEMORY_SCOPE_AGENT) >= target) break;
    __builtin_amdgcn_s_sleep(1);
  }
}
__global__ __launch_bounds__(448, 2) void k_mix(const u16* __restrict__ wsp, const u16* __restrict__ bsp, char* __restrict__ ws) {
  extern __shared__ __attribute__((aligned(16))) char smem[];
  const int tid = threadIdx.x, w = rfl(tid >> 6);
  const int b = blockIdx.x, xcd = b & 7, idx = b >> 3;
  const int tm = (xcd & 1) * 8 + (idx & 7), tn = (xcd >> 1) * 4 + (idx >> 3);
  char* ws_dbg = ws;
  unsigned* cnt1 = (unsigned*)(ws + WS_CNT + tm * 128);
  unsigned* cnt2 = (unsigned*)(ws + WS_CNT + (16 + tn) * 128);
  if (__builtin_expect(w == 4 || w == 5, 1)) {
    const int lane = tid & 63, p = w - 4;
    if (p == 0) STAMPW(3, 4);
    const unsigned ldsb = (unsigned)(size_t)smem;
    const unsigned mA = ldsb + p * 8192, mB = ldsb + 16384 + p * 9216;
    const unsigned sw0 = ((lane & 7) ^ (lane >> 4)) << 4, sw1 = ((lane & 7) ^ (4 + (lane >> 4))) << 4;
    const int g = lane >> 4;
    constexpr int NQ = 17;
    {
      const __amdgpu_buffer_rsrc_t srdA = mksrd((const u16*)(ws + WS_H) + (size_t)tm * 128 * D_, 128 * D_ * 2), srdB = mksrd(ws + WS_WIN, 32 * 9 * WIN_TB);
      const unsigned va0 = (p * 64 + (lane >> 3)) * (D_ * 2) + sw0, va1 = (p * 64 + (lane >> 3)) * (D_ * 2) + sw1, vb = p * 9216 + lane * 16;
      auto issue = [&](int s) {
        const int ph = s >= 9, kt = s - 9 * ph;
        const unsigned so = (s & 3) * MST, sa = mA + so, sb = mB + so;
        const unsigned soA = kt * 128, soB = ((ph ? tn : tn + 16) * 9 + kt) * WIN_TB;
#pragma unroll
        for (int j = 0; j < 8; ++j) { const unsigned v = ((j & 1) ? va1 : va0) + j * 8 * (D_ * 2); DMAQ(sa, j * 1024, v, srdA, soA); }
#pragma unroll
        for (int j = 0; j < 9; ++j) { const unsigned v = vb + j * 1024; DMAQ(sb, j * 1024, v, srdB, soB); }
      };
      issue(0); issue(1); issue(2);
      if (p == 0) STAMPW(3, 5);
      for (int s = 0; s < 15; ++s) { WAITV(2 * NQ); BAR(); issue(s + 3); }
      WAITV(2 * NQ); BAR(); WAITV(NQ); BAR(); WAITV(0); BAR();
    }
    BAR();
    BAR();
    if (p == 0) STAMPW(5, 0);
    {
      const __amdgpu_buffer_rsrc_t srdA = mksrd(wsp + (size_t)tm * 128 * S_, 128 * S_ * 2), srdB = mksrd((const u16*)(ws + WS_Z2N) + (size_t)tn * 144 * S_, 144 * S_ * 2);
      const unsigned va0 = (p * 64 + (lane >> 3)) * (S_ * 2) + sw0, va1 = (p * 64 + (lane >> 3)) * (S_ * 2) + sw1;
      const unsigned vb0 = (p * 72 + (lane >> 3)) * (S_ * 2) + (p ? sw1 : sw0), vb1 = (p * 72 + (lane >> 3)) * (S_ * 2) + (p ? sw0 : sw1);
      auto issueA = [&](int kt) {
        const unsigned sa = mA + (kt & 3) * MST, sk = kt * 128;
#pragma unroll
        for (int j = 0; j < 8; ++j) { const unsigned v = ((j & 1) ? va1 : va0) + j * 8 * (S_ * 2); DMAQ(sa, j * 1024, v, srdA, sk); }
      };
      auto issueB = [&](int kt) {
        const unsigned sb = mB + (kt & 3) * MST, sk = kt * 128;
#pragma unroll
        for (int j = 0; j < 9; ++j) { const unsigned v = ((j & 1) ? vb1 : vb0) + j * 8 * (S_ * 2); DMAQ_SC1(sb, j * 1024, v, srdB, sk); }
      };
      auto issue = [&](int kt) { issueA(kt); issueB(kt); };
      issueA(0); issueA(1); issueA(2);
      if (lane == 0) spin_until(cnt2, 16u);
      asm volatile("" ::: "memory");
      if (p == 0) STAMPW(5, 1);
      if (p == 0) STAMPW(5, 2);
      issueB(0); issueB(1); issueB(2);
      if (p == 0) STAMPW(5, 3);
      WAITV(18); BAR(); issue(3);
      WAITV(9 + NQ); BAR(); issue(4);
      for (int kt = 2; kt < 29; ++kt) { WAITV(2 * NQ); BAR(); issue(kt + 3); }
      WAITV(2 * NQ); BAR(); WAITV(NQ); BAR(); WAITV(0); BAR();
    }
    BAR();
    return;
  }
  if (w == 6) {
    const int lane = tid & 63;
    f32x2* rst = (f32x2*)(smem + M_RS);
#pragma unroll
    for (int i = 0; i < 2; ++i) {
      const f32x4* q = (const f32x4*)(ws + WS_HST + (size_t)(tm * 128 + 64 * i + lane) * 64);
      const f32x4 a = q[0], bb = q[1], c = q[2], d = q[3];
      const float s1 = (a[0] + a[2]) + (bb[0] + bb[2]) + (c[0] + c[2]) + (d[0] + d[2]), s2 = (a[1] + a[3]) + (bb[1] + bb[3]) + (c[1] + c[3]) + (d[1] + d[3]);
      const float mu = s1 * (1.f / D_);
      rst[64 * i + lane] = f32x2{mu, rsqrtf(fmaxf(s2 * (1.f / D_) - mu * mu, 0.f) + 1e-5f)};
    }
    if (lane < 36) {
      *(f32x4*)(smem + M_CSA + lane * 16) = *(const f32x4*)(ws + WS_CS + (size_t)((tn + 16) * 144 + lane * 4) * 4);
      *(f32x4*)(smem + M_CSB + lane * 16) = *(const f32x4*)(ws + WS_CS + (size_t)(tn * 144 + lane * 4) * 4);
    }
    WAITL0();
    for (int s = 0; s < 15; ++s) BAR();
    if (lane == 0) spin_until(cnt1, 16u);
    asm volatile("" ::: "memory");
    const __amdgpu_buffer_rsrc_t srdS = mksrd(ws + WS_ST + (size_t)tm * (16 * 128 * 8), 16 * 128 * 8);
    u32x4 pv[16];
#pragma unroll
    for (int q = 0; q < 16; ++q) pv[q] = __builtin_amdgcn_raw_buffer_load_b128(srdS, (q * 128 + 2 * lane) * 8, 0, 16);
    SB(); BAR(); BAR(); SB();
    f32x4 t4 = {0.f, 0.f, 0.f, 0.f};
#pragma unroll
    for (int q = 0; q < 16; ++q) t4 += __builtin_bit_cast(f32x4, pv[q]);
    {
      const float m0 = t4[0] * (1.f / FH), m1 = t4[2] * (1.f / FH);
      const float r0 = rsqrtf(fmaxf(t4[1] * (1.f / FH) - m0 * m0, 0.f) + 1e-5f), r1 = rsqrtf(fmaxf(t4[3] * (1.f / FH) - m1 * m1, 0.f) + 1e-5f);
      *(f32x4*)(smem + M_S2 + lane * 16) = f32x4{r0, -m0 * r0, r1, -m1 * r1};
    }
    WAITL0();
    BAR();
    BAR();
    return;
  }
  const int lane = tid & 63, g = lane >> 4, fr = lane & 15, f = fr >> 1;
  const int l0 = fr * 128 + ((g ^ f) << 4), l1 = fr * 128 + (((4 + g) ^ f) << 4);
  const int tb = 16384 + (16 * (g >> 1) + 4 * (g & 1) + (fr >> 2)) * 288 + (lane & 3) * 8;
  const int tok0 = tm * 128 + w * 32;
  f32x4 acc[2][9];
  bf16x8 a0[2], b0[9], a1[2], b1[9];
  auto zero_acc = [&]() {
#pragma unroll
    for (int i = 0; i < 2; ++i)
#pragma unroll
      for (int j = 0; j < 9; ++j) acc[i][j] = f32x4{0.f, 0.f, 0.f, 0.f};
  };
  auto mm = [&](bf16x8* a, bf16x8* bb) {
#pragma unroll
    for (int j = 0; j < 9; ++j)
#pragma unroll
      for (int i = 0; i < 2; ++i) acc[i][j] = MFMA16(bb[j], a[i], acc[i][j]);
  };
  auto rdKN = [&](bf16x8* a, bf16x8* bb, int s, int ks) {
    const char* sb = smem + (s & 3) * MST;
#pragma unroll
    for (int i = 0; i < 2; ++i) a[i] = *(const bf16x8*)(sb + w * 4096 + i * 2048 + (ks ? l1 : l0));
#pragma unroll
    for (int j = 0; j < 9; ++j) { const char* q = sb + tb + ks * (32 * 288) + j * 32; bb[j] = cat8(trread(q), trread(q + 8 * 288)); }
  };
  auto rdNK = [&](bf16x8* a, bf16x8* bb, int s, int ks) {
    const char* sb = smem + (s & 3) * MST + (ks ? l1 : l0);
#pragma unroll
    for (int i = 0; i < 2; ++i) a[i] = *(const bf16x8*)(sb + w * 4096 + i * 2048);
#pragma unroll
    for (int j = 0; j < 9; ++j) bb[j] = *(const bf16x8*)(sb + 16384 + j * 2048);
  };
#define SCHED(id, ND, RPG) _Pragma("unroll") for (int q = 0; q < 18; ++q) { __builtin_amdgcn_sched_group_barrier(0x008, 1, id); \
    if (q * RPG < ND) __builtin_amdgcn_sched_group_barrier(0x080, RPG, id); }
  auto gelu = [&](u32x2 (&zp)[2][9], int csoff, f32x2 (&sum)[2]) {
    const float c1 = -2.f * 0.7978845608028654f * 1.4426950408889634f, c2 = c1 * 0.044715f;
    const float* cs = (const float*)(smem + csoff);
    const f32x2* rst = (const f32x2*)(smem + M_RS) + w * 32;
    float nmu[2], k1[2], k3[2], irs[2];
    f32x2 s1[2], s2[2];
#pragma unroll
    for (int i = 0; i < 2; ++i) {
      const f32x2 st = rst[16 * i + fr];
      nmu[i] = -st[0]; k1[i] = c1 * st[1]; k3[i] = c2 * st[1] * st[1] * st[1]; irs[i] = __builtin_amdgcn_rcpf(st[1]);
      s1[i] = f32x2{0.f, 0.f}; s2[i] = f32x2{0.f, 0.f};
    }
    f32x4 c4 = *(const f32x4*)(cs + 4 * g);
#pragma unroll
    for (int j = 0; j < 9; ++j) {
      const f32x4 c4n = *(const f32x4*)(cs + 16 * (j < 8 ? j + 1 : 8) + 4 * g);
      SB();
#pragma unroll
      for (int i = 0; i < 2; ++i)
#pragma unroll
        for (int hh = 0; hh < 2; ++hh) {
          const f32x2 a2 = {acc[i][j][2 * hh], acc[i][j][2 * hh + 1]}, c2v = {c4[2 * hh], c4[2 * hh + 1]};
          const f32x2 t = f32x2{nmu[i], nmu[i]} * c2v + a2;
          const f32x2 y = t * (t * t * f32x2{k3[i], k3[i]} + f32x2{k1[i], k1[i]});
          const f32x2 e = {__builtin_amdgcn_exp2f(y[0]), __builtin_amdgcn_exp2f(y[1])};
          const f32x2 d = e * f32x2{irs[i], irs[i]} + f32x2{irs[i], irs[i]};
          const f32x2 z = t * f32x2{__builtin_amdgcn_rcpf(d[0]), __builtin_amdgcn_rcpf(d[1])};
          s1[i] += z; s2[i] += z * z;
          zp[i][j][hh] = cvtpk(z[0], z[1]);
        }
      VREG(zp[0][j]); VREG(zp[1][j]);
      c4 = c4n;
      SB();
    }
#pragma unroll
    for (int i = 0; i < 2; ++i) sum[i] = f32x2{s1[i][0] + s1[i][1], s2[i][0] + s2[i][1]};
  };
  const __amdgpu_buffer_rsrc_t srdS = mksrd(ws + WS_ST + (size_t)tm * (16 * 128 * 8), 16 * 128 * 8);
  zero_acc();
  STAMP(3, 0);
  BAR();
  STAMP(3, 1);
  rdKN(a0, b0, 0, 0);
  u32x2 z2p[2][9];
  auto stageAB = [&](int s) {
    SB(); rdKN(a1, b1, s, 1); mm(a0, b0); SCHED(0, 20, 2) SB();
    WAITL0(); BAR();
    SB(); rdKN(a0, b0, s + 1, 0); mm(a1, b1); SCHED(1, 20, 2)
  };
  for (int s = 0; s < 9; ++s) stageAB(s);
  SB();
  STAMP(5, 6);
  {
    f32x2 sum[2];
    gelu(z2p, M_CSA, sum);
#pragma unroll
    for (int i = 0; i < 2; ++i) {
      f32x2 v = sum[i];
      v[0] += xor16(v[0]); v[0] += xor32(v[0]); v[1] += xor16(v[1]); v[1] += xor32(v[1]);
      if (g == 0) __builtin_amdgcn_raw_buffer_store_b64(__builtin_bit_cast(u32x2, v), srdS, (tn * 128 + w * 32 + 16 * i + fr) * 8, 0, 16);
    }
    asm volatile("s_waitcnt vmcnt(0)" ::: "memory");
    zero_acc();
  }
  SB();
  STAMP(5, 4);
  stageAB(9);
  if (w == 0 && lane == 0) __hip_atomic_fetch_add(cnt1, 1u, __ATOMIC_RELAXED, __HIP_MEMORY_SCOPE_AGENT);
  for (int s = 10; s < 17; ++s) stageAB(s);
  SB(); rdKN(a1, b1, 17, 1); mm(a0, b0); mm(a1, b1); SB();
  WAITL0(); BAR();
  STAMP(3, 2);
  {
    const f32x2* st2 = (const f32x2*)(smem + M_S2) + w * 32;
    char* reg = smem + w * (32 * 304);
#pragma unroll
    for (int i = 0; i < 2; ++i) {
      const f32x2 st = st2[16 * i + fr];
#pragma unroll
      for (int j = 0; j < 9; ++j) {
        u32x2 v;
        v[0] = cvtpk(fmaf(bflo(z2p[i][j][0]), st[0], st[1]), fmaf(bfhi(z2p[i][j][0]), st[0], st[1]));
        v[1] = cvtpk(fmaf(bflo(z2p[i][j][1]), st[0], st[1]), fmaf(bfhi(z2p[i][j][1]), st[0], st[1]));
        *(u32x2*)(reg + (16 * i + fr) * 304 + (16 * j + 4 * g) * 2) = v;
      }
    }
    WAITL0();
    const __amdgpu_buffer_rsrc_t so = mksrd((u16*)(ws + WS_Z2N) + (size_t)(tn * 144) * S_ + tok0, 144 * S_ * 2);
    const char* rp = reg + (8 * g + (fr >> 2)) * 304 + (lane & 3) * 8;
#pragma unroll
    for (int j = 0; j < 9; ++j)
      __builtin_amdgcn_raw_buffer_store_b128(__builtin_bit_cast(u32x4, cat8(trread(rp + j * 32), trread(rp + 4 * 304 + j * 32))), so, (j * 16 + fr) * (S_ * 2) + 8 * g * 2, 0, 16);
    asm volatile("s_waitcnt vmcnt(0)" ::: "memory");
    WAITL0(); BAR();
    if (w == 0 && lane == 0) __hip_atomic_fetch_add(cnt2, 1u, __ATOMIC_RELAXED, __HIP_MEMORY_SCOPE_AGENT);
  }
  u32x2 z1p[2][9];
  { f32x2 sum[2]; gelu(z1p, M_CSB, sum); }
  const unsigned bias0 = bsp[tok0 + fr], bias1 = bsp[tok0 + 16 + fr];
  zero_acc();
  STAMP(4, 0);
  BAR();
  STAMP(4, 1);
  rdNK(a0, b0, 0, 0);
  for (int kt = 0; kt < 31; ++kt) {
    SB(); rdNK(a1, b1, kt, 1); mm(a0, b0); SCHED(0, 11, 1) SB();
    WAITL0(); BAR();
    SB(); rdNK(a0, b0, kt + 1, 0); mm(a1, b1); SCHED(1, 11, 1)
  }
  SB(); rdNK(a1, b1, 31, 1); mm(a0, b0); mm(a1, b1); SB();
  WAITL0(); BAR();
  SB();
  STAMP(4, 2);
#undef SCHED
  char* reg = smem + w * (32 * 304);
#pragma unroll
  for (int i = 0; i < 2; ++i) {
    const float bi = bflo(i ? bias1 : bias0);
#pragma unroll
    for (int j = 0; j < 9; ++j) {
      u32x2 v;
      v[0] = cvtpk(bflo(z1p[i][j][0]) * (acc[i][j][0] + bi), bfhi(z1p[i][j][0]) * (acc[i][j][1] + bi));
      v[1] = cvtpk(bflo(z1p[i][j][1]) * (acc[i][j][2] + bi), bfhi(z1p[i][j][1]) * (acc[i][j][3] + bi));
      *(u32x2*)(reg + (16 * i + fr) * 304 + (16 * j + 4 * g) * 2) = v;
    }
  }
  WAITL0();
  const __amdgpu_buffer_rsrc_t so = mksrd((u16*)(ws + WS_U) + (size_t)tok0 * FH + tn * 144, 32 * FH * 2);
#pragma unroll
  for (int i = 0; i < 9; ++i) {
    const int c = i * 64 + lane, row = c / 18, ch = c - row * 18;
    st16(so, row * (FH * 2) + ch * 16, *(const u32x4*)(reg + row * 304 + ch * 16));
  }
}

extern "C" void kernel_launch(void* const* d_in, const int* in_sizes, int n_in, void* d_out, int out_size, void* d_ws, size_t ws_size, hipStream_t stream) {
  const u16* x = (const u16*)d_in[0];
  const u16* wqkv = (const u16*)d_in[1];
  const u16* wo = (const u16*)d_in[2];
  const u16* win = (const u16*)d_in[4];
  const u16* wsp = (const u16*)d_in[5];
  const u16* bsp = (const u16*)d_in[6];
  const u16* wout = (const u16*)d_in[7];
  char* ws = (char*)d_ws;
  (void)hipFuncSetAttribute((const void*)k_qkv, hipFuncAttributeMaxDynamicSharedMemorySize, C1::LDS);
  (void)hipFuncSetAttribute((const void*)k_attn, hipFuncAttributeMaxDynamicSharedMemorySize, ATT_LDS);
  (void)hipFuncSetAttribute((const void*)k_res<9>, hipFuncAttributeMaxDynamicSharedMemorySize, CR<9>::LDS);
  (void)hipFuncSetAttribute((const void*)k_res<36>, hipFuncAttributeMaxDynamicSharedMemorySize, CR<36>::LDS);
  (void)hipFuncSetAttribute((const void*)k_mix, hipFuncAttributeMaxDynamicSharedMemorySize, M_LDS);
  hipLaunchKernelGGL(k_qkv, dim3(256), dim3(512), C1::LDS, stream, x, wqkv, win, wout, wo, ws);
  hipLaunchKernelGGL(k_attn, dim3(256), dim3(512), ATT_LDS, stream, ws, wsp, (const char*)d_out);
  hipLaunchKernelGGL(k_res<9>, dim3(256), dim3(384), CR<9>::LDS, stream, (const u16*)(ws + WS_AO), (const char*)(ws + WS_WO), x, (u16*)(ws + WS_H), ws);
  hipLaunchKernelGGL(k_mix, dim3(256), dim3(448), M_LDS, stream, wsp, bsp, ws);
  hipLaunchKernelGGL(k_res<36>, dim3(256), dim3(384), CR<36>::LDS, stream, (const u16*)(ws + WS_U), (const char*)(ws + WS_WOUT), (const u16*)(ws + WS_H), (u16*)d_out, ws);
}
```

```cpp
#include <hip/hip_runtime.h>
#include <stdint.h>

typedef unsigned short u16;
typedef short bf16x8 __attribute__((ext_vector_type(8)));
typedef short s16x4 __attribute__((ext_vector_type(4)));
typedef float f32x2 __attribute__((ext_vector_type(2)));
typedef float f32x4 __attribute__((ext_vector_type(4)));
typedef float f32x16 __attribute__((ext_vector_type(16)));
typedef unsigned u32x2 __attribute__((ext_vector_type(2)));
typedef unsigned u32x4 __attribute__((ext_vector_type(4)));
typedef __bf16 bf16x2_t __attribute__((ext_vector_type(2)));
typedef __attribute__((address_space(3))) s16x4* lds_s16x4_ptr;
#define DI __device__ __forceinline__

constexpr int S_ = 2048, D_ = 576, H_ = 32, DH = 18, FH = 2304, F2 = 4608;
constexpr int QK_TILE = 1152;
constexpr int V_OFF = 1152;
constexpr int KV_TILE = 1152 + 4 * 304;
constexpr int KV_HEAD = 64 * KV_TILE;
constexpr int Q_HEAD = 64 * QK_TILE;
constexpr int WIN_TB = 64 * 288, WR_TB = 64 * 160;
constexpr size_t WS_Q = 0;
constexpr size_t WS_KV = WS_Q + (size_t)H_ * Q_HEAD;
constexpr size_t WS_AO = WS_KV + (size_t)H_ * KV_HEAD + 8192;
constexpr size_t WS_H = WS_AO + (size_t)S_ * D_ * 2;
constexpr size_t WS_Z1 = WS_H + (size_t)S_ * D_ * 2;
constexpr size_t WS_Z2T = WS_Z1 + (size_t)S_ * FH * 2;
constexpr size_t WS_Z2N = WS_Z2T + (size_t)S_ * FH * 2;
constexpr size_t WS_ST = WS_Z2N + (size_t)S_ * FH * 2;
constexpr size_t WS_CNT = WS_ST + (size_t)S_ * 16 * 2 * 4;
constexpr size_t WS_HST = WS_ST + (size_t)S_ * 32 * 2 * 4;
constexpr size_t WS_U = WS_HST + (size_t)S_ * 8 * 2 * 4;
constexpr size_t WS_WIN = WS_U + (size_t)S_ * FH * 2;
constexpr size_t WS_WOUT = WS_WIN + (size_t)32 * 9 * WIN_TB;
constexpr size_t WS_WO = WS_WOUT + (size_t)8 * 36 * WR_TB;
constexpr size_t WS_CS = WS_WO + (size_t)8 * 9 * WR_TB;
constexpr size_t WS_END = WS_CS + (size_t)F2 * 4;

DI unsigned cvtpk(float lo, float hi) { f32x2 v = {lo, hi}; bf16x2_t b = __builtin_convertvector(v, bf16x2_t); return __builtin_bit_cast(unsigned, b); }
DI float bflo(unsigned u) { return __uint_as_float(u << 16); }
DI float bfhi(unsigned u) { return __uint_as_float(u & 0xffff0000u); }
DI unsigned rfl(unsigned x) { return __builtin_amdgcn_readfirstlane(x); }
DI float xor16(float v) { return __shfl_xor(v, 16); }
DI float xor32(float v) { return __shfl_xor(v, 32); }
#define DMAQ(sb, imm, voff, srd, soff) asm volatile("s_add_u32 m0, %0, %1\n\ts_nop 0\n\tbuffer_load_dwordx4 %2, %3, %4 offen lds" :: "s"(sb), "i"(imm), "v"(voff), "s"(srd), "s"(soff) : "scc")
#define DMAQ_SC1(sb, imm, voff, srd, soff) asm volatile("s_add_u32 m0, %0, %1\n\ts_nop 0\n\tbuffer_load_dwordx4 %2, %3, %4 offen sc1 lds" :: "s"(sb), "i"(imm), "v"(voff), "s"(srd), "s"(soff) : "scc")
#define WAITV(n) asm volatile("s_waitcnt vmcnt(%0)" :: "i"(n) : "memory")
#define WAITL0() asm volatile("s_waitcnt lgkmcnt(0)" ::: "memory")
#define BAR() asm volatile("s_barrier" ::: "memory")
#define SB() __builtin_amdgcn_sched_barrier(0)
#define MFMA16(a, b, c) __builtin_amdgcn_mfma_f32_16x16x32_bf16((a), (b), (c), 0, 0, 0)
#define MFMA32(a, b, c) __builtin_amdgcn_mfma_f32_32x32x16_bf16((a), (b), (c), 0, 0, 0)
#define VREG(x) asm volatile("" : "+v"(x))
DI __amdgpu_buffer_rsrc_t mksrd(const void* p, unsigned bytes) { return __builtin_amdgcn_make_buffer_rsrc((void*)p, (short)0, (int)bytes, 0x00020000); }
DI s16x4 trread(const char* p) { return __builtin_amdgcn_ds_read_tr16_b64_v4i16((lds_s16x4_ptr)(p)); }
DI bf16x8 cat8(s16x4 lo, s16x4 hi) { return __builtin_shufflevector(lo, hi, 0, 1, 2, 3, 4, 5, 6, 7); }
DI bf16x8 ones8() { const u32x4 o = {0x3F803F80u, 0x3F803F80u, 0x3F803F80u, 0x3F803F80u}; return __builtin_bit_cast(bf16x8, o); }
#define ROT1 0
#define ST_AUX 16
DI void st16(__amdgpu_buffer_rsrc_t srd, unsigned off, u32x4 v) { __builtin_amdgcn_raw_buffer_store_b128(v, srd, off, 0, ST_AUX); }
#define TOUCH(dummy, ptr) asm volatile("global_load_dword %0, %1, off" : "+v"(dummy) : "v"(ptr))
#define STAMP(kid, slot)
#define STAMPW(kid, slot)
#define STAMP_END(kid)

enum { B_NK = 0, B_KN = 1, B_TILED = 2 };
template <int BM_, int NT_, int NP_, int NST_, int BMODE_, int KT_, bool COLSUM_>
struct Cfg {
  static constexpr int BM = BM_, NT = NT_, NP = NP_, NST = NST_, KT = KT_, BMODE = BMODE_;
  static constexpr bool BKN = BMODE_ != B_NK, COLSUM = COLSUM_;
  static constexpr int BN = NT * 16;
  static constexpr int NA = BM / 8, NB = BN / 8, NI = NA + NB;
  static constexpr int ABYTES = BM * 128, BBYTES = BN * 128, STAGE = ABYTES + BBYTES;
  static constexpr int RING = NST * STAGE;
  static constexpr int CS_OFF = RING;
  static constexpr int RS_OFF = RING + BN * 4;
  static constexpr int LDS = RS_OFF + BM * 8;
};

template <class C, int TAILN = 0, int TCPR = 1>
DI void run_producer(char* smem, const void* Ab, unsigned abytes, const void* Bb, unsigned bbytes, int lda, int ldb, int p, int lane, char* ws_dbg, int kid,
                     int rot = 0, const void* Tb = nullptr, unsigned tbytes = 0, int ldt = 0) {
  if (p == 0) STAMPW(kid, 4);
  constexpr int HA = C::NA / 2, HB = C::NB / 2, NQ = HA + HB;
  static_assert(C::NA % 4 == 0 && C::NB % 2 == 0, "piece split");
  const __amdgpu_buffer_rsrc_t srdA = mksrd(Ab, abytes), srdB = mksrd(Bb, bbytes);
  const unsigned ldsb = (unsigned)(size_t)smem;
  const unsigned mA = ldsb + p * (HA * 1024), mB = ldsb + C::ABYTES + p * (HB * 1024);
  const unsigned sw0 = ((lane & 7) ^ (lane >> 4)) << 4, sw1 = ((lane & 7) ^ (4 + (lane >> 4))) << 4;
  const unsigned va0 = (p * HA * 8 + (lane >> 3)) * (lda * 2) + sw0, va1 = (p * HA * 8 + (lane >> 3)) * (lda * 2) + sw1;
  unsigned vb[HB];
#pragma unroll
  for (int j = 0; j < HB; ++j) {
    if (C::BMODE == B_NK) {
      const int q = p * HB + j;
      vb[j] = (8 * q + (lane >> 3)) * (ldb * 2) + ((q & 1) ? sw1 : sw0);
    } else if (C::BMODE == B_KN) {
      constexpr int CPR = C::BN / 8;
      const int c = 64 * (p * HB + j) + lane, rho = c / CPR, ch = c - rho * CPR;
      const int k = (rho & ~12) | ((rho & 4) << 1) | ((rho & 8) >> 1);
      vb[j] = k * (ldb * 2) + ch * 16;
    } else {
      vb[j] = (p * HB + j) * 1024 + lane * 16;
    }
  }
  auto issue = [&](int kt) {
    const unsigned so = (kt % C::NST) * C::STAGE, sa = mA + so, sb = mB + so;
    int kr = kt + rot; if (kr >= C::KT) kr -= C::KT;
    const unsigned soA = kr * 128, soB = C::BMODE == B_NK ? kr * 128 : (C::BMODE == B_KN ? kr * 64 * ldb * 2 : kr * C::BBYTES);
#pragma unroll
    for (int j = 0; j < HA; ++j) { const unsigned v = ((j & 1) ? va1 : va0) + j * 8 * (lda * 2); DMAQ(sa, j * 1024, v, srdA, soA); }
#pragma unroll
    for (int j = 0; j < HB; ++j) DMAQ(sb, j * 1024, vb[j], srdB, soB);
  };
  constexpr int PF = C::NST - 1;
#pragma unroll
  for (int s = 0; s < PF; ++s) issue(s);
  if (p == 0) STAMPW(kid, 5);
  constexpr int NJ = C::COLSUM ? (C::NT + 1) / 2 : 0;
  f32x4 cs[NJ > 0 ? NJ : 1];
#pragma unroll
  for (int j = 0; j < NJ; ++j) cs[j] = f32x4{0.f, 0.f, 0.f, 0.f};
  const int g = lane >> 4;
  const int tb = C::ABYTES + (16 * (g >> 1) + 4 * (g & 1) + ((lane & 15) >> 2)) * (C::BN * 2) + (lane & 3) * 8 + p * 32;
  auto colsum = [&](int kt) {
    if constexpr (C::COLSUM) {
      const char* sb = smem + (kt % C::NST) * C::STAGE + tb;
#pragma unroll
      for (int jj = 0; jj < NJ; ++jj) {
#pragma unroll
        for (int ks = 0; ks < 2; ++ks) {
          const char* q = sb + ks * (32 * C::BN * 2) + jj * 64;
          cs[jj] = MFMA16(cat8(trread(q), trread(q + 8 * C::BN * 2)), ones8(), cs[jj]);
        }
      }
      WAITL0();
    }
  };
  for (int kt = 0; kt < C::KT - PF; ++kt) { WAITV((PF - 1) * NQ); BAR(); issue(kt + PF); colsum(kt); }
  constexpr int NTL = TAILN / 4;
  auto tail = [&](int half) {
    const unsigned sb = ldsb + ((C::KT + half) % C::NST) * C::STAGE + p * (NTL * 1024);
#pragma unroll
    for (int j = 0; j < NTL; ++j) {
      const int c = 64 * (half * 2 * NTL + p * NTL + j) + lane, row = c / TCPR, ch = c - row * TCPR;
      const unsigned vo2 = row * ldt + ch * 16;
      DMAQ(sb, j * 1024, vo2, mksrd(Tb, tbytes), 0u);
    }
  };
#pragma unroll
  for (int r = PF - 1; r >= 0; --r) {
    WAITV(r * NQ + (TAILN > 0 ? (r < PF - 1 ? NTL : 0) + (r < PF - 2 ? NTL : 0) : 0)); BAR();
    if (TAILN > 0 && r == PF - 1) tail(0);
    if (TAILN > 0 && r == PF - 2) tail(1);
    colsum(C::KT - 1 - r);
  }
  if (TAILN > 0) WAITV(0);
  if constexpr (C::COLSUM) {
    float* st = (float*)(smem + C::CS_OFF);
#pragma unroll
    for (int jj = 0; jj < NJ; ++jj) {
      if ((lane & 15) == 0 && p + 2 * jj < C::NT) *(f32x4*)(st + 16 * (p + 2 * jj) + 4 * g) = cs[jj];
    }
    WAITL0();
  }
  BAR();
}

struct NoHook { DI void operator()() const {} };
template <class C, int MI, int NTW, int J0, bool ROLL, bool ROWSTAT, class Hook>
DI void consumer(char* smem, int row0, int lane, f32x4 (&acc)[MI][NTW], float (&rs)[MI], float (&rq)[MI], char* ws_dbg, int kid, int hook_kt, Hook hook) {
  const int g = lane >> 4, fr = lane & 15, f = fr >> 1;
  const int la0 = row0 * 128 + fr * 128 + ((g ^ f) << 4), la1 = row0 * 128 + fr * 128 + (((4 + g) ^ f) << 4);
  const int lb0 = C::ABYTES + J0 * 2048 + fr * 128 + ((g ^ f) << 4), lb1 = C::ABYTES + J0 * 2048 + fr * 128 + (((4 + g) ^ f) << 4);
  const int tb = C::ABYTES + J0 * 32 + (16 * (g >> 1) + 4 * (g & 1) + (fr >> 2)) * (C::BN * 2) + (lane & 3) * 8;
  f32x4 ars[MI], arq[MI];
#pragma unroll
  for (int i = 0; i < MI; ++i) {
    ars[i] = f32x4{0.f, 0.f, 0.f, 0.f}; arq[i] = f32x4{0.f, 0.f, 0.f, 0.f};
#pragma unroll
    for (int j = 0; j < NTW; ++j) acc[i][j] = f32x4{0.f, 0.f, 0.f, 0.f};
  }
  bf16x8 a0[MI], b0[NTW], a1[MI], b1[ROLL ? 1 : NTW];
  auto rda = [&](bf16x8* a, int kt, int ks) {
    const char* sb = smem + (kt % C::NST) * C::STAGE + (ks ? la1 : la0);
#pragma unroll
    for (int i = 0; i < MI; ++i) a[i] = *(const bf16x8*)(sb + i * 2048);
  };
  auto rdb1 = [&](int j, int kt, int ks) -> bf16x8 {
    const char* sb = smem + (kt % C::NST) * C::STAGE;
    if constexpr (C::BKN) {
      const char* p = sb + tb + ks * (32 * C::BN * 2) + j * 32;
      return cat8(trread(p), trread(p + 8 * C::BN * 2));
    } else {
      return *(const bf16x8*)(sb + (ks ? lb1 : lb0) + j * 2048);
    }
  };
  auto stats = [&](bf16x8* a) {
    if constexpr (ROWSTAT) {
#pragma unroll
      for (int i = 0; i < MI; ++i) { ars[i] = MFMA16(ones8(), a[i], ars[i]); arq[i] = MFMA16(a[i], a[i], arq[i]); }
    }
  };
  STAMP(kid, 0);
  BAR();
  STAMP(kid, 1);
  if constexpr (!ROLL) {
    constexpr int NM = MI * NTW + (ROWSTAT ? 2 * MI : 0), ND = MI + NTW * (C::BKN ? 2 : 1), RPG = (ND + (NM * 2 / 3) - 1) / (NM * 2 / 3);
#define SCHED(id) _Pragma("unroll") for (int q = 0; q < NM; ++q) { __builtin_amdgcn_sched_group_barrier(0x008, 1, id); \
    if (q * RPG < ND) __builtin_amdgcn_sched_group_barrier(0x080, RPG, id); }
    auto rd = [&](bf16x8* a, bf16x8* bb, int kt, int ks) {
      rda(a, kt, ks);
#pragma unroll
      for (int j = 0; j < NTW; ++j) bb[j] = rdb1(j, kt, ks);
    };
    auto mm = [&](bf16x8* a, bf16x8* bb) {
#pragma unroll
      for (int j = 0; j < NTW; ++j)
#pragma unroll
        for (int i = 0; i < MI; ++i) acc[i][j] = MFMA16(bb[j], a[i], acc[i][j]);
      stats(a);
    };
    auto body = [&](int kt) {
      SB();
      rd(a1, b1, kt, 1);
      mm(a0, b0);
      SCHED(0)
      SB();
      WAITL0(); BAR();
      SB();
      rd(a0, b0, kt + 1, 0);
      mm(a1, b1);
      SCHED(1)
    };
    rd(a0, b0, 0, 0);
    if (hook_kt > 0) {
      for (int kt = 0; kt < hook_kt; ++kt) body(kt);
      SB(); hook(); SB();
      for (int kt = hook_kt; kt < C::KT - 1; ++kt) body(kt);
    } else {
      for (int kt = 0; kt < C::KT - 1; ++kt) body(kt);
    }
    SB();
    rd(a1, b1, C::KT - 1, 1);
    mm(a0, b0);
    mm(a1, b1);
#undef SCHED
  } else {
    auto sub = [&](bf16x8* a, int kt, int ks, bool more) {
#pragma unroll
      for (int j = 0; j < NTW; ++j) {
#pragma unroll
        for (int i = 0; i < MI; ++i) acc[i][j] = MFMA16(b0[j], a[i], acc[i][j]);
        if (more) b0[j] = rdb1(j, kt, ks);
        SB();
      }
      stats(a);
    };
    rda(a0, 0, 0);
#pragma unroll
    for (int j = 0; j < NTW; ++j) b0[j] = rdb1(j, 0, 0);
    for (int kt = 0; kt < C::KT - 1; ++kt) {
      rda(a1, kt, 1);
      sub(a0, kt, 1, true);
      WAITL0(); BAR();
      rda(a0, kt + 1, 0);
      sub(a1, kt + 1, 0, true);
    }
    rda(a1, C::KT - 1, 1);
    sub(a0, C::KT - 1, 1, true);
    sub(a1, 0, 0, false);
  }
  if constexpr (ROWSTAT) {
#pragma unroll
    for (int i = 0; i < MI; ++i) {
      rs[i] = ars[i][0];
      const float d = (fr & 2) ? ((fr & 1) ? arq[i][3] : arq[i][2]) : ((fr & 1) ? arq[i][1] : arq[i][0]);
      rq[i] = __shfl(d, fr + 16 * (fr >> 2));
    }
  }
  SB();
  WAITL0(); BAR();
  SB();
  STAMP(kid, 2);
}

using C1 = Cfg<128, 9, 4, 4, B_KN, 9, false>;
constexpr int RU1 = 32 * 9 * 18, RU2 = RU1 + 8 * 36 * 10, RU3 = RU2 + 8 * 9 * 10;
DI const u16* retile_src(const u16* __restrict__ win, const u16* __restrict__ wout, const u16* __restrict__ wo, int u, int lane) {
  if (u < RU1) {
    const int tile = u / 18, q = u - tile * 18, tn = tile / 9, kt = tile - tn * 9;
    const int c = 64 * q + lane, rho = c / 18, ch = c - rho * 18;
    const int k = (rho & ~12) | ((rho & 4) << 1) | ((rho & 8) >> 1);
    return win + (size_t)(kt * 64 + k) * F2 + tn * 144 + ch * 8;
  }
  const bool o = u < RU2; const int v = u - (o ? RU1 : RU2), KT = o ? 36 : 9;
  const int tile = v / 10, q = v - tile * 10, tn = tile / KT, kt = tile - tn * KT;
  const int c = 64 * q + lane, rho = c / 10, ch = c - rho * 10;
  const int k = (rho & ~12) | ((rho & 4) << 1) | ((rho & 8) >> 1);
  int col = tn * 72 + ch * 8; if (col > D_ - 8) col = D_ - 8;
  return (o ? wout : wo) + (size_t)(kt * 64 + k) * D_ + col;
}
__global__ __launch_bounds__(512, 2) void k_qkv(const u16* __restrict__ x, const u16* __restrict__ wqkv, const u16* __restrict__ win, const u16* __restrict__ wout,
                                                const u16* __restrict__ wo, char* __restrict__ ws) {
  extern __shared__ __attribute__((aligned(16))) char smem[];
  const int tid = threadIdx.x, w = rfl(tid >> 6);
  const int b = blockIdx.x, xcd = b & 7, idx = b >> 3;
  char* ws_dbg = ws;
  if (b == 255 && tid < 32) *(unsigned*)(ws + WS_CNT + tid * 128) = 0u;
  if (idx >= 24) {
    STAMP(0, 0); STAMP(0, 1); STAMP(0, 2);
    const int pw = (idx - 24) * 8 + xcd, lane = tid & 63, gw = pw * 8 + w;
    constexpr int NU = (RU3 + 511) / 512;
    u32x4 v[NU];
#pragma unroll
    for (int i = 0; i < NU; ++i) { const int u = gw + 512 * i; v[i] = *(const u32x4*)retile_src(win, wout, wo, u < RU3 ? u : gw, lane); }
    const int cc = tid % 9, rg = tid / 9;
    u32x4 cv[11];
    if (rg < 56) {
#pragma unroll
      for (int i = 0; i < 11; ++i) { const int r = rg + 56 * i; cv[i] = *(const u32x4*)(win + (size_t)(r < D_ ? r : rg) * F2 + pw * 72 + cc * 8); }
    }
    const __amdgpu_buffer_rsrc_t so = mksrd(ws + WS_WIN, RU3 * 1024);
#pragma unroll
    for (int i = 0; i < NU; ++i) { const int u = gw + 512 * i; if (u < RU3) st16(so, u * 1024 + lane * 16, v[i]); }
    float* red = (float*)smem;
    if (rg < 56) {
      f32x4 sa = {0.f, 0.f, 0.f, 0.f}, sb = {0.f, 0.f, 0.f, 0.f};
#pragma unroll
      for (int i = 0; i < 11; ++i) if (rg + 56 * i < D_) {
        sa += f32x4{bflo(cv[i][0]), bfhi(cv[i][0]), bflo(cv[i][1]), bfhi(cv[i][1])};
        sb += f32x4{bflo(cv[i][2]), bfhi(cv[i][2]), bflo(cv[i][3]), bfhi(cv[i][3])};
      }
      *(f32x4*)(red + rg * 72 + cc * 8) = sa; *(f32x4*)(red + rg * 72 + cc * 8 + 4) = sb;
    }
    __syncthreads();
    if (tid < 288) {
      const int c = tid % 72, q = tid / 72;
      float t = 0.f;
#pragma unroll
      for (int r = 0; r < 14; ++r) t += red[(q * 14 + r) * 72 + c];
      red[56 * 72 + tid] = t;
    }
    __syncthreads();
    if (tid < 72) ((float*)(ws + WS_CS))[pw * 72 + tid] = (red[56 * 72 + tid] + red[56 * 72 + 72 + tid]) + (red[56 * 72 + 144 + tid] + red[56 * 72 + 216 + tid]);
    STAMP_END(0);
    return;
  }
  const int tm = (xcd & 3) * 4 + (idx & 3), tn = (xcd >> 2) * 6 + (idx >> 2);
  if (__builtin_expect(w == 4 || w == 5, 1)) {
    run_producer<C1>(smem, x + (size_t)tm * 128 * D_, 128 * D_ * 2, wqkv + tn * 144, (unsigned)((D_ * 3 * D_ - tn * 144) * 2), D_, 3 * D_, w - 4, tid & 63, ws, 0, ROT1);
    return;
  }
  if (w >= 6) {
    const int lane = tid & 63, p = w - 6, g = lane >> 4;
    f32x4 cs[5];
#pragma unroll
    for (int j = 0; j < 5; ++j) cs[j] = f32x4{0.f, 0.f, 0.f, 0.f};
    const int tb = C1::ABYTES + (16 * (g >> 1) + 4 * (g & 1) + ((lane & 15) >> 2)) * 288 + (lane & 3) * 8 + p * 32;
    for (int kt = 0; kt < 9; ++kt) {
      BAR();
      const char* sb = smem + (kt % C1::NST) * C1::STAGE + tb;
#pragma unroll
      for (int jj = 0; jj < 5; ++jj)
#pragma unroll
        for (int ks = 0; ks < 2; ++ks) {
          const char* q = sb + ks * (32 * 288) + jj * 64;
          cs[jj] = MFMA16(cat8(trread(q), trread(q + 8 * 288)), ones8(), cs[jj]);
        }
      WAITL0();
    }
    float* st = (float*)(smem + C1::CS_OFF);
#pragma unroll
    for (int jj = 0; jj < 5; ++jj) if ((lane & 15) == 0 && p + 2 * jj < 9) *(f32x4*)(st + 16 * (p + 2 * jj) + 4 * g) = cs[jj];
    WAITL0(); BAR();
    return;
  }
  f32x4 acc[2][9]; float rs[2], rq[2];
  consumer<C1, 2, 9, 0, false, true>(smem, w * 32, tid & 63, acc, rs, rq, ws, 0, 0, NoHook());
  int tz = threadIdx.x; VREG(tz);
  const int lane = tz & 63, g = lane >> 4, fr = lane & 15;
  const float* cs = (const float*)(smem + C1::CS_OFF);
  const int kind = tn >> 2, head0 = (tn & 3) * 8;
  const int tile32 = tm * 4 + w;
  const float sc = kind == 0 ? 0.23570226039551584f * 1.4426950408889634f : 1.f;
  float mu[2], rstd[2];
#pragma unroll
  for (int i = 0; i < 2; ++i) {
    mu[i] = rs[i] * (1.f / D_);
    rstd[i] = rsqrtf(fmaxf(rq[i] * (1.f / D_) - mu[i] * mu[i], 0.f) + 1e-5f) * sc;
  }
  constexpr int RP = 336;
  char* reg = smem + w * (32 * RP);
  f32x4 c4a[9];
#pragma unroll
  for (int j = 0; j < 9; ++j) c4a[j] = *(const f32x4*)(cs + 16 * j + 4 * g);
#pragma unroll
  for (int j = 0; j < 9; ++j) {
    const f32x4 c4 = c4a[j];
#pragma unroll
    for (int rr = 0; rr < 2; ++rr) {
      const int c = 16 * j + 4 * g + 2 * rr, off = c * 2 + (c / 18) * 4;
#pragma unroll
      for (int i = 0; i < 2; ++i)
        *(unsigned*)(reg + (16 * i + fr) * RP + off) = cvtpk((acc[i][j][2 * rr] - mu[i] * c4[2 * rr]) * rstd[i], (acc[i][j][2 * rr + 1] - mu[i] * c4[2 * rr + 1]) * rstd[i]);
    }
  }
  WAITL0();
  if (kind < 2) {
    char* base = ws + (kind == 0 ? WS_Q + (size_t)tile32 * QK_TILE + (size_t)head0 * Q_HEAD : WS_KV + (size_t)tile32 * KV_TILE + (size_t)head0 * KV_HEAD);
    const int hstride = kind == 0 ? Q_HEAD : KV_HEAD;
    const char* rp = reg + (lane & 31) * RP + (lane >> 5) * 16;
#pragma unroll
    for (int hl = 0; hl < 8; ++hl) {
      const u32x2 lo = *(const u32x2*)(rp + hl * 40), hi = *(const u32x2*)(rp + hl * 40 + 8);
      *(u32x4*)(base + (size_t)hl * hstride + lane * 16) = u32x4{lo[0], lo[1], hi[0], hi[1]};
    }
#pragma unroll
    for (int q = 0; q < 4; ++q) {
      const int hl = 2 * q + (lane >> 5);
      *(unsigned*)(base + (size_t)hl * hstride + 1024 + (lane & 31) * 4) = *(const unsigned*)(reg + (lane & 31) * RP + hl * 40 + 32);
    }
  } else {
    char* base = ws + WS_KV + (size_t)tile32 * KV_TILE + V_OFF + (size_t)head0 * KV_HEAD;
    const int s = g >> 1, h = g & 1;
    const char* rp = reg + (16 * s + 4 * h + (fr >> 2)) * RP + (lane & 3) * 8;
#pragma unroll
    for (int j = 0; j < 10; ++j) {
      const bf16x8 v = cat8(trread(rp + j * 32), trread(rp + 8 * RP + j * 32));
      const int c = 16 * j + fr, hl = c / 20, d = c - hl * 20;
      if (d < 18) *(bf16x8*)(base + (size_t)hl * KV_HEAD + s * 608 + h * 304 + d * 16) = v;
    }
    if (lane < 32) {
      *(bf16x8*)(base + (size_t)(lane >> 2) * KV_HEAD + (lane & 3) * 304 + 18 * 16) = ones8();
    }
  }
  STAMP_END(0);
}

constexpr int ATT_LDS = 152 * 1024;
__global__ __launch_bounds__(512, 2) void k_attn(char* __restrict__ ws, const u16* __restrict__ wsp) {
  extern __shared__ __attribute__((aligned(16))) char smem[];
  const int tid = threadIdx.x, lane = tid & 63, w = rfl(tid >> 6);
  const int b = blockIdx.x, xcd = b & 7, idx = b >> 3;
  const int head = xcd * 4 + (idx & 3), qb = idx >> 2;
  char* ws_dbg = ws; STAMP(1, 0);
  const int q31 = lane & 31, h = lane >> 5;
  const char* qp = ws + WS_Q + (size_t)head * Q_HEAD + (size_t)(qb * 8 + w) * QK_TILE;
  bf16x8 qf1; unsigned q2;
  asm volatile("global_load_dwordx4 %0, %1, off" : "=v"(qf1) : "v"(qp + lane * 16));
  asm volatile("global_load_dword %0, %1, off" : "=v"(q2) : "v"(qp + 1024 + q31 * 4));
  {
    __amdgpu_buffer_rsrc_t srd = mksrd(ws + WS_KV + (size_t)head * KV_HEAD, KV_HEAD);
    const unsigned ldsb = (unsigned)(size_t)smem + w * 1024, vo = lane * 16 + w * 1024;
#pragma unroll
    for (int j = 0; j < 19; ++j) DMAQ(ldsb, j * 8192, vo, srd, (unsigned)(j * 8192));
  }
  asm volatile("s_waitcnt vmcnt(16)" : "+v"(qf1), "+v"(q2) :: "memory");
  BAR();
  u32x4 q2v = {h == 0 ? q2 : 0u, 0u, 0u, 0u};
  const bf16x8 qf2 = __builtin_bit_cast(bf16x8, q2v);
  const int dcl = q31 < 19 ? q31 : (q31 >= 20 && q31 < 28 ? 12 : 16);
  const int koff = lane * 16, k2off = 1024 + q31 * 4, voff = V_OFF + h * 304 + dcl * 16;
  f32x16 o;
#pragma unroll
  for (int r = 0; r < 16; ++r) o[r] = 0.f;
  auto scores = [&](int t, const f32x16& c) {
    const char* tp = smem + t * KV_TILE;
    const bf16x8 k1 = *(const bf16x8*)(tp + koff);
    u32x4 k2v = {*(const unsigned*)(tp + k2off), 0u, 0u, 0u};
    f32x16 s = MFMA32(k1, qf1, c);
    return MFMA32(__builtin_bit_cast(bf16x8, k2v), qf2, s);
  };
  f32x16 zero;
#pragma unroll
  for (int r = 0; r < 16; ++r) zero[r] = 0.f;
  STAMP(1, 1);
  unsigned dummy_keep;
  f32x16 negm;
  {
    const f32x16 s = scores(0, zero);
    float m = s[0];
#pragma unroll
    for (int r = 1; r < 16; ++r) m = fmaxf(m, s[r]);
    m = fmaxf(m, xor32(m));
#pragma unroll
    for (int r = 0; r < 16; ++r) negm[r] = -m;
  }
  {
    bf16x8 k1, v0, v1; u32x4 k2 = {0u, 0u, 0u, 0u};
    f32x16 sA = scores(0, negm), sB;
    k1 = *(const bf16x8*)(smem + KV_TILE + koff); k2[0] = *(const unsigned*)(smem + KV_TILE + k2off);
    v0 = *(const bf16x8*)(smem + voff); v1 = *(const bf16x8*)(smem + voff + 608);
    auto step = [&](int t, f32x16& sc, f32x16& sn) {
      const char* tp = smem + t * KV_TILE;
      u32x4 p0, p1;
      sn = MFMA32(k1, qf1, negm);
      k1 = *(const bf16x8*)(tp + 2 * KV_TILE + koff);
#pragma unroll
      for (int e = 0; e < 4; ++e) sc[e] = __builtin_amdgcn_exp2f(sc[e]);
      SB();
      sn = MFMA32(__builtin_bit_cast(bf16x8, k2), qf2, sn);
      k2[0] = *(const unsigned*)(tp + 2 * KV_TILE + k2off);
#pragma unroll
      for (int e = 4; e < 8; ++e) sc[e] = __builtin_amdgcn_exp2f(sc[e]);
#pragma unroll
      for (int e = 0; e < 4; ++e) p0[e] = cvtpk(sc[2 * e], sc[2 * e + 1]);
      SB();
      o = MFMA32(v0, __builtin_bit_cast(bf16x8, p0), o);
      v0 = *(const bf16x8*)(tp + KV_TILE + voff);
#pragma unroll
      for (int e = 8; e < 16; ++e) sc[e] = __builtin_amdgcn_exp2f(sc[e]);
#pragma unroll
      for (int e = 0; e < 4; ++e) p1[e] = cvtpk(sc[8 + 2 * e], sc[8 + 2 * e + 1]);
      SB();
      o = MFMA32(v1, __builtin_bit_cast(bf16x8, p1), o);
      v1 = *(const bf16x8*)(tp + KV_TILE + voff + 608);
      SB();
    };
    unsigned dummy = 0;
    auto warm = [&]() { const unsigned T = b * 512 + tid; TOUCH(dummy, (const char*)wsp + (size_t)(T < 65536u ? T : 0u) * 128); };
#define CHUNK(c, nv) { if (c > 0) { WAITV(nv); BAR(); } if (c == 3) warm(); \
    for (int t = 8 * c; t < 8 * c + 8; t += 2) { step(t, sA, sB); step(t + 1, sB, sA); } }
    CHUNK(0, 16) CHUNK(1, 13) CHUNK(2, 11) CHUNK(3, 9) CHUNK(4, 7) CHUNK(5, 5) CHUNK(6, 3) CHUNK(7, 1)
#undef CHUNK
    VREG(dummy);
    dummy_keep = dummy;
  }
  STAMP(1, 2);
  float l = __shfl(o[10], q31);
  bool ok = l < 1.8446744e19f;
#pragma unroll
  for (int r = 0; r < 12; ++r) ok = ok && (fabsf(o[r]) <= 3.0e38f);
  if (!__all(ok)) {
    float m = -3.0e38f;
#pragma unroll
    for (int r = 0; r < 16; ++r) o[r] = 0.f;
    for (int t = 0; t < 64; ++t) {
      f32x16 s = scores(t, zero);
      float tmax = s[0];
#pragma unroll
      for (int r = 1; r < 16; ++r) tmax = fmaxf(tmax, s[r]);
      tmax = fmaxf(tmax, xor32(tmax));
      const float mn = fmaxf(m, tmax), alpha = __builtin_amdgcn_exp2f(m - mn);
      m = mn;
#pragma unroll
      for (int r = 0; r < 16; ++r) { o[r] *= alpha; s[r] = __builtin_amdgcn_exp2f(s[r] - mn); }
      const char* tp = smem + t * KV_TILE + voff;
      const bf16x8 v0 = *(const bf16x8*)(tp), v1 = *(const bf16x8*)(tp + 608);
      u32x4 p0, p1;
#pragma unroll
      for (int e = 0; e < 4; ++e) { p0[e] = cvtpk(s[2 * e], s[2 * e + 1]); p1[e] = cvtpk(s[8 + 2 * e], s[8 + 2 * e + 1]); }
      o = MFMA32(v0, __builtin_bit_cast(bf16x8, p0), o);
      o = MFMA32(v1, __builtin_bit_cast(bf16x8, p1), o);
    }
    l = __shfl(o[10], q31);
  }
  const float inv = 1.f / l;
  u16* op = (u16*)(ws + WS_AO) + (size_t)((qb * 8 + w) * 32 + q31) * D_ + head * DH + 4 * h;
  *(unsigned*)(op + 0) = cvtpk(o[0] * inv, o[1] * inv);
  *(unsigned*)(op + 2) = cvtpk(o[2] * inv, o[3] * inv);
  *(unsigned*)(op + 8) = cvtpk(o[4] * inv, o[5] * inv);
  *(unsigned*)(op + 10) = cvtpk(o[6] * inv, o[7] * inv);
  if (h == 0) *(unsigned*)(op + 16) = cvtpk(o[8] * inv, o[9] * inv);
  asm volatile("s_waitcnt vmcnt(0)" : "+v"(dummy_keep) :: "memory");
  STAMP_END(1);
}

template <int KT> using CR = Cfg<64, 5, 4, 8, B_TILED, KT, false>;
template <int KT>
__global__ __launch_bounds__(384, 2) void k_res(const u16* __restrict__ A, const char* __restrict__ Wt, const u16* __restrict__ res, u16* __restrict__ out, char* __restrict__ ws) {
  using C = CR<KT>;
  extern __shared__ __attribute__((aligned(16))) char smem[];
  const int tid = threadIdx.x, lane = tid & 63, w = rfl(tid >> 6);
  const int b = blockIdx.x, xcd = b & 7, idx = b >> 3;
  const int tm = (xcd & 3) * 8 + (idx & 7), tn = (xcd >> 2) * 4 + (idx >> 3);
  constexpr int K = KT * 64;
  char* ws_dbg = ws;
  if (__builtin_expect(w >= 4, 1)) {
    run_producer<C>(smem, A + (size_t)tm * 64 * K, 64 * K * 2, Wt + (size_t)tn * KT * WR_TB, KT * WR_TB, K, 0, w - 4, lane, ws, KT == 9 ? 2 : 6);
    return;
  }
  const int g = lane >> 4, fr = lane & 15;
  const int tok = tm * 64 + w * 16 + fr;
  const size_t ro = (size_t)tok * D_ + tn * 72 + 4 * g;
  u32x2 r[5];
#pragma unroll
  for (int j = 0; j < 5; ++j) r[j] = *(const u32x2*)(res + ro + ((j < 4 || g < 2) ? 16 * j : 0));
  f32x4 acc[1][5]; float rs[1], rq[1];
  consumer<C, 1, 5, 0, false, false>(smem, w * 16, lane, acc, rs, rq, ws, KT == 9 ? 2 : 6, 0, NoHook());
  float s1 = 0.f, s2 = 0.f;
#pragma unroll
  for (int j = 0; j < 5; ++j) {
    if (j < 4 || g < 2) {
      u32x2 v;
      v[0] = cvtpk(bflo(r[j][0]) + acc[0][j][0], bfhi(r[j][0]) + acc[0][j][1]);
      v[1] = cvtpk(bflo(r[j][1]) + acc[0][j][2], bfhi(r[j][1]) + acc[0][j][3]);
      *(u32x2*)(out + ro + 16 * j) = v;
      if constexpr (KT == 9) {
        const float a0 = bflo(v[0]), a1 = bfhi(v[0]), a2 = bflo(v[1]), a3 = bfhi(v[1]);
        s1 += (a0 + a1) + (a2 + a3);
        s2 = fmaf(a0, a0, fmaf(a1, a1, fmaf(a2, a2, fmaf(a3, a3, s2))));
      }
    }
  }
  if constexpr (KT == 9) {
    s1 += xor16(s1); s1 += xor32(s1); s2 += xor16(s2); s2 += xor32(s2);
    if (g == 0) *(f32x2*)(ws + WS_HST + ((size_t)tok * 8 + tn) * 8) = f32x2{s1, s2};
  }
  STAMP_END(KT == 9 ? 2 : 6);
}

constexpr int MST = 36864, MNST = 4;
constexpr int M_CSA = MNST * MST, M_CSB = M_CSA + 576, M_RS = M_CSB + 576, M_S2 = M_RS + 1024, M_LDS = M_S2 + 1024;
DI void spin_until(const unsigned* cnt, unsigned target) {
  for (unsigned spins = 0; spins < (1u << 24); ++spins) {
    if (__hip_atomic_load(cnt, __ATOMIC_RELAXED, __HIP_MEMORY_SCOPE_AGENT) >= target) break;
    __builtin_amdgcn_s_sleep(1);
  }
}
__global__ __launch_bounds__(448, 2) void k_mix(const u16* __restrict__ wsp, const u16* __restrict__ bsp, char* __restrict__ ws) {
  extern __shared__ __attribute__((aligned(16))) char smem[];
  const int tid = threadIdx.x, w = rfl(tid >> 6);
  const int b = blockIdx.x, xcd = b & 7, idx = b >> 3;
  const int tm = (xcd & 1) * 8 + (idx & 7), tn = (xcd >> 1) * 4 + (idx >> 3);
  char* ws_dbg = ws;
  unsigned* cnt1 = (unsigned*)(ws + WS_CNT + tm * 128);
  unsigned* cnt2 = (unsigned*)(ws + WS_CNT + (16 + tn) * 128);
  if (__builtin_expect(w == 4 || w == 5, 1)) {
    const int lane = tid & 63, p = w - 4;
    if (p == 0) STAMPW(3, 4);
    const unsigned ldsb = (unsigned)(size_t)smem;
    const unsigned mA = ldsb + p * 8192, mB = ldsb + 16384 + p * 9216;
    const unsigned sw0 = ((lane & 7) ^ (lane >> 4)) << 4, sw1 = ((lane & 7) ^ (4 + (lane >> 4))) << 4;
    const int g = lane >> 4;
    constexpr int NQ = 17;
    {
      const __amdgpu_buffer_rsrc_t srdA = mksrd((const u16*)(ws + WS_H) + (size_t)tm * 128 * D_, 128 * D_ * 2), srdB = mksrd(ws + WS_WIN, 32 * 9 * WIN_TB);
      const unsigned va0 = (p * 64 + (lane >> 3)) * (D_ * 2) + sw0, va1 = (p * 64 + (lane >> 3)) * (D_ * 2) + sw1, vb = p * 9216 + lane * 16;
      auto issue = [&](int s) {
        const int ph = s >= 9, kt = s - 9 * ph;
        const unsigned so = (s & 3) * MST, sa = mA + so, sb = mB + so;
        const unsigned soA = kt * 128, soB = ((ph ? tn : tn + 16) * 9 + kt) * WIN_TB;
#pragma unroll
        for (int j = 0; j < 8; ++j) { const unsigned v = ((j & 1) ? va1 : va0) + j * 8 * (D_ * 2); DMAQ(sa, j * 1024, v, srdA, soA); }
#pragma unroll
        for (int j = 0; j < 9; ++j) { const unsigned v = vb + j * 1024; DMAQ(sb, j * 1024, v, srdB, soB); }
      };
      issue(0); issue(1); issue(2);
      if (p == 0) STAMPW(3, 5);
      for (int s = 0; s < 15; ++s) { WAITV(2 * NQ); BAR(); issue(s + 3); }
      WAITV(2 * NQ); BAR(); WAITV(NQ); BAR(); WAITV(0); BAR();
    }
    BAR();
    BAR();
    if (p == 0) STAMPW(5, 0);
    {
      const __amdgpu_buffer_rsrc_t srdA = mksrd(wsp + (size_t)tm * 128 * S_, 128 * S_ * 2), srdB = mksrd((const u16*)(ws + WS_Z2N) + (size_t)tn * 144 * S_, 144 * S_ * 2);
      const unsigned va0 = (p * 64 + (lane >> 3)) * (S_ * 2) + sw0, va1 = (p * 64 + (lane >> 3)) * (S_ * 2) + sw1;
      const unsigned vb0 = (p * 72 + (lane >> 3)) * (S_ * 2) + (p ? sw1 : sw0), vb1 = (p * 72 + (lane >> 3)) * (S_ * 2) + (p ? sw0 : sw1);
      auto issueA = [&](int kt) {
        const unsigned sa = mA + (kt & 3) * MST, sk = kt * 128;
#pragma unroll
        for (int j = 0; j < 8; ++j) { const unsigned v = ((j & 1) ? va1 : va0) + j * 8 * (S_ * 2); DMAQ(sa, j * 1024, v, srdA, sk); }
      };
      auto issueB = [&](int kt) {
        const unsigned sb = mB + (kt & 3) * MST, sk = kt * 128;
#pragma unroll
        for (int j = 0; j < 9; ++j) { const unsigned v = ((j & 1) ? vb1 : vb0) + j * 8 * (S_ * 2); DMAQ_SC1(sb, j * 1024, v, srdB, sk); }
      };
      auto issue = [&](int kt) { issueA(kt); issueB(kt); };
      issueA(0); issueA(1); issueA(2);
      if (lane == 0) spin_until(cnt2, 16u);
      asm volatile("" ::: "memory");
      if (p == 0) STAMPW(5, 1);
      if (p == 0) STAMPW(5, 2);
      issueB(0); issueB(1); issueB(2);
      if (p == 0) STAMPW(5, 3);
      WAITV(18); BAR(); issue(3);
      WAITV(9 + NQ); BAR(); issue(4);
      for (int kt = 2; kt < 29; ++kt) { WAITV(2 * NQ); BAR(); issue(kt + 3); }
      WAITV(2 * NQ); BAR(); WAITV(NQ); BAR(); WAITV(0); BAR();
    }
    BAR();
    return;
  }
  if (w == 6) {
    const int lane = tid & 63;
    f32x2* rst = (f32x2*)(smem + M_RS);
#pragma unroll
    for (int i = 0; i < 2; ++i) {
      const f32x4* q = (const f32x4*)(ws + WS_HST + (size_t)(tm * 128 + 64 * i + lane) * 64);
      const f32x4 a = q[0], bb = q[1], c = q[2], d = q[3];
      const float s1 = (a[0] + a[2]) + (bb[0] + bb[2]) + (c[0] + c[2]) + (d[0] + d[2]), s2 = (a[1] + a[3]) + (bb[1] + bb[3]) + (c[1] + c[3]) + (d[1] + d[3]);
      const float mu = s1 * (1.f / D_);
      rst[64 * i + lane] = f32x2{mu, rsqrtf(fmaxf(s2 * (1.f / D_) - mu * mu, 0.f) + 1e-5f)};
    }
    if (lane < 36) {
      *(f32x4*)(smem + M_CSA + lane * 16) = *(const f32x4*)(ws + WS_CS + (size_t)((tn + 16) * 144 + lane * 4) * 4);
      *(f32x4*)(smem + M_CSB + lane * 16) = *(const f32x4*)(ws + WS_CS + (size_t)(tn * 144 + lane * 4) * 4);
    }
    WAITL0();
    for (int s = 0; s < 15; ++s) BAR();
    if (lane == 0) spin_until(cnt1, 16u);
    asm volatile("" ::: "memory");
    const __amdgpu_buffer_rsrc_t srdS = mksrd(ws + WS_ST + (size_t)tm * (16 * 128 * 8), 16 * 128 * 8);
    u32x4 pv[16];
#pragma unroll
    for (int q = 0; q < 16; ++q) pv[q] = __builtin_amdgcn_raw_buffer_load_b128(srdS, (q * 128 + 2 * lane) * 8, 0, 16);
    SB(); BAR(); BAR(); SB();
    f32x4 t4 = {0.f, 0.f, 0.f, 0.f};
#pragma unroll
    for (int q = 0; q < 16; ++q) t4 += __builtin_bit_cast(f32x4, pv[q]);
    {
      const float m0 = t4[0] * (1.f / FH), m1 = t4[2] * (1.f / FH);
      const float r0 = rsqrtf(fmaxf(t4[1] * (1.f / FH) - m0 * m0, 0.f) + 1e-5f), r1 = rsqrtf(fmaxf(t4[3] * (1.f / FH) - m1 * m1, 0.f) + 1e-5f);
      *(f32x4*)(smem + M_S2 + lane * 16) = f32x4{r0, -m0 * r0, r1, -m1 * r1};
    }
    WAITL0();
    BAR();
    BAR();
    return;
  }
  const int lane = tid & 63, g = lane >> 4, fr = lane & 15, f = fr >> 1;
  const int l0 = fr * 128 + ((g ^ f) << 4), l1 = fr * 128 + (((4 + g) ^ f) << 4);
  const int tb = 16384 + (16 * (g >> 1) + 4 * (g & 1) + (fr >> 2)) * 288 + (lane & 3) * 8;
  const int tok0 = tm * 128 + w * 32;
  f32x4 acc[2][9];
  bf16x8 a0[2], b0[9], a1[2], b1[9];
  auto zero_acc = [&]() {
#pragma unroll
    for (int i = 0; i < 2; ++i)
#pragma unroll
      for (int j = 0; j < 9; ++j) acc[i][j] = f32x4{0.f, 0.f, 0.f, 0.f};
  };
  auto mm = [&](bf16x8* a, bf16x8* bb) {
#pragma unroll
    for (int j = 0; j < 9; ++j)
#pragma unroll
      for (int i = 0; i < 2; ++i) acc[i][j] = MFMA16(bb[j], a[i], acc[i][j]);
  };
  auto rdKN = [&](bf16x8* a, bf16x8* bb, int s, int ks) {
    const char* sb = smem + (s & 3) * MST;
#pragma unroll
    for (int i = 0; i < 2; ++i) a[i] = *(const bf16x8*)(sb + w * 4096 + i * 2048 + (ks ? l1 : l0));
#pragma unroll
    for (int j = 0; j < 9; ++j) { const char* q = sb + tb + ks * (32 * 288) + j * 32; bb[j] = cat8(trread(q), trread(q + 8 * 288)); }
  };
  auto rdNK = [&](bf16x8* a, bf16x8* bb, int s, int ks) {
    const char* sb = smem + (s & 3) * MST + (ks ? l1 : l0);
#pragma unroll
    for (int i = 0; i < 2; ++i) a[i] = *(const bf16x8*)(sb + w * 4096 + i * 2048);
#pragma unroll
    for (int j = 0; j < 9; ++j) bb[j] = *(const bf16x8*)(sb + 16384 + j * 2048);
  };
#define SCHED(id, ND, RPG) _Pragma("unroll") for (int q = 0; q < 18; ++q) { __builtin_amdgcn_sched_group_barrier(0x008, 1, id); \
    if (q * RPG < ND) __builtin_amdgcn_sched_group_barrier(0x080, RPG, id); }
  auto gelu = [&](u32x2 (&zp)[2][9], int csoff, f32x2 (&sum)[2]) {
    const float c1 = -2.f * 0.7978845608028654f * 1.4426950408889634f, c2 = c1 * 0.044715f;
    const float* cs = (const float*)(smem + csoff);
    const f32x2* rst = (const f32x2*)(smem + M_RS) + w * 32;
    float nmu[2], k1[2], k3[2], irs[2];
    f32x2 s1[2], s2[2];
#pragma unroll
    for (int i = 0; i < 2; ++i) {
      const f32x2 st = rst[16 * i + fr];
      nmu[i] = -st[0]; k1[i] = c1 * st[1]; k3[i] = c2 * st[1] * st[1] * st[1]; irs[i] = __builtin_amdgcn_rcpf(st[1]);
      s1[i] = f32x2{0.f, 0.f}; s2[i] = f32x2{0.f, 0.f};
    }
    f32x4 c4 = *(const f32x4*)(cs + 4 * g);
#pragma unroll
    for (int j = 0; j < 9; ++j) {
      const f32x4 c4n = *(const f32x4*)(cs + 16 * (j < 8 ? j + 1 : 8) + 4 * g);
      SB();
#pragma unroll
      for (int i = 0; i < 2; ++i)
#pragma unroll
        for (int hh = 0; hh < 2; ++hh) {
          const f32x2 a2 = {acc[i][j][2 * hh], acc[i][j][2 * hh + 1]}, c2v = {c4[2 * hh], c4[2 * hh + 1]};
          const f32x2 t = f32x2{nmu[i], nmu[i]} * c2v + a2;
          const f32x2 y = t * (t * t * f32x2{k3[i], k3[i]} + f32x2{k1[i], k1[i]});
          const f32x2 e = {__builtin_amdgcn_exp2f(y[0]), __builtin_amdgcn_exp2f(y[1])};
          const f32x2 d = e * f32x2{irs[i], irs[i]} + f32x2{irs[i], irs[i]};
          const f32x2 z = t * f32x2{__builtin_amdgcn_rcpf(d[0]), __builtin_amdgcn_rcpf(d[1])};
          s1[i] += z; s2[i] += z * z;
          zp[i][j][hh] = cvtpk(z[0], z[1]);
        }
      VREG(zp[0][j]); VREG(zp[1][j]);
      c4 = c4n;
      SB();
    }
#pragma unroll
    for (int i = 0; i < 2; ++i) sum[i] = f32x2{s1[i][0] + s1[i][1], s2[i][0] + s2[i][1]};
  };
  const __amdgpu_buffer_rsrc_t srdS = mksrd(ws + WS_ST + (size_t)tm * (16 * 128 * 8), 16 * 128 * 8);
  zero_acc();
  STAMP(3, 0);
  BAR();
  STAMP(3, 1);
  rdKN(a0, b0, 0, 0);
  u32x2 z2p[2][9];
  auto stageAB = [&](int s) {
    SB(); rdKN(a1, b1, s, 1); mm(a0, b0); SCHED(0, 20, 2) SB();
    WAITL0(); BAR();
    SB(); rdKN(a0, b0, s + 1, 0); mm(a1, b1); SCHED(1, 20, 2)
  };
  for (int s = 0; s < 9; ++s) stageAB(s);
  SB();
  STAMP(5, 6);
  {
    f32x2 sum[2];
    gelu(z2p, M_CSA, sum);
#pragma unroll
    for (int i = 0; i < 2; ++i) {
      f32x2 v = sum[i];
      v[0] += xor16(v[0]); v[0] += xor32(v[0]); v[1] += xor16(v[1]); v[1] += xor32(v[1]);
      if (g == 0) __builtin_amdgcn_raw_buffer_store_b64(__builtin_bit_cast(u32x2, v), srdS, (tn * 128 + w * 32 + 16 * i + fr) * 8, 0, 16);
    }
    asm volatile("s_waitcnt vmcnt(0)" ::: "memory");
    zero_acc();
  }
  SB();
  STAMP(5, 4);
  stageAB(9);
  if (w == 0 && lane == 0) __hip_atomic_fetch_add(cnt1, 1u, __ATOMIC_RELAXED, __HIP_MEMORY_SCOPE_AGENT);
  for (int s = 10; s < 17; ++s) stageAB(s);
  SB(); rdKN(a1, b1, 17, 1); mm(a0, b0); mm(a1, b1); SB();
  WAITL0(); BAR();
  STAMP(3, 2);
  {
    const f32x2* st2 = (const f32x2*)(smem + M_S2) + w * 32;
    char* reg = smem + w * (32 * 304);
#pragma unroll
    for (int i = 0; i < 2; ++i) {
      const f32x2 st = st2[16 * i + fr];
#pragma unroll
      for (int j = 0; j < 9; ++j) {
        u32x2 v;
        v[0] = cvtpk(fmaf(bflo(z2p[i][j][0]), st[0], st[1]), fmaf(bfhi(z2p[i][j][0]), st[0], st[1]));
        v[1] = cvtpk(fmaf(bflo(z2p[i][j][1]), st[0], st[1]), fmaf(bfhi(z2p[i][j][1]), st[0], st[1]));
        *(u32x2*)(reg + (16 * i + fr) * 304 + (16 * j + 4 * g) * 2) = v;
      }
    }
    WAITL0();
    const __amdgpu_buffer_rsrc_t so = mksrd((u16*)(ws + WS_Z2N) + (size_t)(tn * 144) * S_ + tok0, 144 * S_ * 2);
    const char* rp = reg + (8 * g + (fr >> 2)) * 304 + (lane & 3) * 8;
#pragma unroll
    for (int j = 0; j < 9; ++j)
      __builtin_amdgcn_raw_buffer_store_b128(__builtin_bit_cast(u32x4, cat8(trread(rp + j * 32), trread(rp + 4 * 304 + j * 32))), so, (j * 16 + fr) * (S_ * 2) + 8 * g * 2, 0, 16);
    asm volatile("s_waitcnt vmcnt(0)" ::: "memory");
    WAITL0(); BAR();
    if (w == 0 && lane == 0) __hip_atomic_fetch_add(cnt2, 1u, __ATOMIC_RELAXED, __HIP_MEMORY_SCOPE_AGENT);
  }
  u32x2 z1p[2][9];
  { f32x2 sum[2]; gelu(z1p, M_CSB, sum); }
  const unsigned bias0 = bsp[tok0 + fr], bias1 = bsp[tok0 + 16 + fr];
  zero_acc();
  STAMP(4, 0);
  BAR();
  STAMP(4, 1);
  rdNK(a0, b0, 0, 0);
  for (int kt = 0; kt < 31; ++kt) {
    SB(); rdNK(a1, b1, kt, 1); mm(a0, b0); SCHED(0, 11, 1) SB();
    WAITL0(); BAR();
    SB(); rdNK(a0, b0, kt + 1, 0); mm(a1, b1); SCHED(1, 11, 1)
  }
  SB(); rdNK(a1, b1, 31, 1); mm(a0, b0); mm(a1, b1); SB();
  WAITL0(); BAR();
  SB();
  STAMP(4, 2);
#undef SCHED
  char* reg = smem + w * (32 * 304);
#pragma unroll
  for (int i = 0; i < 2; ++i) {
    const float bi = bflo(i ? bias1 : bias0);
#pragma unroll
    for (int j = 0; j < 9; ++j) {
      u32x2 v;
      v[0] = cvtpk(bflo(z1p[i][j][0]) * (acc[i][j][0] + bi), bfhi(z1p[i][j][0]) * (acc[i][j][1] + bi));
      v[1] = cvtpk(bflo(z1p[i][j][1]) * (acc[i][j][2] + bi), bfhi(z1p[i][j][1]) * (acc[i][j][3] + bi));
      *(u32x2*)(reg + (16 * i + fr) * 304 + (16 * j + 4 * g) * 2) = v;
    }
  }
  WAITL0();
  const __amdgpu_buffer_rsrc_t so = mksrd((u16*)(ws + WS_U) + (size_t)tok0 * FH + tn * 144, 32 * FH * 2);
#pragma unroll
  for (int i = 0; i < 9; ++i) {
    const int c = i * 64 + lane, row = c / 18, ch = c - row * 18;
    st16(so, row * (FH * 2) + ch * 16, *(const u32x4*)(reg + row * 304 + ch * 16));
  }
}

extern "C" void kernel_launch(void* const* d_in, const int* in_sizes, int n_in, void* d_out, int out_size, void* d_ws, size_t ws_size, hipStream_t stream) {
  const u16* x = (const u16*)d_in[0];
  const u16* wqkv = (const u16*)d_in[1];
  const u16* wo = (const u16*)d_in[2];
  const u16* win = (const u16*)d_in[4];
  const u16* wsp = (const u16*)d_in[5];
  const u16* bsp = (const u16*)d_in[6];
  const u16* wout = (const u16*)d_in[7];
  char* ws = (char*)d_ws;
  (void)hipFuncSetAttribute((const void*)k_qkv, hipFuncAttributeMaxDynamicSharedMemorySize, C1::LDS);
  (void)hipFuncSetAttribute((const void*)k_attn, hipFuncAttributeMaxDynamicSharedMemorySize, ATT_LDS);
  (void)hipFuncSetAttribute((const void*)k_res<9>, hipFuncAttributeMaxDynamicSharedMemorySize, CR<9>::LDS);
  (void)hipFuncSetAttribute((const void*)k_res<36>, hipFuncAttributeMaxDynamicSharedMemorySize, CR<36>::LDS);
  (void)hipFuncSetAttribute((const void*)k_mix, hipFuncAttributeMaxDynamicSharedMemorySize, M_LDS);
  hipLaunchKernelGGL(k_qkv, dim3(256), dim3(512), C1::LDS, stream, x, wqkv, win, wout, wo, ws);
  hipLaunchKernelGGL(k_attn, dim3(256), dim3(512), ATT_LDS, stream, ws, wsp);
  hipLaunchKernelGGL(k_res<9>, dim3(256), dim3(384), CR<9>::LDS, stream, (const u16*)(ws + WS_AO), (const char*)(ws + WS_WO), x, (u16*)(ws + WS_H), ws);
  hipLaunchKernelGGL(k_mix, dim3(256), dim3(448), M_LDS, stream, wsp, bsp, ws);
  hipLaunchKernelGGL(k_res<36>, dim3(256), dim3(384), CR<36>::LDS, stream, (const u16*)(ws + WS_U), (const char*)(ws + WS_WOUT), (const u16*)(ws + WS_H), (u16*)d_out, ws);
}
```

```cpp
#include <hip/hip_runtime.h>
#include <stdint.h>

typedef unsigned short u16;
typedef short bf16x8 __attribute__((ext_vector_type(8)));
typedef short s16x4 __attribute__((ext_vector_type(4)));
typedef float f32x2 __attribute__((ext_vector_type(2)));
typedef float f32x4 __attribute__((ext_vector_type(4)));
typedef float f32x16 __attribute__((ext_vector_type(16)));
typedef unsigned u32x2 __attribute__((ext_vector_type(2)));
typedef unsigned u32x4 __attribute__((ext_vector_type(4)));
typedef __bf16 bf16x2_t __attribute__((ext_vector_type(2)));
typedef __attribute__((address_space(3))) s16x4* lds_s16x4_ptr;
#define DI __device__ __forceinline__

constexpr int S_ = 2048, D_ = 576, H_ = 32, DH = 18, FH = 2304, F2 = 4608;
constexpr int QK_TILE = 1152;
constexpr int V_OFF = 1152;
constexpr int KV_TILE = 1152 + 4 * 304;
constexpr int KV_HEAD = 64 * KV_TILE;
constexpr int Q_HEAD = 64 * QK_TILE;
constexpr int WIN_TB = 64 * 288, WR_TB = 64 * 160;
constexpr size_t WS_Q = 0;
constexpr size_t WS_KV = WS_Q + (size_t)H_ * Q_HEAD;
constexpr size_t WS_AO = WS_KV + (size_t)H_ * KV_HEAD + 8192;
constexpr size_t WS_H = WS_AO + (size_t)S_ * D_ * 2;
constexpr size_t WS_Z1 = WS_H + (size_t)S_ * D_ * 2;
constexpr size_t WS_Z2T = WS_Z1 + (size_t)S_ * FH * 2;
constexpr size_t WS_Z2N = WS_Z2T + (size_t)S_ * FH * 2;
constexpr size_t WS_ST = WS_Z2N + (size_t)S_ * FH * 2;
constexpr size_t WS_CNT = WS_ST + (size_t)S_ * 16 * 2 * 4;
constexpr size_t WS_HST = WS_ST + (size_t)S_ * 32 * 2 * 4;
constexpr size_t WS_U = WS_HST + (size_t)S_ * 8 * 2 * 4;
constexpr size_t WS_WIN = WS_U + (size_t)S_ * FH * 2;
constexpr size_t WS_WOUT = WS_WIN + (size_t)32 * 9 * WIN_TB;
constexpr size_t WS_WO = WS_WOUT + (size_t)8 * 36 * WR_TB;
constexpr size_t WS_CS = WS_WO + (size_t)8 * 9 * WR_TB;
constexpr size_t WS_END = WS_CS + (size_t)F2 * 4;

DI unsigned cvtpk(float lo, float hi) { f32x2 v = {lo, hi}; bf16x2_t b = __builtin_convertvector(v, bf16x2_t); return __builtin_bit_cast(unsigned, b); }
DI float bflo(unsigned u) { return __uint_as_float(u << 16); }
DI float bfhi(unsigned u) { return __uint_as_float(u & 0xffff0000u); }
DI unsigned rfl(unsigned x) { return __builtin_amdgcn_readfirstlane(x); }
DI float xor16(float v) { return __shfl_xor(v, 16); }
DI float xor32(float v) { return __shfl_xor(v, 32); }
#define DMAQ(sb, imm, voff, srd, soff) asm volatile("s_add_u32 m0, %0, %1\n\ts_nop 0\n\tbuffer_load_dwordx4 %2, %3, %4 offen lds" :: "s"(sb), "i"(imm), "v"(voff), "s"(srd), "s"(soff) : "scc")
#define DMAQ_SC1(sb, imm, voff, srd, soff) asm volatile("s_add_u32 m0, %0, %1\n\ts_nop 0\n\tbuffer_load_dwordx4 %2, %3, %4 offen sc1 lds" :: "s"(sb), "i"(imm), "v"(voff), "s"(srd), "s"(soff) : "scc")
#define WAITV(n) asm volatile("s_waitcnt vmcnt(%0)" :: "i"(n) : "memory")
#define WAITL0() asm volatile("s_waitcnt lgkmcnt(0)" ::: "memory")
#define BAR() asm volatile("s_barrier" ::: "memory")
#define SB() __builtin_amdgcn_sched_barrier(0)
#define MFMA16(a, b, c) __builtin_amdgcn_mfma_f32_16x16x32_bf16((a), (b), (c), 0, 0, 0)
#define MFMA32(a, b, c) __builtin_amdgcn_mfma_f32_32x32x16_bf16((a), (b), (c), 0, 0, 0)
#define VREG(x) asm volatile("" : "+v"(x))
DI __amdgpu_buffer_rsrc_t mksrd(const void* p, unsigned bytes) { return __builtin_amdgcn_make_buffer_rsrc((void*)p, (short)0, (int)bytes, 0x00020000); }
DI s16x4 trread(const char* p) { return __builtin_amdgcn_ds_read_tr16_b64_v4i16((lds_s16x4_ptr)(p)); }
DI bf16x8 cat8(s16x4 lo, s16x4 hi) { return __builtin_shufflevector(lo, hi, 0, 1, 2, 3, 4, 5, 6, 7); }
DI bf16x8 ones8() { const u32x4 o = {0x3F803F80u, 0x3F803F80u, 0x3F803F80u, 0x3F803F80u}; return __builtin_bit_cast(bf16x8, o); }
#define ROT1 (3 * (((idx >> 2) + (idx & 3)) % 3))
#define ST_AUX 16
DI void st16(__amdgpu_buffer_rsrc_t srd, unsigned off, u32x4 v) { __builtin_amdgcn_raw_buffer_store_b128(v, srd, off, 0, ST_AUX); }
#define TOUCH(dummy, ptr) asm volatile("global_load_dword %0, %1, off" : "+v"(dummy) : "v"(ptr))
#define STAMP(kid, slot)
#define STAMPW(kid, slot)
#define STAMP_END(kid)

enum { B_NK = 0, B_KN = 1, B_TILED = 2 };
template <int BM_, int NT_, int NP_, int NST_, int BMODE_, int KT_, bool COLSUM_>
struct Cfg {
  static constexpr int BM = BM_, NT = NT_, NP = NP_, NST = NST_, KT = KT_, BMODE = BMODE_;
  static constexpr bool BKN = BMODE_ != B_NK, COLSUM = COLSUM_;
  static constexpr int BN = NT * 16;
  static constexpr int NA = BM / 8, NB = BN / 8, NI = NA + NB;
  static constexpr int ABYTES = BM * 128, BBYTES = BN * 128, STAGE = ABYTES + BBYTES;
  static constexpr int RING = NST * STAGE;
  static constexpr int CS_OFF = RING;
  static constexpr int RS_OFF = RING + BN * 4;
  static constexpr int LDS = RS_OFF + BM * 8;
};

template <class C, int TAILN = 0, int TCPR = 1>
DI void run_producer(char* smem, const void* Ab, unsigned abytes, const void* Bb, unsigned bbytes, int lda, int ldb, int p, int lane, char* ws_dbg, int kid,
                     int rot = 0, const void* Tb = nullptr, unsigned tbytes = 0, int ldt = 0) {
  if (p == 0) STAMPW(kid, 4);
  constexpr int HA = C::NA / 2, HB = C::NB / 2, NQ = HA + HB;
  static_assert(C::NA % 4 == 0 && C::NB % 2 == 0, "piece split");
  const __amdgpu_buffer_rsrc_t srdA = mksrd(Ab, abytes), srdB = mksrd(Bb, bbytes);
  const unsigned ldsb = (unsigned)(size_t)smem;
  const unsigned mA = ldsb + p * (HA * 1024), mB = ldsb + C::ABYTES + p * (HB * 1024);
  const unsigned sw0 = ((lane & 7) ^ (lane >> 4)) << 4, sw1 = ((lane & 7) ^ (4 + (lane >> 4))) << 4;
  const unsigned va0 = (p * HA * 8 + (lane >> 3)) * (lda * 2) + sw0, va1 = (p * HA * 8 + (lane >> 3)) * (lda * 2) + sw1;
  unsigned vb[HB];
#pragma unroll
  for (int j = 0; j < HB; ++j) {
    if (C::BMODE == B_NK) {
      const int q = p * HB + j;
      vb[j] = (8 * q + (lane >> 3)) * (ldb * 2) + ((q & 1) ? sw1 : sw0);
    } else if (C::BMODE == B_KN) {
      constexpr int CPR = C::BN / 8;
      const int c = 64 * (p * HB + j) + lane, rho = c / CPR, ch = c - rho * CPR;
      const int k = (rho & ~12) | ((rho & 4) << 1) | ((rho & 8) >> 1);
      vb[j] = k * (ldb * 2) + ch * 16;
    } else {
      vb[j] = (p * HB + j) * 1024 + lane * 16;
    }
  }
  auto issue = [&](int kt) {
    const unsigned so = (kt % C::NST) * C::STAGE, sa = mA + so, sb = mB + so;
    int kr = kt + rot; if (kr >= C::KT) kr -= C::KT;
    const unsigned soA = kr * 128, soB = C::BMODE == B_NK ? kr * 128 : (C::BMODE == B_KN ? kr * 64 * ldb * 2 : kr * C::BBYTES);
#pragma unroll
    for (int j = 0; j < HA; ++j) { const unsigned v = ((j & 1) ? va1 : va0) + j * 8 * (lda * 2); DMAQ(sa, j * 1024, v, srdA, soA); }
#pragma unroll
    for (int j = 0; j < HB; ++j) DMAQ(sb, j * 1024, vb[j], srdB, soB);
  };
  constexpr int PF = C::NST - 1;
#pragma unroll
  for (int s = 0; s < PF; ++s) issue(s);
  if (p == 0) STAMPW(kid, 5);
  constexpr int NJ = C::COLSUM ? (C::NT + 1) / 2 : 0;
  f32x4 cs[NJ > 0 ? NJ : 1];
#pragma unroll
  for (int j = 0; j < NJ; ++j) cs[j] = f32x4{0.f, 0.f, 0.f, 0.f};
  const int g = lane >> 4;
  const int tb = C::ABYTES + (16 * (g >> 1) + 4 * (g & 1) + ((lane & 15) >> 2)) * (C::BN * 2) + (lane & 3) * 8 + p * 32;
  auto colsum = [&](int kt) {
    if constexpr (C::COLSUM) {
      const char* sb = smem + (kt % C::NST) * C::STAGE + tb;
#pragma unroll
      for (int jj = 0; jj < NJ; ++jj) {
#pragma unroll
        for (int ks = 0; ks < 2; ++ks) {
          const char* q = sb + ks * (32 * C::BN * 2) + jj * 64;
          cs[jj] = MFMA16(cat8(trread(q), trread(q + 8 * C::BN * 2)), ones8(), cs[jj]);
        }
      }
      WAITL0();
    }
  };
  for (int kt = 0; kt < C::KT - PF; ++kt) { WAITV((PF - 1) * NQ); BAR(); issue(kt + PF); colsum(kt); }
  constexpr int NTL = TAILN / 4;
  auto tail = [&](int half) {
    const unsigned sb = ldsb + ((C::KT + half) % C::NST) * C::STAGE + p * (NTL * 1024);
#pragma unroll
    for (int j = 0; j < NTL; ++j) {
      const int c = 64 * (half * 2 * NTL + p * NTL + j) + lane, row = c / TCPR, ch = c - row * TCPR;
      const unsigned vo2 = row * ldt + ch * 16;
      DMAQ(sb, j * 1024, vo2, mksrd(Tb, tbytes), 0u);
    }
  };
#pragma unroll
  for (int r = PF - 1; r >= 0; --r) {
    WAITV(r * NQ + (TAILN > 0 ? (r < PF - 1 ? NTL : 0) + (r < PF - 2 ? NTL : 0) : 0)); BAR();
    if (TAILN > 0 && r == PF - 1) tail(0);
    if (TAILN > 0 && r == PF - 2) tail(1);
    colsum(C::KT - 1 - r);
  }
  if (TAILN > 0) WAITV(0);
  if constexpr (C::COLSUM) {
    float* st = (float*)(smem + C::CS_OFF);
#pragma unroll
    for (int jj = 0; jj < NJ; ++jj) {
      if ((lane & 15) == 0 && p + 2 * jj < C::NT) *(f32x4*)(st + 16 * (p + 2 * jj) + 4 * g) = cs[jj];
    }
    WAITL0();
  }
  BAR();
}

struct NoHook { DI void operator()() const {} };
template <class C, int MI, int NTW, int J0, bool ROLL, bool ROWSTAT, class Hook>
DI void consumer(char* smem, int row0, int lane, f32x4 (&acc)[MI][NTW], float (&rs)[MI], float (&rq)[MI], char* ws_dbg, int kid, int hook_kt, Hook hook) {
  const int g = lane >> 4, fr = lane & 15, f = fr >> 1;
  const int la0 = row0 * 128 + fr * 128 + ((g ^ f) << 4), la1 = row0 * 128 + fr * 128 + (((4 + g) ^ f) << 4);
  const int lb0 = C::ABYTES + J0 * 2048 + fr * 128 + ((g ^ f) << 4), lb1 = C::ABYTES + J0 * 2048 + fr * 128 + (((4 + g) ^ f) << 4);
  const int tb = C::ABYTES + J0 * 32 + (16 * (g >> 1) + 4 * (g & 1) + (fr >> 2)) * (C::BN * 2) + (lane & 3) * 8;
  f32x4 ars[MI], arq[MI];
#pragma unroll
  for (int i = 0; i < MI; ++i) {
    ars[i] = f32x4{0.f, 0.f, 0.f, 0.f}; arq[i] = f32x4{0.f, 0.f, 0.f, 0.f};
#pragma unroll
    for (int j = 0; j < NTW; ++j) acc[i][j] = f32x4{0.f, 0.f, 0.f, 0.f};
  }
  bf16x8 a0[MI], b0[NTW], a1[MI], b1[ROLL ? 1 : NTW];
  auto rda = [&](bf16x8* a, int kt, int ks) {
    const char* sb = smem + (kt % C::NST) * C::STAGE + (ks ? la1 : la0);
#pragma unroll
    for (int i = 0; i < MI; ++i) a[i] = *(const bf16x8*)(sb + i * 2048);
  };
  auto rdb1 = [&](int j, int kt, int ks) -> bf16x8 {
    const char* sb = smem + (kt % C::NST) * C::STAGE;
    if constexpr (C::BKN) {
      const char* p = sb + tb + ks * (32 * C::BN * 2) + j * 32;
      return cat8(trread(p), trread(p + 8 * C::BN * 2));
    } else {
      return *(const bf16x8*)(sb + (ks ? lb1 : lb0) + j * 2048);
    }
  };
  auto stats = [&](bf16x8* a) {
    if constexpr (ROWSTAT) {
#pragma unroll
      for (int i = 0; i < MI; ++i) { ars[i] = MFMA16(ones8(), a[i], ars[i]); arq[i] = MFMA16(a[i], a[i], arq[i]); }
    }
  };
  STAMP(kid, 0);
  BAR();
  STAMP(kid, 1);
  if constexpr (!ROLL) {
    constexpr int NM = MI * NTW + (ROWSTAT ? 2 * MI : 0), ND = MI + NTW * (C::BKN ? 2 : 1), RPG = (ND + (NM * 2 / 3) - 1) / (NM * 2 / 3);
#define SCHED(id) _Pragma("unroll") for (int q = 0; q < NM; ++q) { __builtin_amdgcn_sched_group_barrier(0x008, 1, id); \
    if (q * RPG < ND) __builtin_amdgcn_sched_group_barrier(0x080, RPG, id); }
    auto rd = [&](bf16x8* a, bf16x8* bb, int kt, int ks) {
      rda(a, kt, ks);
#pragma unroll
      for (int j = 0; j < NTW; ++j) bb[j] = rdb1(j, kt, ks);
    };
    auto mm = [&](bf16x8* a, bf16x8* bb) {
#pragma unroll
      for (int j = 0; j < NTW; ++j)
#pragma unroll
        for (int i = 0; i < MI; ++i) acc[i][j] = MFMA16(bb[j], a[i], acc[i][j]);
      stats(a);
    };
    auto body = [&](int kt) {
      SB();
      rd(a1, b1, kt, 1);
      mm(a0, b0);
      SCHED(0)
      SB();
      WAITL0(); BAR();
      SB();
      rd(a0, b0, kt + 1, 0);
      mm(a1, b1);
      SCHED(1)
    };
    rd(a0, b0, 0, 0);
    if (hook_kt > 0) {
      for (int kt = 0; kt < hook_kt; ++kt) body(kt);
      SB(); hook(); SB();
      for (int kt = hook_kt; kt < C::KT - 1; ++kt) body(kt);
    } else {
      for (int kt = 0; kt < C::KT - 1; ++kt) body(kt);
    }
    SB();
    rd(a1, b1, C::KT - 1, 1);
    mm(a0, b0);
    mm(a1, b1);
#undef SCHED
  } else {
    auto sub = [&](bf16x8* a, int kt, int ks, bool more) {
#pragma unroll
      for (int j = 0; j < NTW; ++j) {
#pragma unroll
        for (int i = 0; i < MI; ++i) acc[i][j] = MFMA16(b0[j], a[i], acc[i][j]);
        if (more) b0[j] = rdb1(j, kt, ks);
        SB();
      }
      stats(a);
    };
    rda(a0, 0, 0);
#pragma unroll
    for (int j = 0; j < NTW; ++j) b0[j] = rdb1(j, 0, 0);
    for (int kt = 0; kt < C::KT - 1; ++kt) {
      rda(a1, kt, 1);
      sub(a0, kt, 1, true);
      WAITL0(); BAR();
      rda(a0, kt + 1, 0);
      sub(a1, kt + 1, 0, true);
    }
    rda(a1, C::KT - 1, 1);
    sub(a0, C::KT - 1, 1, true);
    sub(a1, 0, 0, false);
  }
  if constexpr (ROWSTAT) {
#pragma unroll
    for (int i = 0; i < MI; ++i) {
      rs[i] = ars[i][0];
      const float d = (fr & 2) ? ((fr & 1) ? arq[i][3] : arq[i][2]) : ((fr & 1) ? arq[i][1] : arq[i][0]);
      rq[i] = __shfl(d, fr + 16 * (fr >> 2));
    }
  }
  SB();
  WAITL0(); BAR();
  SB();
  STAMP(kid, 2);
}

using C1 = Cfg<128, 9, 4, 4, B_KN, 9, false>;
constexpr int RU1 = 32 * 9 * 18, RU2 = RU1 + 8 * 36 * 10, RU3 = RU2 + 8 * 9 * 10;
DI const u16* retile_src(const u16* __restrict__ win, const u16* __restrict__ wout, const u16* __restrict__ wo, int u, int lane) {
  if (u < RU1) {
    const int tile = u / 18, q = u - tile * 18, tn = tile / 9, kt = tile - tn * 9;
    const int c = 64 * q + lane, rho = c / 18, ch = c - rho * 18;
    const int k = (rho & ~12) | ((rho & 4) << 1) | ((rho & 8) >> 1);
    return win + (size_t)(kt * 64 + k) * F2 + tn * 144 + ch * 8;
  }
  const bool o = u < RU2; const int v = u - (o ? RU1 : RU2), KT = o ? 36 : 9;
  const int tile = v / 10, q = v - tile * 10, tn = tile / KT, kt = tile - tn * KT;
  const int c = 64 * q + lane, rho = c / 10, ch = c - rho * 10;
  const int k = (rho & ~12) | ((rho & 4) << 1) | ((rho & 8) >> 1);
  int col = tn * 72 + ch * 8; if (col > D_ - 8) col = D_ - 8;
  int kr = kt * 64 + k;
  if (!o) kr = kr < 512 ? 18 * (kr >> 4) + (kr & 15) : 18 * ((kr - 512) >> 1) + 16 + (kr & 1);
  return (o ? wout : wo) + (size_t)kr * D_ + col;
}
__global__ __launch_bounds__(512, 2) void k_qkv(const u16* __restrict__ x, const u16* __restrict__ wqkv, const u16* __restrict__ win, const u16* __restrict__ wout,
                                                const u16* __restrict__ wo, char* __restrict__ ws) {
  extern __shared__ __attribute__((aligned(16))) char smem[];
  const int tid = threadIdx.x, w = rfl(tid >> 6);
  const int b = blockIdx.x, xcd = b & 7, idx = b >> 3;
  char* ws_dbg = ws;
  if (b == 255 && tid < 40) *(unsigned*)(ws + WS_CNT + tid * 128) = 0u;
  if (idx >= 24) {
    STAMP(0, 0); STAMP(0, 1); STAMP(0, 2);
    const int pw = (idx - 24) * 8 + xcd, lane = tid & 63, gw = pw * 8 + w;
    constexpr int NU = (RU3 + 511) / 512;
    u32x4 v[NU];
#pragma unroll
    for (int i = 0; i < NU; ++i) { const int u = gw + 512 * i; v[i] = *(const u32x4*)retile_src(win, wout, wo, u < RU3 ? u : gw, lane); }
    const int cc = tid % 9, rg = tid / 9;
    u32x4 cv[11];
    if (rg < 56) {
#pragma unroll
      for (int i = 0; i < 11; ++i) { const int r = rg + 56 * i; cv[i] = *(const u32x4*)(win + (size_t)(r < D_ ? r : rg) * F2 + pw * 72 + cc * 8); }
    }
    const __amdgpu_buffer_rsrc_t so = mksrd(ws + WS_WIN, RU3 * 1024);
#pragma unroll
    for (int i = 0; i < NU; ++i) { const int u = gw + 512 * i; if (u < RU3) st16(so, u * 1024 + lane * 16, v[i]); }
    float* red = (float*)smem;
    if (rg < 56) {
      f32x4 sa = {0.f, 0.f, 0.f, 0.f}, sb = {0.f, 0.f, 0.f, 0.f};
#pragma unroll
      for (int i = 0; i < 11; ++i) if (rg + 56 * i < D_) {
        sa += f32x4{bflo(cv[i][0]), bfhi(cv[i][0]), bflo(cv[i][1]), bfhi(cv[i][1])};
        sb += f32x4{bflo(cv[i][2]), bfhi(cv[i][2]), bflo(cv[i][3]), bfhi(cv[i][3])};
      }
      *(f32x4*)(red + rg * 72 + cc * 8) = sa; *(f32x4*)(red + rg * 72 + cc * 8 + 4) = sb;
    }
    __syncthreads();
    if (tid < 288) {
      const int c = tid % 72, q = tid / 72;
      float t = 0.f;
#pragma unroll
      for (int r = 0; r < 14; ++r) t += red[(q * 14 + r) * 72 + c];
      red[56 * 72 + tid] = t;
    }
    __syncthreads();
    if (tid < 72) ((float*)(ws + WS_CS))[pw * 72 + tid] = (red[56 * 72 + tid] + red[56 * 72 + 72 + tid]) + (red[56 * 72 + 144 + tid] + red[56 * 72 + 216 + tid]);
    STAMP_END(0);
    return;
  }
  const int tm = (xcd & 3) * 4 + (idx & 3), tn = (xcd >> 2) * 6 + (idx >> 2);
  if (__builtin_expect(w == 4 || w == 5, 1)) {
    run_producer<C1>(smem, x + (size_t)tm * 128 * D_, 128 * D_ * 2, wqkv + tn * 144, (unsigned)((D_ * 3 * D_ - tn * 144) * 2), D_, 3 * D_, w - 4, tid & 63, ws, 0, ROT1);
    return;
  }
  if (w >= 6) {
    const int lane = tid & 63, p = w - 6, g = lane >> 4;
    f32x4 cs[5];
#pragma unroll
    for (int j = 0; j < 5; ++j) cs[j] = f32x4{0.f, 0.f, 0.f, 0.f};
    const int tb = C1::ABYTES + (16 * (g >> 1) + 4 * (g & 1) + ((lane & 15) >> 2)) * 288 + (lane & 3) * 8 + p * 32;
    for (int kt = 0; kt < 9; ++kt) {
      BAR();
      const char* sb = smem + (kt % C1::NST) * C1::STAGE + tb;
#pragma unroll
      for (int jj = 0; jj < 5; ++jj)
#pragma unroll
        for (int ks = 0; ks < 2; ++ks) {
          const char* q = sb + ks * (32 * 288) + jj * 64;
          cs[jj] = MFMA16(cat8(trread(q), trread(q + 8 * 288)), ones8(), cs[jj]);
        }
      WAITL0();
    }
    float* st = (float*)(smem + C1::CS_OFF);
#pragma unroll
    for (int jj = 0; jj < 5; ++jj) if ((lane & 15) == 0 && p + 2 * jj < 9) *(f32x4*)(st + 16 * (p + 2 * jj) + 4 * g) = cs[jj];
    WAITL0(); BAR();
    return;
  }
  f32x4 acc[2][9]; float rs[2], rq[2];
  consumer<C1, 2, 9, 0, false, true>(smem, w * 32, tid & 63, acc, rs, rq, ws, 0, 0, NoHook());
  int tz = threadIdx.x; VREG(tz);
  const int lane = tz & 63, g = lane >> 4, fr = lane & 15;
  const float* cs = (const float*)(smem + C1::CS_OFF);
  const int kind = tn >> 2, head0 = (tn & 3) * 8;
  const int tile32 = tm * 4 + w;
  const float sc = kind == 0 ? 0.23570226039551584f * 1.4426950408889634f : 1.f;
  float mu[2], rstd[2];
#pragma unroll
  for (int i = 0; i < 2; ++i) {
    mu[i] = rs[i] * (1.f / D_);
    rstd[i] = rsqrtf(fmaxf(rq[i] * (1.f / D_) - mu[i] * mu[i], 0.f) + 1e-5f) * sc;
  }
  constexpr int RP = 336;
  char* reg = smem + w * (32 * RP);
  f32x4 c4a[9];
#pragma unroll
  for (int j = 0; j < 9; ++j) c4a[j] = *(const f32x4*)(cs + 16 * j + 4 * g);
#pragma unroll
  for (int j = 0; j < 9; ++j) {
    const f32x4 c4 = c4a[j];
#pragma unroll
    for (int rr = 0; rr < 2; ++rr) {
      const int c = 16 * j + 4 * g + 2 * rr, off = c * 2 + (c / 18) * 4;
#pragma unroll
      for (int i = 0; i < 2; ++i)
        *(unsigned*)(reg + (16 * i + fr) * RP + off) = cvtpk((acc[i][j][2 * rr] - mu[i] * c4[2 * rr]) * rstd[i], (acc[i][j][2 * rr + 1] - mu[i] * c4[2 * rr + 1]) * rstd[i]);
    }
  }
  WAITL0();
  if (kind < 2) {
    char* base = ws + (kind == 0 ? WS_Q + (size_t)tile32 * QK_TILE + (size_t)head0 * Q_HEAD : WS_KV + (size_t)tile32 * KV_TILE + (size_t)head0 * KV_HEAD);
    const int hstride = kind == 0 ? Q_HEAD : KV_HEAD;
    const char* rp = reg + (lane & 31) * RP + (lane >> 5) * 16;
#pragma unroll
    for (int hl = 0; hl < 8; ++hl) {
      const u32x2 lo = *(const u32x2*)(rp + hl * 40), hi = *(const u32x2*)(rp + hl * 40 + 8);
      *(u32x4*)(base + (size_t)hl * hstride + lane * 16) = u32x4{lo[0], lo[1], hi[0], hi[1]};
    }
#pragma unroll
    for (int q = 0; q < 4; ++q) {
      const int hl = 2 * q + (lane >> 5);
      *(unsigned*)(base + (size_t)hl * hstride + 1024 + (lane & 31) * 4) = *(const unsigned*)(reg + (lane & 31) * RP + hl * 40 + 32);
    }
  } else {
    char* base = ws + WS_KV + (size_t)tile32 * KV_TILE + V_OFF + (size_t)head0 * KV_HEAD;
    const int s = g >> 1, h = g & 1;
    const char* rp = reg + (16 * s + 4 * h + (fr >> 2)) * RP + (lane & 3) * 8;
#pragma unroll
    for (int j = 0; j < 10; ++j) {
      const bf16x8 v = cat8(trread(rp + j * 32), trread(rp + 8 * RP + j * 32));
      const int c = 16 * j + fr, hl = c / 20, d = c - hl * 20;
      if (d < 18) *(bf16x8*)(base + (size_t)hl * KV_HEAD + s * 608 + h * 304 + d * 16) = v;
    }
    if (lane < 32) {
      *(bf16x8*)(base + (size_t)(lane >> 2) * KV_HEAD + (lane & 3) * 304 + 18 * 16) = ones8();
    }
  }
  STAMP_END(0);
}

DI void spin_until(const unsigned* cnt, unsigned target) {
  for (unsigned spins = 0; spins < (1u << 24); ++spins) {
    if (__hip_atomic_load(cnt, __ATOMIC_RELAXED, __HIP_MEMORY_SCOPE_AGENT) >= target) break;
    __builtin_amdgcn_s_sleep(1);
  }
}
template <int KT> using CR = Cfg<64, 5, 4, 8, B_TILED, KT, false>;
constexpr int ATT_LDS = 152 * 1024;
__global__ __launch_bounds__(512, 2) void k_attn(char* __restrict__ ws, const u16* __restrict__ wsp, const u16* __restrict__ x) {
  extern __shared__ __attribute__((aligned(16))) char smem[];
  const int tid = threadIdx.x, lane = tid & 63, w = rfl(tid >> 6);
  const int b = blockIdx.x, xcd = b & 7, idx = b >> 3;
  const int head = xcd * 4 + (idx & 3), qb = idx >> 2;
  char* ws_dbg = ws; STAMP(1, 0);
  const int q31 = lane & 31, h = lane >> 5;
  const char* qp = ws + WS_Q + (size_t)head * Q_HEAD + (size_t)(qb * 8 + w) * QK_TILE;
  bf16x8 qf1; unsigned q2;
  asm volatile("global_load_dwordx4 %0, %1, off" : "=v"(qf1) : "v"(qp + lane * 16));
  asm volatile("global_load_dword %0, %1, off" : "=v"(q2) : "v"(qp + 1024 + q31 * 4));
  {
    __amdgpu_buffer_rsrc_t srd = mksrd(ws + WS_KV + (size_t)head * KV_HEAD, KV_HEAD);
    const unsigned ldsb = (unsigned)(size_t)smem + w * 1024, vo = lane * 16 + w * 1024;
#pragma unroll
    for (int j = 0; j < 19; ++j) DMAQ(ldsb, j * 8192, vo, srd, (unsigned)(j * 8192));
  }
  asm volatile("s_waitcnt vmcnt(16)" : "+v"(qf1), "+v"(q2) :: "memory");
  BAR();
  u32x4 q2v = {h == 0 ? q2 : 0u, 0u, 0u, 0u};
  const bf16x8 qf2 = __builtin_bit_cast(bf16x8, q2v);
  const int dcl = q31 < 19 ? q31 : (q31 >= 20 && q31 < 28 ? 12 : 16);
  const int koff = lane * 16, k2off = 1024 + q31 * 4, voff = V_OFF + h * 304 + dcl * 16;
  f32x16 o;
#pragma unroll
  for (int r = 0; r < 16; ++r) o[r] = 0.f;
  auto scores = [&](int t, const f32x16& c) {
    const char* tp = smem + t * KV_TILE;
    const bf16x8 k1 = *(const bf16x8*)(tp + koff);
    u32x4 k2v = {*(const unsigned*)(tp + k2off), 0u, 0u, 0u};
    f32x16 s = MFMA32(k1, qf1, c);
    return MFMA32(__builtin_bit_cast(bf16x8, k2v), qf2, s);
  };
  f32x16 zero;
#pragma unroll
  for (int r = 0; r < 16; ++r) zero[r] = 0.f;
  STAMP(1, 1);
  unsigned dummy_keep;
  f32x16 negm;
  {
    const f32x16 s = scores(0, zero);
    float m = s[0];
#pragma unroll
    for (int r = 1; r < 16; ++r) m = fmaxf(m, s[r]);
    m = fmaxf(m, xor32(m));
#pragma unroll
    for (int r = 0; r < 16; ++r) negm[r] = -m;
  }
  {
    bf16x8 k1, v0, v1; u32x4 k2 = {0u, 0u, 0u, 0u};
    f32x16 sA = scores(0, negm), sB;
    k1 = *(const bf16x8*)(smem + KV_TILE + koff); k2[0] = *(const unsigned*)(smem + KV_TILE + k2off);
    v0 = *(const bf16x8*)(smem + voff); v1 = *(const bf16x8*)(smem + voff + 608);
    auto step = [&](int t, f32x16& sc, f32x16& sn) {
      const char* tp = smem + t * KV_TILE;
      u32x4 p0, p1;
      sn = MFMA32(k1, qf1, negm);
      k1 = *(const bf16x8*)(tp + 2 * KV_TILE + koff);
#pragma unroll
      for (int e = 0; e < 4; ++e) sc[e] = __builtin_amdgcn_exp2f(sc[e]);
      SB();
      sn = MFMA32(__builtin_bit_cast(bf16x8, k2), qf2, sn);
      k2[0] = *(const unsigned*)(tp + 2 * KV_TILE + k2off);
#pragma unroll
      for (int e = 4; e < 8; ++e) sc[e] = __builtin_amdgcn_exp2f(sc[e]);
#pragma unroll
      for (int e = 0; e < 4; ++e) p0[e] = cvtpk(sc[2 * e], sc[2 * e + 1]);
      SB();
      o = MFMA32(v0, __builtin_bit_cast(bf16x8, p0), o);
      v0 = *(const bf16x8*)(tp + KV_TILE + voff);
#pragma unroll
      for (int e = 8; e < 16; ++e) sc[e] = __builtin_amdgcn_exp2f(sc[e]);
#pragma unroll
      for (int e = 0; e < 4; ++e) p1[e] = cvtpk(sc[8 + 2 * e], sc[8 + 2 * e + 1]);
      SB();
      o = MFMA32(v1, __builtin_bit_cast(bf16x8, p1), o);
      v1 = *(const bf16x8*)(tp + KV_TILE + voff + 608);
      SB();
    };
    unsigned dummy = 0;
    auto warm = [&]() { const unsigned T = b * 512 + tid; TOUCH(dummy, (const char*)wsp + (size_t)(T < 65536u ? T : 0u) * 128); };
#define CHUNK(c, nv) { if (c > 0) { WAITV(nv); BAR(); } if (c == 3) warm(); \
    for (int t = 8 * c; t < 8 * c + 8; t += 2) { step(t, sA, sB); step(t + 1, sB, sA); } }
    CHUNK(0, 16) CHUNK(1, 13) CHUNK(2, 11) CHUNK(3, 9) CHUNK(4, 7) CHUNK(5, 5) CHUNK(6, 3) CHUNK(7, 1)
#undef CHUNK
    VREG(dummy);
    dummy_keep = dummy;
  }
  STAMP(1, 2);
  float l = __shfl(o[10], q31);
  bool ok = l < 1.8446744e19f;
#pragma unroll
  for (int r = 0; r < 12; ++r) ok = ok && (fabsf(o[r]) <= 3.0e38f);
  if (!__all(ok)) {
    float m = -3.0e38f;
#pragma unroll
    for (int r = 0; r < 16; ++r) o[r] = 0.f;
    for (int t = 0; t < 64; ++t) {
      f32x16 s = scores(t, zero);
      float tmax = s[0];
#pragma unroll
      for (int r = 1; r < 16; ++r) tmax = fmaxf(tmax, s[r]);
      tmax = fmaxf(tmax, xor32(tmax));
      const float mn = fmaxf(m, tmax), alpha = __builtin_amdgcn_exp2f(m - mn);
      m = mn;
#pragma unroll
      for (int r = 0; r < 16; ++r) { o[r] *= alpha; s[r] = __builtin_amdgcn_exp2f(s[r] - mn); }
      const char* tp = smem + t * KV_TILE + voff;
      const bf16x8 v0 = *(const bf16x8*)(tp), v1 = *(const bf16x8*)(tp + 608);
      u32x4 p0, p1;
#pragma unroll
      for (int e = 0; e < 4; ++e) { p0[e] = cvtpk(s[2 * e], s[2 * e + 1]); p1[e] = cvtpk(s[8 + 2 * e], s[8 + 2 * e + 1]); }
      o = MFMA32(v0, __builtin_bit_cast(bf16x8, p0), o);
      o = MFMA32(v1, __builtin_bit_cast(bf16x8, p1), o);
    }
    l = __shfl(o[10], q31);
  }
  const float inv = 1.f / l;
  {
    unsigned a0 = cvtpk(o[0] * inv, o[1] * inv), a1 = cvtpk(o[2] * inv, o[3] * inv), b0 = cvtpk(o[4] * inv, o[5] * inv), b1 = cvtpk(o[6] * inv, o[7] * inv);
    { auto r = __builtin_amdgcn_permlane32_swap(a0, b0, false, false); a0 = r[0]; b0 = r[1]; }
    { auto r = __builtin_amdgcn_permlane32_swap(a1, b1, false, false); a1 = r[0]; b1 = r[1]; }
    const __amdgpu_buffer_rsrc_t so = mksrd(ws + WS_AO + (size_t)(qb * 32 + head) * 8192, 8192);
    __builtin_amdgcn_raw_buffer_store_b128(u32x4{a0, a1, b0, b1}, so, (w * 32 + q31) * 32 + h * 16, 0, 16);
    const __amdgpu_buffer_rsrc_t s2 = mksrd(ws + WS_AO + (size_t)S_ * 1024, S_ * 128);
    if (h == 0) __builtin_amdgcn_raw_buffer_store_b32(cvtpk(o[8] * inv, o[9] * inv), s2, (qb * 256 + w * 32 + q31) * 128 + head * 4, 0, 16);
  }
  asm volatile("s_waitcnt vmcnt(0)" : "+v"(dummy_keep) :: "memory");
  WAITL0(); BAR();
  STAMP(2, 0); STAMP(1, 3);
  using C = CR<9>;
  unsigned* cnt = (unsigned*)(ws + WS_CNT + (32 + qb) * 128);
  if (w == 0 && lane == 0) __hip_atomic_fetch_add(cnt, 1u, __ATOMIC_RELAXED, __HIP_MEMORY_SCOPE_AGENT);
  if (w >= 6) return;
  const int row0 = qb * 256 + (head >> 3) * 64, tn = head & 7;
  if (w >= 4) {
    const int p = w - 4;
    const __amdgpu_buffer_rsrc_t srdA = mksrd(ws + WS_AO, S_ * 1152), srdB = mksrd(ws + WS_WO + (size_t)tn * 9 * WR_TB, 9 * WR_TB);
    const unsigned ldsb = (unsigned)(size_t)smem, mA = ldsb + p * 4096, mB = ldsb + C::ABYTES + p * 5120;
    const int c0 = (lane & 7) ^ (lane >> 4), c1 = (lane & 7) ^ (4 + (lane >> 4));
    const int rl = (head >> 3) * 64 + p * 32 + (lane >> 3);
    const unsigned va0 = (unsigned)(qb * 32 + (c0 >> 1)) * 8192 + rl * 32 + (c0 & 1) * 16, va1 = (unsigned)(qb * 32 + (c1 >> 1)) * 8192 + rl * 32 + (c1 & 1) * 16;
    const unsigned vt0 = S_ * 1024 + (qb * 256 + rl) * 128 + c0 * 16, vt1 = S_ * 1024 + (qb * 256 + rl) * 128 + c1 * 16, vb = p * 5120 + lane * 16;
    auto issueA = [&](int kt) {
      const unsigned sa = mA + (kt & 7) * C::STAGE;
      if (kt < 8) {
#pragma unroll
        for (int j = 0; j < 4; ++j) { const unsigned v = ((j & 1) ? va1 : va0) + j * 8 * 32; DMAQ_SC1(sa, j * 1024, v, srdA, (unsigned)(kt * 4 * 8192)); }
      } else {
#pragma unroll
        for (int j = 0; j < 4; ++j) { const unsigned v = ((j & 1) ? vt1 : vt0) + j * 8 * 128; DMAQ_SC1(sa, j * 1024, v, srdA, 0u); }
      }
    };
    auto issueB = [&](int kt) {
      const unsigned sb = mB + (kt & 7) * C::STAGE, sk = kt * WR_TB;
#pragma unroll
      for (int j = 0; j < 5; ++j) { const unsigned v = vb + j * 1024; DMAQ(sb, j * 1024, v, srdB, sk); }
    };
#pragma unroll
    for (int s = 0; s < 7; ++s) issueB(s);
    if (lane == 0) spin_until(cnt, 32u);
    asm volatile("" ::: "memory");
#pragma unroll
    for (int s = 0; s < 7; ++s) issueA(s);
    WAITV(24); BAR(); issueA(7); issueB(7);
    WAITV(29); BAR(); issueA(8); issueB(8);
    WAITV(34); BAR(); WAITV(30); BAR(); WAITV(26); BAR(); WAITV(22); BAR(); WAITV(18); BAR(); WAITV(9); BAR(); WAITV(0); BAR();
    BAR();
    return;
  }
  {
    const int g = lane >> 4, fr = lane & 15;
    const int tok = row0 + w * 16 + fr;
    const size_t ro = (size_t)tok * D_ + tn * 72 + 4 * g;
    u32x2 r[5];
#pragma unroll
    for (int j = 0; j < 5; ++j) r[j] = *(const u32x2*)(x + ro + ((j < 4 || g < 2) ? 16 * j : 0));
    f32x4 acc[1][5]; float rs[1], rq[1];
    consumer<C, 1, 5, 0, false, false>(smem, w * 16, lane, acc, rs, rq, ws, 7, 0, NoHook());
    u16* out = (u16*)(ws + WS_H);
    float s1 = 0.f, s2 = 0.f;
#pragma unroll
    for (int j = 0; j < 5; ++j) {
      if (j < 4 || g < 2) {
        u32x2 v;
        v[0] = cvtpk(bflo(r[j][0]) + acc[0][j][0], bfhi(r[j][0]) + acc[0][j][1]);
        v[1] = cvtpk(bflo(r[j][1]) + acc[0][j][2], bfhi(r[j][1]) + acc[0][j][3]);
        *(u32x2*)(out + ro + 16 * j) = v;
        const float a0 = bflo(v[0]), a1 = bfhi(v[0]), a2 = bflo(v[1]), a3 = bfhi(v[1]);
        s1 += (a0 + a1) + (a2 + a3);
        s2 = fmaf(a0, a0, fmaf(a1, a1, fmaf(a2, a2, fmaf(a3, a3, s2))));
      }
    }
    s1 += xor16(s1); s1 += xor32(s1); s2 += xor16(s2); s2 += xor32(s2);
    if (g == 0) *(f32x2*)(ws + WS_HST + ((size_t)tok * 8 + tn) * 8) = f32x2{s1, s2};
  }
  STAMP_END(2);
}

template <int KT>
__global__ __launch_bounds__(384, 2) void k_res(const u16* __restrict__ A, const char* __restrict__ Wt, const u16* __restrict__ res, u16* __restrict__ out, char* __restrict__ ws) {
  using C = CR<KT>;
  extern __shared__ __attribute__((aligned(16))) char smem[];
  const int tid = threadIdx.x, lane = tid & 63, w = rfl(tid >> 6);
  const int b = blockIdx.x, xcd = b & 7, idx = b >> 3;
  const int tm = (xcd & 3) * 8 + (idx & 7), tn = (xcd >> 2) * 4 + (idx >> 3);
  constexpr int K = KT * 64;
  char* ws_dbg = ws;
  if (__builtin_expect(w >= 4, 1)) {
    run_producer<C>(smem, A + (size_t)tm * 64 * K, 64 * K * 2, Wt + (size_t)tn * KT * WR_TB, KT * WR_TB, K, 0, w - 4, lane, ws, KT == 9 ? 2 : 6);
    return;
  }
  const int g = lane >> 4, fr = lane & 15;
  const int tok = tm * 64 + w * 16 + fr;
  const size_t ro = (size_t)tok * D_ + tn * 72 + 4 * g;
  u32x2 r[5];
#pragma unroll
  for (int j = 0; j < 5; ++j) r[j] = *(const u32x2*)(res + ro + ((j < 4 || g < 2) ? 16 * j : 0));
  f32x4 acc[1][5]; float rs[1], rq[1];
  consumer<C, 1, 5, 0, false, false>(smem, w * 16, lane, acc, rs, rq, ws, KT == 9 ? 2 : 6, 0, NoHook());
  float s1 = 0.f, s2 = 0.f;
#pragma unroll
  for (int j = 0; j < 5; ++j) {
    if (j < 4 || g < 2) {
      u32x2 v;
      v[0] = cvtpk(bflo(r[j][0]) + acc[0][j][0], bfhi(r[j][0]) + acc[0][j][1]);
      v[1] = cvtpk(bflo(r[j][1]) + acc[0][j][2], bfhi(r[j][1]) + acc[0][j][3]);
      *(u32x2*)(out + ro + 16 * j) = v;
      if constexpr (KT == 9) {
        const float a0 = bflo(v[0]), a1 = bfhi(v[0]), a2 = bflo(v[1]), a3 = bfhi(v[1]);
        s1 += (a0 + a1) + (a2 + a3);
        s2 = fmaf(a0, a0, fmaf(a1, a1, fmaf(a2, a2, fmaf(a3, a3, s2))));
      }
    }
  }
  if constexpr (KT == 9) {
    s1 += xor16(s1); s1 += xor32(s1); s2 += xor16(s2); s2 += xor32(s2);
    if (g == 0) *(f32x2*)(ws + WS_HST + ((size_t)tok * 8 + tn) * 8) = f32x2{s1, s2};
  }
  STAMP_END(KT == 9 ? 2 : 6);
}

constexpr int MST = 36864, MNST = 4;
constexpr int M_CSA = MNST * MST, M_CSB = M_CSA + 576, M_RS = M_CSB + 576, M_S2 = M_RS + 1024, M_LDS = M_S2 + 1024;
__global__ __launch_bounds__(448, 2) void k_mix(const u16* __restrict__ wsp, const u16* __restrict__ bsp, char* __restrict__ ws) {
  extern __shared__ __attribute__((aligned(16))) char smem[];
  const int tid = threadIdx.x, w = rfl(tid >> 6);
  const int b = blockIdx.x, xcd = b & 7, idx = b >> 3;
  const int tm = (xcd & 1) * 8 + (idx & 7), tn = (xcd >> 1) * 4 + (idx >> 3);
  char* ws_dbg = ws;
  unsigned* cnt1 = (unsigned*)(ws + WS_CNT + tm * 128);
  unsigned* cnt2 = (unsigned*)(ws + WS_CNT + (16 + tn) * 128);
  if (__builtin_expect(w == 4 || w == 5, 1)) {
    const int lane = tid & 63, p = w - 4;
    if (p == 0) STAMPW(3, 4);
    const unsigned ldsb = (unsigned)(size_t)smem;
    const unsigned mA = ldsb + p * 8192, mB = ldsb + 16384 + p * 9216;
    const unsigned sw0 = ((lane & 7) ^ (lane >> 4)) << 4, sw1 = ((lane & 7) ^ (4 + (lane >> 4))) << 4;
    const int g = lane >> 4;
    constexpr int NQ = 17;
    {
      const __amdgpu_buffer_rsrc_t srdA = mksrd((const u16*)(ws + WS_H) + (size_t)tm * 128 * D_, 128 * D_ * 2), srdB = mksrd(ws + WS_WIN, 32 * 9 * WIN_TB);
      const unsigned va0 = (p * 64 + (lane >> 3)) * (D_ * 2) + sw0, va1 = (p * 64 + (lane >> 3)) * (D_ * 2) + sw1, vb = p * 9216 + lane * 16;
      auto issue = [&](int s) {
        const int ph = s >= 9, kt = s - 9 * ph;
        const unsigned so = (s & 3) * MST, sa = mA + so, sb = mB + so;
        const unsigned soA = kt * 128, soB = ((ph ? tn : tn + 16) * 9 + kt) * WIN_TB;
#pragma unroll
        for (int j = 0; j < 8; ++j) { const unsigned v = ((j & 1) ? va1 : va0) + j * 8 * (D_ * 2); DMAQ(sa, j * 1024, v, srdA, soA); }
#pragma unroll
        for (int j = 0; j < 9; ++j) { const unsigned v = vb + j * 1024; DMAQ(sb, j * 1024, v, srdB, soB); }
      };
      issue(0); issue(1); issue(2);
      if (p == 0) STAMPW(3, 5);
      for (int s = 0; s < 15; ++s) { WAITV(2 * NQ); BAR(); issue(s + 3); }
      WAITV(2 * NQ); BAR(); WAITV(NQ); BAR(); WAITV(0); BAR();
    }
    BAR();
    BAR();
    if (p == 0) STAMPW(5, 0);
    {
      const __amdgpu_buffer_rsrc_t srdA = mksrd(wsp + (size_t)tm * 128 * S_, 128 * S_ * 2), srdB = mksrd((const u16*)(ws + WS_Z2N) + (size_t)tn * 144 * S_, 144 * S_ * 2);
      const unsigned va0 = (p * 64 + (lane >> 3)) * (S_ * 2) + sw0, va1 = (p * 64 + (lane >> 3)) * (S_ * 2) + sw1;
      const unsigned vb0 = (p * 72 + (lane >> 3)) * (S_ * 2) + (p ? sw1 : sw0), vb1 = (p * 72 + (lane >> 3)) * (S_ * 2) + (p ? sw0 : sw1);
      auto issueA = [&](int kt) {
        const unsigned sa = mA + (kt & 3) * MST, sk = kt * 128;
#pragma unroll
        for (int j = 0; j < 8; ++j) { const unsigned v = ((j & 1) ? va1 : va0) + j * 8 * (S_ * 2); DMAQ(sa, j * 1024, v, srdA, sk); }
      };
      auto issueB = [&](int kt) {
        const unsigned sb = mB + (kt & 3) * MST, sk = kt * 128;
#pragma unroll
        for (int j = 0; j < 9; ++j) { const unsigned v = ((j & 1) ? vb1 : vb0) + j * 8 * (S_ * 2); DMAQ_SC1(sb, j * 1024, v, srdB, sk); }
      };
      auto issue = [&](int kt) { issueA(kt); issueB(kt); };
      issueA(0); issueA(1); issueA(2);
      if (lane == 0) spin_until(cnt2, 16u);
      asm volatile("" ::: "memory");
      if (p == 0) STAMPW(5, 1);
      if (p == 0) STAMPW(5, 2);
      issueB(0); issueB(1); issueB(2);
      if (p == 0) STAMPW(5, 3);
      WAITV(18); BAR(); issue(3);
      WAITV(9 + NQ); BAR(); issue(4);
      for (int kt = 2; kt < 29; ++kt) { WAITV(2 * NQ); BAR(); issue(kt + 3); }
      WAITV(2 * NQ); BAR(); WAITV(NQ); BAR(); WAITV(0); BAR();
    }
    BAR();
    return;
  }
  if (w == 6) {
    const int lane = tid & 63;
    f32x2* rst = (f32x2*)(smem + M_RS);
#pragma unroll
    for (int i = 0; i < 2; ++i) {
      const f32x4* q = (const f32x4*)(ws + WS_HST + (size_t)(tm * 128 + 64 * i + lane) * 64);
      const f32x4 a = q[0], bb = q[1], c = q[2], d = q[3];
      const float s1 = (a[0] + a[2]) + (bb[0] + bb[2]) + (c[0] + c[2]) + (d[0] + d[2]), s2 = (a[1] + a[3]) + (bb[1] + bb[3]) + (c[1] + c[3]) + (d[1] + d[3]);
      const float mu = s1 * (1.f / D_);
      rst[64 * i + lane] = f32x2{mu, rsqrtf(fmaxf(s2 * (1.f / D_) - mu * mu, 0.f) + 1e-5f)};
    }
    if (lane < 36) {
      *(f32x4*)(smem + M_CSA + lane * 16) = *(const f32x4*)(ws + WS_CS + (size_t)((tn + 16) * 144 + lane * 4) * 4);
      *(f32x4*)(smem + M_CSB + lane * 16) = *(const f32x4*)(ws + WS_CS + (size_t)(tn * 144 + lane * 4) * 4);
    }
    WAITL0();
    for (int s = 0; s < 15; ++s) BAR();
    if (lane == 0) spin_until(cnt1, 16u);
    asm volatile("" ::: "memory");
    const __amdgpu_buffer_rsrc_t srdS = mksrd(ws + WS_ST + (size_t)tm * (16 * 128 * 8), 16 * 128 * 8);
    u32x4 pv[16];
#pragma unroll
    for (int q = 0; q < 16; ++q) pv[q] = __builtin_amdgcn_raw_buffer_load_b128(srdS, (q * 128 + 2 * lane) * 8, 0, 16);
    SB(); BAR(); BAR(); SB();
    f32x4 t4 = {0.f, 0.f, 0.f, 0.f};
#pragma unroll
    for (int q = 0; q < 16; ++q) t4 += __builtin_bit_cast(f32x4, pv[q]);
    {
      const float m0 = t4[0] * (1.f / FH), m1 = t4[2] * (1.f / FH);
      const float r0 = rsqrtf(fmaxf(t4[1] * (1.f / FH) - m0 * m0, 0.f) + 1e-5f), r1 = rsqrtf(fmaxf(t4[3] * (1.f / FH) - m1 * m1, 0.f) + 1e-5f);
      *(f32x4*)(smem + M_S2 + lane * 16) = f32x4{r0, -m0 * r0, r1, -m1 * r1};
    }
    WAITL0();
    BAR();
    BAR();
    return;
  }
  const int lane = tid & 63, g = lane >> 4, fr = lane & 15, f = fr >> 1;
  const int l0 = fr * 128 + ((g ^ f) << 4), l1 = fr * 128 + (((4 + g) ^ f) << 4);
  const int tb = 16384 + (16 * (g >> 1) + 4 * (g & 1) + (fr >> 2)) * 288 + (lane & 3) * 8;
  const int tok0 = tm * 128 + w * 32;
  f32x4 acc[2][9];
  bf16x8 a0[2], b0[9], a1[2], b1[9];
  auto zero_acc = [&]() {
#pragma unroll
    for (int i = 0; i < 2; ++i)
#pragma unroll
      for (int j = 0; j < 9; ++j) acc[i][j] = f32x4{0.f, 0.f, 0.f, 0.f};
  };
  auto mm = [&](bf16x8* a, bf16x8* bb) {
#pragma unroll
    for (int j = 0; j < 9; ++j)
#pragma unroll
      for (int i = 0; i < 2; ++i) acc[i][j] = MFMA16(bb[j], a[i], acc[i][j]);
  };
  auto rdKN = [&](bf16x8* a, bf16x8* bb, int s, int ks) {
    const char* sb = smem + (s & 3) * MST;
#pragma unroll
    for (int i = 0; i < 2; ++i) a[i] = *(const bf16x8*)(sb + w * 4096 + i * 2048 + (ks ? l1 : l0));
#pragma unroll
    for (int j = 0; j < 9; ++j) { const char* q = sb + tb + ks * (32 * 288) + j * 32; bb[j] = cat8(trread(q), trread(q + 8 * 288)); }
  };
  auto rdNK = [&](bf16x8* a, bf16x8* bb, int s, int ks) {
    const char* sb = smem + (s & 3) * MST + (ks ? l1 : l0);
#pragma unroll
    for (int i = 0; i < 2; ++i) a[i] = *(const bf16x8*)(sb + w * 4096 + i * 2048);
#pragma unroll
    for (int j = 0; j < 9; ++j) bb[j] = *(const bf16x8*)(sb + 16384 + j * 2048);
  };
#define SCHED(id, ND, RPG) _Pragma("unroll") for (int q = 0; q < 18; ++q) { __builtin_amdgcn_sched_group_barrier(0x008, 1, id); \
    if (q * RPG < ND) __builtin_amdgcn_sched_group_barrier(0x080, RPG, id); }
  auto gelu = [&](u32x2 (&zp)[2][9], int csoff, f32x2 (&sum)[2]) {
    const float c1 = -2.f * 0.7978845608028654f * 1.4426950408889634f, c2 = c1 * 0.044715f;
    const float* cs = (const float*)(smem + csoff);
    const f32x2* rst = (const f32x2*)(smem + M_RS) + w * 32;
    float nmu[2], k1[2], k3[2], irs[2];
    f32x2 s1[2], s2[2];
#pragma unroll
    for (int i = 0; i < 2; ++i) {
      const f32x2 st = rst[16 * i + fr];
      nmu[i] = -st[0]; k1[i] = c1 * st[1]; k3[i] = c2 * st[1] * st[1] * st[1]; irs[i] = __builtin_amdgcn_rcpf(st[1]);
      s1[i] = f32x2{0.f, 0.f}; s2[i] = f32x2{0.f, 0.f};
    }
    f32x4 c4 = *(const f32x4*)(cs + 4 * g);
#pragma unroll
    for (int j = 0; j < 9; ++j) {
      const f32x4 c4n = *(const f32x4*)(cs + 16 * (j < 8 ? j + 1 : 8) + 4 * g);
      SB();
#pragma unroll
      for (int i = 0; i < 2; ++i)
#pragma unroll
        for (int hh = 0; hh < 2; ++hh) {
          const f32x2 a2 = {acc[i][j][2 * hh], acc[i][j][2 * hh + 1]}, c2v = {c4[2 * hh], c4[2 * hh + 1]};
          const f32x2 t = f32x2{nmu[i], nmu[i]} * c2v + a2;
          const f32x2 y = t * (t * t * f32x2{k3[i], k3[i]} + f32x2{k1[i], k1[i]});
          const f32x2 e = {__builtin_amdgcn_exp2f(y[0]), __builtin_amdgcn_exp2f(y[1])};
          const f32x2 d = e * f32x2{irs[i], irs[i]} + f32x2{irs[i], irs[i]};
          const f32x2 z = t * f32x2{__builtin_amdgcn_rcpf(d[0]), __builtin_amdgcn_rcpf(d[1])};
          s1[i] += z; s2[i] += z * z;
          zp[i][j][hh] = cvtpk(z[0], z[1]);
        }
      VREG(zp[0][j]); VREG(zp[1][j]);
      c4 = c4n;
      SB();
    }
#pragma unroll
    for (int i = 0; i < 2; ++i) sum[i] = f32x2{s1[i][0] + s1[i][1], s2[i][0] + s2[i][1]};
  };
  const __amdgpu_buffer_rsrc_t srdS = mksrd(ws + WS_ST + (size_t)tm * (16 * 128 * 8), 16 * 128 * 8);
  zero_acc();
  STAMP(3, 0);
  BAR();
  STAMP(3, 1);
  rdKN(a0, b0, 0, 0);
  u32x2 z2p[2][9];
  auto stageAB = [&](int s) {
    SB(); rdKN(a1, b1, s, 1); mm(a0, b0); SCHED(0, 20, 2) SB();
    WAITL0(); BAR();
    SB(); rdKN(a0, b0, s + 1, 0); mm(a1, b1); SCHED(1, 20, 2)
  };
  for (int s = 0; s < 9; ++s) stageAB(s);
  SB();
  STAMP(5, 6);
  {
    f32x2 sum[2];
    gelu(z2p, M_CSA, sum);
#pragma unroll
    for (int i = 0; i < 2; ++i) {
      f32x2 v = sum[i];
      v[0] += xor16(v[0]); v[0] += xor32(v[0]); v[1] += xor16(v[1]); v[1] += xor32(v[1]);
      if (g == 0) __builtin_amdgcn_raw_buffer_store_b64(__builtin_bit_cast(u32x2, v), srdS, (tn * 128 + w * 32 + 16 * i + fr) * 8, 0, 16);
    }
    asm volatile("s_waitcnt vmcnt(0)" ::: "memory");
    zero_acc();
  }
  SB();
  STAMP(5, 4);
  stageAB(9);
  if (w == 0 && lane == 0) __hip_atomic_fetch_add(cnt1, 1u, __ATOMIC_RELAXED, __HIP_MEMORY_SCOPE_AGENT);
  for (int s = 10; s < 17; ++s) stageAB(s);
  SB(); rdKN(a1, b1, 17, 1); mm(a0, b0); mm(a1, b1); SB();
  WAITL0(); BAR();
  STAMP(3, 2);
  {
    const f32x2* st2 = (const f32x2*)(smem + M_S2) + w * 32;
    char* reg = smem + w * (32 * 304);
#pragma unroll
    for (int i = 0; i < 2; ++i) {
      const f32x2 st = st2[16 * i + fr];
#pragma unroll
      for (int j = 0; j < 9; ++j) {
        u32x2 v;
        v[0] = cvtpk(fmaf(bflo(z2p[i][j][0]), st[0], st[1]), fmaf(bfhi(z2p[i][j][0]), st[0], st[1]));
        v[1] = cvtpk(fmaf(bflo(z2p[i][j][1]), st[0], st[1]), fmaf(bfhi(z2p[i][j][1]), st[0], st[1]));
        *(u32x2*)(reg + (16 * i + fr) * 304 + (16 * j + 4 * g) * 2) = v;
      }
    }
    WAITL0();
    const __amdgpu_buffer_rsrc_t so = mksrd((u16*)(ws + WS_Z2N) + (size_t)(tn * 144) * S_ + tok0, 144 * S_ * 2);
    const char* rp = reg + (8 * g + (fr >> 2)) * 304 + (lane & 3) * 8;
#pragma unroll
    for (int j = 0; j < 9; ++j)
      __builtin_amdgcn_raw_buffer_store_b128(__builtin_bit_cast(u32x4, cat8(trread(rp + j * 32), trread(rp + 4 * 304 + j * 32))), so, (j * 16 + fr) * (S_ * 2) + 8 * g * 2, 0, 16);
    asm volatile("s_waitcnt vmcnt(0)" ::: "memory");
    WAITL0(); BAR();
    if (w == 0 && lane == 0) __hip_atomic_fetch_add(cnt2, 1u, __ATOMIC_RELAXED, __HIP_MEMORY_SCOPE_AGENT);
  }
  u32x2 z1p[2][9];
  { f32x2 sum[2]; gelu(z1p, M_CSB, sum); }
  const unsigned bias0 = bsp[tok0 + fr], bias1 = bsp[tok0 + 16 + fr];
  zero_acc();
  STAMP(4, 0);
  BAR();
  STAMP(4, 1);
  rdNK(a0, b0, 0, 0);
  for (int kt = 0; kt < 31; ++kt) {
    SB(); rdNK(a1, b1, kt, 1); mm(a0, b0); SCHED(0, 11, 1) SB();
    WAITL0(); BAR();
    SB(); rdNK(a0, b0, kt + 1, 0); mm(a1, b1); SCHED(1, 11, 1)
  }
  SB(); rdNK(a1, b1, 31, 1); mm(a0, b0); mm(a1, b1); SB();
  WAITL0(); BAR();
  SB();
  STAMP(4, 2);
#undef SCHED
  char* reg = smem + w * (32 * 304);
#pragma unroll
  for (int i = 0; i < 2; ++i) {
    const float bi = bflo(i ? bias1 : bias0);
#pragma unroll
    for (int j = 0; j < 9; ++j) {
      u32x2 v;
      v[0] = cvtpk(bflo(z1p[i][j][0]) * (acc[i][j][0] + bi), bfhi(z1p[i][j][0]) * (acc[i][j][1] + bi));
      v[1] = cvtpk(bflo(z1p[i][j][1]) * (acc[i][j][2] + bi), bfhi(z1p[i][j][1]) * (acc[i][j][3] + bi));
      *(u32x2*)(reg + (16 * i + fr) * 304 + (16 * j + 4 * g) * 2) = v;
    }
  }
  WAITL0();
  const __amdgpu_buffer_rsrc_t so = mksrd((u16*)(ws + WS_U) + (size_t)tok0 * FH + tn * 144, 32 * FH * 2);
#pragma unroll
  for (int i = 0; i < 9; ++i) {
    const int c = i * 64 + lane, row = c / 18, ch = c - row * 18;
    st16(so, row * (FH * 2) + ch * 16, *(const u32x4*)(reg + row * 304 + ch * 16));
  }
}

extern "C" void kernel_launch(void* const* d_in, const int* in_sizes, int n_in, void* d_out, int out_size, void* d_ws, size_t ws_size, hipStream_t stream) {
  const u16* x = (const u16*)d_in[0];
  const u16* wqkv = (const u16*)d_in[1];
  const u16* wo = (const u16*)d_in[2];
  const u16* win = (const u16*)d_in[4];
  const u16* wsp = (const u16*)d_in[5];
  const u16* bsp = (const u16*)d_in[6];
  const u16* wout = (const u16*)d_in[7];
  char* ws = (char*)d_ws;
  (void)hipFuncSetAttribute((const void*)k_qkv, hipFuncAttributeMaxDynamicSharedMemorySize, C1::LDS);
  (void)hipFuncSetAttribute((const void*)k_attn, hipFuncAttributeMaxDynamicSharedMemorySize, ATT_LDS);
  (void)hipFuncSetAttribute((const void*)k_res<36>, hipFuncAttributeMaxDynamicSharedMemorySize, CR<36>::LDS);
  (void)hipFuncSetAttribute((const void*)k_mix, hipFuncAttributeMaxDynamicSharedMemorySize, M_LDS);
  hipLaunchKernelGGL(k_qkv, dim3(256), dim3(512), C1::LDS, stream, x, wqkv, win, wout, wo, ws);
  hipLaunchKernelGGL(k_attn, dim3(256), dim3(512), ATT_LDS, stream, ws, wsp, x);
  hipLaunchKernelGGL(k_mix, dim3(256), dim3(448), M_LDS, stream, wsp, bsp, ws);
  hipLaunchKernelGGL(k_res<36>, dim3(256), dim3(384), CR<36>::LDS, stream, (const u16*)(ws + WS_U), (const char*)(ws + WS_WOUT), (const u16*)(ws + WS_H), (u16*)d_out, ws);
}
```

```cpp
#include <hip/hip_runtime.h>
#include <stdint.h>

typedef unsigned short u16;
typedef short bf16x8 __attribute__((ext_vector_type(8)));
typedef short s16x4 __attribute__((ext_vector_type(4)));
typedef float f32x2 __attribute__((ext_vector_type(2)));
typedef float f32x4 __attribute__((ext_vector_type(4)));
typedef float f32x16 __attribute__((ext_vector_type(16)));
typedef unsigned u32x2 __attribute__((ext_vector_type(2)));
typedef unsigned u32x4 __attribute__((ext_vector_type(4)));
typedef __bf16 bf16x2_t __attribute__((ext_vector_type(2)));
typedef __attribute__((address_space(3))) s16x4* lds_s16x4_ptr;
#define DI __device__ __forceinline__

constexpr int S_ = 2048, D_ = 576, H_ = 32, DH = 18, FH = 2304, F2 = 4608;
constexpr int QK_TILE = 1152;
constexpr int V_OFF = 1152;
constexpr int KV_TILE = 1152 + 4 * 304;
constexpr int KV_HEAD = 64 * KV_TILE;
constexpr int Q_HEAD = 64 * QK_TILE;
constexpr int WIN_TB = 64 * 288, WR_TB = 64 * 160;
constexpr size_t WS_Q = 0;
constexpr size_t WS_KV = WS_Q + (size_t)H_ * Q_HEAD;
constexpr size_t WS_AO = WS_KV + (size_t)H_ * KV_HEAD + 8192;
constexpr size_t WS_H = WS_AO + (size_t)S_ * D_ * 2;
constexpr size_t WS_Z1 = WS_H + (size_t)S_ * D_ * 2;
constexpr size_t WS_Z2T = WS_Z1 + (size_t)S_ * FH * 2;
constexpr size_t WS_Z2N = WS_Z2T + (size_t)S_ * FH * 2;
constexpr size_t WS_ST = WS_Z2N + (size_t)S_ * FH * 2;
constexpr size_t WS_CNT = WS_ST + (size_t)S_ * 16 * 2 * 4;
constexpr size_t WS_HST = WS_ST + (size_t)S_ * 32 * 2 * 4;
constexpr size_t WS_U = WS_HST + (size_t)S_ * 8 * 2 * 4;
constexpr size_t WS_WIN = WS_U + (size_t)S_ * FH * 2;
constexpr size_t WS_WOUT = WS_WIN + (size_t)32 * 9 * WIN_TB;
constexpr size_t WS_WO = WS_WOUT + (size_t)8 * 36 * WR_TB;
constexpr size_t WS_CS = WS_WO + (size_t)8 * 9 * WR_TB;
constexpr size_t WS_END = WS_CS + (size_t)F2 * 4;

DI unsigned cvtpk(float lo, float hi) { f32x2 v = {lo, hi}; bf16x2_t b = __builtin_convertvector(v, bf16x2_t); return __builtin_bit_cast(unsigned, b); }
DI float bflo(unsigned u) { return __uint_as_float(u << 16); }
DI float bfhi(unsigned u) { return __uint_as_float(u & 0xffff0000u); }
DI float bfr(float x) { return __uint_as_float(cvtpk(x, x) << 16); }
DI f32x2 ln_stats(float s1, float s2, float inv_n) {
  const float mu = s1 * inv_n, var = fmaxf(s2 * inv_n - mu * mu, 0.f);
  return f32x2{bfr(mu), 1.f / bfr(sqrtf(bfr(bfr(var) + 1e-5f)))};
}
DI unsigned rfl(unsigned x) { return __builtin_amdgcn_readfirstlane(x); }
DI float xor16(float v) { return __shfl_xor(v, 16); }
DI float xor32(float v) { return __shfl_xor(v, 32); }
#define DMAQ(sb, imm, voff, srd, soff) asm volatile("s_add_u32 m0, %0, %1\n\ts_nop 0\n\tbuffer_load_dwordx4 %2, %3, %4 offen lds" :: "s"(sb), "i"(imm), "v"(voff), "s"(srd), "s"(soff) : "scc")
#define DMAQ_SC1(sb, imm, voff, srd, soff) asm volatile("s_add_u32 m0, %0, %1\n\ts_nop 0\n\tbuffer_load_dwordx4 %2, %3, %4 offen sc1 lds" :: "s"(sb), "i"(imm), "v"(voff), "s"(srd), "s"(soff) : "scc")
#define WAITV(n) asm volatile("s_waitcnt vmcnt(%0)" :: "i"(n) : "memory")
#define WAITL0() asm volatile("s_waitcnt lgkmcnt(0)" ::: "memory")
#define BAR() asm volatile("s_barrier" ::: "memory")
#define SB() __builtin_amdgcn_sched_barrier(0)
#define MFMA16(a, b, c) __builtin_amdgcn_mfma_f32_16x16x32_bf16((a), (b), (c), 0, 0, 0)
#define MFMA32(a, b, c) __builtin_amdgcn_mfma_f32_32x32x16_bf16((a), (b), (c), 0, 0, 0)
#define VREG(x) asm volatile("" : "+v"(x))
DI __amdgpu_buffer_rsrc_t mksrd(const void* p, unsigned bytes) { return __builtin_amdgcn_make_buffer_rsrc((void*)p, (short)0, (int)bytes, 0x00020000); }
DI s16x4 trread(const char* p) { return __builtin_amdgcn_ds_read_tr16_b64_v4i16((lds_s16x4_ptr)(p)); }
DI bf16x8 cat8(s16x4 lo, s16x4 hi) { return __builtin_shufflevector(lo, hi, 0, 1, 2, 3, 4, 5, 6, 7); }
DI bf16x8 ones8() { const u32x4 o = {0x3F803F80u, 0x3F803F80u, 0x3F803F80u, 0x3F803F80u}; return __builtin_bit_cast(bf16x8, o); }
#define ROT1 (3 * (((idx >> 2) + (idx & 3)) % 3))
#define ST_AUX 16
DI void st16(__amdgpu_buffer_rsrc_t srd, unsigned off, u32x4 v) { __builtin_amdgcn_raw_buffer_store_b128(v, srd, off, 0, ST_AUX); }
#define TOUCH(dummy, ptr) asm volatile("global_load_dword %0, %1, off" : "+v"(dummy) : "v"(ptr))
#define STAMP(kid, slot)
#define STAMPW(kid, slot)
#define STAMP_END(kid)

enum { B_NK = 0, B_KN = 1, B_TILED = 2 };
template <int BM_, int NT_, int NP_, int NST_, int BMODE_, int KT_, bool COLSUM_>
struct Cfg {
  static constexpr int BM = BM_, NT = NT_, NP = NP_, NST = NST_, KT = KT_, BMODE = BMODE_;
  static constexpr bool BKN = BMODE_ != B_NK, COLSUM = COLSUM_;
  static constexpr int BN = NT * 16;
  static constexpr int NA = BM / 8, NB = BN / 8, NI = NA + NB;
  static constexpr int ABYTES = BM * 128, BBYTES = BN * 128, STAGE = ABYTES + BBYTES;
  static constexpr int RING = NST * STAGE;
  static constexpr int CS_OFF = RING;
  static constexpr int RS_OFF = RING + BN * 4;
  static constexpr int LDS = RS_OFF + BM * 8;
};

template <class C, int TAILN = 0, int TCPR = 1>
DI void run_producer(char* smem, const void* Ab, unsigned abytes, const void* Bb, unsigned bbytes, int lda, int ldb, int p, int lane, char* ws_dbg, int kid,
                     int rot = 0, const void* Tb = nullptr, unsigned tbytes = 0, int ldt = 0) {
  if (p == 0) STAMPW(kid, 4);
  constexpr int HA = C::NA / 2, HB = C::NB / 2, NQ = HA + HB;
  static_assert(C::NA % 4 == 0 && C::NB % 2 == 0, "piece split");
  const __amdgpu_buffer_rsrc_t srdA = mksrd(Ab, abytes), srdB = mksrd(Bb, bbytes);
  const unsigned ldsb = (unsigned)(size_t)smem;
  const unsigned mA = ldsb + p * (HA * 1024), mB = ldsb + C::ABYTES + p * (HB * 1024);
  const unsigned sw0 = ((lane & 7) ^ (lane >> 4)) << 4, sw1 = ((lane & 7) ^ (4 + (lane >> 4))) << 4;
  const unsigned va0 = (p * HA * 8 + (lane >> 3)) * (lda * 2) + sw0, va1 = (p * HA * 8 + (lane >> 3)) * (lda * 2) + sw1;
  unsigned vb[HB];
#pragma unroll
  for (int j = 0; j < HB; ++j) {
    if (C::BMODE == B_NK) {
      const int q = p * HB + j;
      vb[j] = (8 * q + (lane >> 3)) * (ldb * 2) + ((q & 1) ? sw1 : sw0);
    } else if (C::BMODE == B_KN) {
      constexpr int CPR = C::BN / 8;
      const int c = 64 * (p * HB + j) + lane, rho = c / CPR, ch = c - rho * CPR;
      const int k = (rho & ~12) | ((rho & 4) << 1) | ((rho & 8) >> 1);
      vb[j] = k * (ldb * 2) + ch * 16;
    } else {
      vb[j] = (p * HB + j) * 1024 + lane * 16;
    }
  }
  auto issue = [&](int kt) {
    const unsigned so = (kt % C::NST) * C::STAGE, sa = mA + so, sb = mB + so;
    int kr = kt + rot; if (kr >= C::KT) kr -= C::KT;
    const unsigned soA = kr * 128, soB = C::BMODE == B_NK ? kr * 128 : (C::BMODE == B_KN ? kr * 64 * ldb * 2 : kr * C::BBYTES);
#pragma unroll
    for (int j = 0; j < HA; ++j) { const unsigned v = ((j & 1) ? va1 : va0) + j * 8 * (lda * 2); DMAQ(sa, j * 1024, v, srdA, soA); }
#pragma unroll
    for (int j = 0; j < HB; ++j) DMAQ(sb, j * 1024, vb[j], srdB, soB);
  };
  constexpr int PF = C::NST - 1;
#pragma unroll
  for (int s = 0; s < PF; ++s) issue(s);
  if (p == 0) STAMPW(kid, 5);
  constexpr int NJ = C::COLSUM ? (C::NT + 1) / 2 : 0;
  f32x4 cs[NJ > 0 ? NJ : 1];
#pragma unroll
  for (int j = 0; j < NJ; ++j) cs[j] = f32x4{0.f, 0.f, 0.f, 0.f};
  const int g = lane >> 4;
  const int tb = C::ABYTES + (16 * (g >> 1) + 4 * (g & 1) + ((lane & 15) >> 2)) * (C::BN * 2) + (lane & 3) * 8 + p * 32;
  auto colsum = [&](int kt) {
    if constexpr (C::COLSUM) {
      const char* sb = smem + (kt % C::NST) * C::STAGE + tb;
#pragma unroll
      for (int jj = 0; jj < NJ; ++jj) {
#pragma unroll
        for (int ks = 0; ks < 2; ++ks) {
          const char* q = sb + ks * (32 * C::BN * 2) + jj * 64;
          cs[jj] = MFMA16(cat8(trread(q), trread(q + 8 * C::BN * 2)), ones8(), cs[jj]);
        }
      }
      WAITL0();
    }
  };
  for (int kt = 0; kt < C::KT - PF; ++kt) { WAITV((PF - 1) * NQ); BAR(); issue(kt + PF); colsum(kt); }
  constexpr int NTL = TAILN / 4;
  auto tail = [&](int half) {
    const unsigned sb = ldsb + ((C::KT + half) % C::NST) * C::STAGE + p * (NTL * 1024);
#pragma unroll
    for (int j = 0; j < NTL; ++j) {
      const int c = 64 * (half * 2 * NTL + p * NTL + j) + lane, row = c / TCPR, ch = c - row * TCPR;
      const unsigned vo2 = row * ldt + ch * 16;
      DMAQ(sb, j * 1024, vo2, mksrd(Tb, tbytes), 0u);
    }
  };
#pragma unroll
  for (int r = PF - 1; r >= 0; --r) {
    WAITV(r * NQ + (TAILN > 0 ? (r < PF - 1 ? NTL : 0) + (r < PF - 2 ? NTL : 0) : 0)); BAR();
    if (TAILN > 0 && r == PF - 1) tail(0);
    if (TAILN > 0 && r == PF - 2) tail(1);
    colsum(C::KT - 1 - r);
  }
  if (TAILN > 0) WAITV(0);
  if constexpr (C::COLSUM) {
    float* st = (float*)(smem + C::CS_OFF);
#pragma unroll
    for (int jj = 0; jj < NJ; ++jj) {
      if ((lane & 15) == 0 && p + 2 * jj < C::NT) *(f32x4*)(st + 16 * (p + 2 * jj) + 4 * g) = cs[jj];
    }
    WAITL0();
  }
  BAR();
}

struct NoHook { DI void operator()() const {} };
template <class C, int MI, int NTW, int J0, bool ROLL, bool ROWSTAT, class Hook>
DI void consumer(char* smem, int row0, int lane, f32x4 (&acc)[MI][NTW], float (&rs)[MI], float (&rq)[MI], char* ws_dbg, int kid, int hook_kt, Hook hook) {
  const int g = lane >> 4, fr = lane & 15, f = fr >> 1;
  const int la0 = row0 * 128 + fr * 128 + ((g ^ f) << 4), la1 = row0 * 128 + fr * 128 + (((4 + g) ^ f) << 4);
  const int lb0 = C::ABYTES + J0 * 2048 + fr * 128 + ((g ^ f) << 4), lb1 = C::ABYTES + J0 * 2048 + fr * 128 + (((4 + g) ^ f) << 4);
  const int tb = C::ABYTES + J0 * 32 + (16 * (g >> 1) + 4 * (g & 1) + (fr >> 2)) * (C::BN * 2) + (lane & 3) * 8;
  f32x4 ars[MI], arq[MI];
#pragma unroll
  for (int i = 0; i < MI; ++i) {
    ars[i] = f32x4{0.f, 0.f, 0.f, 0.f}; arq[i] = f32x4{0.f, 0.f, 0.f, 0.f};
#pragma unroll
    for (int j = 0; j < NTW; ++j) acc[i][j] = f32x4{0.f, 0.f, 0.f, 0.f};
  }
  bf16x8 a0[MI], b0[NTW], a1[MI], b1[ROLL ? 1 : NTW];
  auto rda = [&](bf16x8* a, int kt, int ks) {
    const char* sb = smem + (kt % C::NST) * C::STAGE + (ks ? la1 : la0);
#pragma unroll
    for (int i = 0; i < MI; ++i) a[i] = *(const bf16x8*)(sb + i * 2048);
  };
  auto rdb1 = [&](int j, int kt, int ks) -> bf16x8 {
    const char* sb = smem + (kt % C::NST) * C::STAGE;
    if constexpr (C::BKN) {
      const char* p = sb + tb + ks * (32 * C::BN * 2) + j * 32;
      return cat8(trread(p), trread(p + 8 * C::BN * 2));
    } else {
      return *(const bf16x8*)(sb + (ks ? lb1 : lb0) + j * 2048);
    }
  };
  auto stats = [&](bf16x8* a) {
    if constexpr (ROWSTAT) {
#pragma unroll
      for (int i = 0; i < MI; ++i) { ars[i] = MFMA16(ones8(), a[i], ars[i]); arq[i] = MFMA16(a[i], a[i], arq[i]); }
    }
  };
  STAMP(kid, 0);
  BAR();
  STAMP(kid, 1);
  if constexpr (!ROLL) {
    constexpr int NM = MI * NTW + (ROWSTAT ? 2 * MI : 0), ND = MI + NTW * (C::BKN ? 2 : 1), RPG = (ND + (NM * 2 / 3) - 1) / (NM * 2 / 3);
#define SCHED(id) _Pragma("unroll") for (int q = 0; q < NM; ++q) { __builtin_amdgcn_sched_group_barrier(0x008, 1, id); \
    if (q * RPG < ND) __builtin_amdgcn_sched_group_barrier(0x080, RPG, id); }
    auto rd = [&](bf16x8* a, bf16x8* bb, int kt, int ks) {
      rda(a, kt, ks);
#pragma unroll
      for (int j = 0; j < NTW; ++j) bb[j] = rdb1(j, kt, ks);
    };
    auto mm = [&](bf16x8* a, bf16x8* bb) {
#pragma unroll
      for (int j = 0; j < NTW; ++j)
#pragma unroll
        for (int i = 0; i < MI; ++i) acc[i][j] = MFMA16(bb[j], a[i], acc[i][j]);
      stats(a);
    };
    auto body = [&](int kt) {
      SB();
      rd(a1, b1, kt, 1);
      mm(a0, b0);
      SCHED(0)
      SB();
      WAITL0(); BAR();
      SB();
      rd(a0, b0, kt + 1, 0);
      mm(a1, b1);
      SCHED(1)
    };
    rd(a0, b0, 0, 0);
    if (hook_kt > 0) {
      for (int kt = 0; kt < hook_kt; ++kt) body(kt);
      SB(); hook(); SB();
      for (int kt = hook_kt; kt < C::KT - 1; ++kt) body(kt);
    } else {
      for (int kt = 0; kt < C::KT - 1; ++kt) body(kt);
    }
    SB();
    rd(a1, b1, C::KT - 1, 1);
    mm(a0, b0);
    mm(a1, b1);
#undef SCHED
  } else {
    auto sub = [&](bf16x8* a, int kt, int ks, bool more) {
#pragma unroll
      for (int j = 0; j < NTW; ++j) {
#pragma unroll
        for (int i = 0; i < MI; ++i) acc[i][j] = MFMA16(b0[j], a[i], acc[i][j]);
        if (more) b0[j] = rdb1(j, kt, ks);
        SB();
      }
      stats(a);
    };
    rda(a0, 0, 0);
#pragma unroll
    for (int j = 0; j < NTW; ++j) b0[j] = rdb1(j, 0, 0);
    for (int kt = 0; kt < C::KT - 1; ++kt) {
      rda(a1, kt, 1);
      sub(a0, kt, 1, true);
      WAITL0(); BAR();
      rda(a0, kt + 1, 0);
      sub(a1, kt + 1, 0, true);
    }
    rda(a1, C::KT - 1, 1);
    sub(a0, C::KT - 1, 1, true);
    sub(a1, 0, 0, false);
  }
  if constexpr (ROWSTAT) {
#pragma unroll
    for (int i = 0; i < MI; ++i) {
      rs[i] = ars[i][0];
      const float d = (fr & 2) ? ((fr & 1) ? arq[i][3] : arq[i][2]) : ((fr & 1) ? arq[i][1] : arq[i][0]);
      rq[i] = __shfl(d, fr + 16 * (fr >> 2));
    }
  }
  SB();
  WAITL0(); BAR();
  SB();
  STAMP(kid, 2);
}

using C1 = Cfg<128, 9, 4, 4, B_KN, 9, false>;
constexpr int RU1 = 32 * 9 * 18, RU2 = RU1 + 8 * 36 * 10, RU3 = RU2 + 8 * 9 * 10;
DI const u16* retile_src(const u16* __restrict__ win, const u16* __restrict__ wout, const u16* __restrict__ wo, int u, int lane) {
  if (u < RU1) {
    const int tile = u / 18, q = u - tile * 18, tn = tile / 9, kt = tile - tn * 9;
    const int c = 64 * q + lane, rho = c / 18, ch = c - rho * 18;
    const int k = (rho & ~12) | ((rho & 4) << 1) | ((rho & 8) >> 1);
    return win + (size_t)(kt * 64 + k) * F2 + tn * 144 + ch * 8;
  }
  const bool o = u < RU2; const int v = u - (o ? RU1 : RU2), KT = o ? 36 : 9;
  const int tile = v / 10, q = v - tile * 10, tn = tile / KT, kt = tile - tn * KT;
  const int c = 64 * q + lane, rho = c / 10, ch = c - rho * 10;
  const int k = (rho & ~12) | ((rho & 4) << 1) | ((rho & 8) >> 1);
  int col = tn * 72 + ch * 8; if (col > D_ - 8) col = D_ - 8;
  int kr = kt * 64 + k;
  if (!o) kr = kr < 512 ? 18 * (kr >> 4) + (kr & 15) : 18 * ((kr - 512) >> 1) + 16 + (kr & 1);
  return (o ? wout : wo) + (size_t)kr * D_ + col;
}
__global__ __launch_bounds__(512, 2) void k_qkv(const u16* __restrict__ x, const u16* __restrict__ wqkv, const u16* __restrict__ win, const u16* __restrict__ wout,
                                                const u16* __restrict__ wo, char* __restrict__ ws) {
  extern __shared__ __attribute__((aligned(16))) char smem[];
  const int tid = threadIdx.x, w = rfl(tid >> 6);
  const int b = blockIdx.x, xcd = b & 7, idx = b >> 3;
  char* ws_dbg = ws;
  if (b == 255 && tid < 56) *(unsigned*)(ws + WS_CNT + tid * 128) = 0u;
  if (idx >= 24) {
    STAMP(0, 0); STAMP(0, 1); STAMP(0, 2);
    const int pw = (idx - 24) * 8 + xcd, lane = tid & 63, gw = pw * 8 + w;
    constexpr int NU = (RU3 + 511) / 512;
    u32x4 v[NU];
#pragma unroll
    for (int i = 0; i < NU; ++i) { const int u = gw + 512 * i; v[i] = *(const u32x4*)retile_src(win, wout, wo, u < RU3 ? u : gw, lane); }
    const int cc = tid % 9, rg = tid / 9;
    u32x4 cv[11];
    if (rg < 56) {
#pragma unroll
      for (int i = 0; i < 11; ++i) { const int r = rg + 56 * i; cv[i] = *(const u32x4*)(win + (size_t)(r < D_ ? r : rg) * F2 + pw * 72 + cc * 8); }
    }
    const __amdgpu_buffer_rsrc_t so = mksrd(ws + WS_WIN, RU3 * 1024);
#pragma unroll
    for (int i = 0; i < NU; ++i) { const int u = gw + 512 * i; if (u < RU3) st16(so, u * 1024 + lane * 16, v[i]); }
    float* red = (float*)smem;
    if (rg < 56) {
      f32x4 sa = {0.f, 0.f, 0.f, 0.f}, sb = {0.f, 0.f, 0.f, 0.f};
#pragma unroll
      for (int i = 0; i < 11; ++i) if (rg + 56 * i < D_) {
        sa += f32x4{bflo(cv[i][0]), bfhi(cv[i][0]), bflo(cv[i][1]), bfhi(cv[i][1])};
        sb += f32x4{bflo(cv[i][2]), bfhi(cv[i][2]), bflo(cv[i][3]), bfhi(cv[i][3])};
      }
      *(f32x4*)(red + rg * 72 + cc * 8) = sa; *(f32x4*)(red + rg * 72 + cc * 8 + 4) = sb;
    }
    __syncthreads();
    if (tid < 288) {
      const int c = tid % 72, q = tid / 72;
      float t = 0.f;
#pragma unroll
      for (int r = 0; r < 14; ++r) t += red[(q * 14 + r) * 72 + c];
      red[56 * 72 + tid] = t;
    }
    __syncthreads();
    if (tid < 72) ((float*)(ws + WS_CS))[pw * 72 + tid] = (red[56 * 72 + tid] + red[56 * 72 + 72 + tid]) + (red[56 * 72 + 144 + tid] + red[56 * 72 + 216 + tid]);
    STAMP_END(0);
    return;
  }
  const int tm = (xcd & 3) * 4 + (idx & 3), tn = (xcd >> 2) * 6 + (idx >> 2);
  if (__builtin_expect(w == 4 || w == 5, 1)) {
    run_producer<C1>(smem, x + (size_t)tm * 128 * D_, 128 * D_ * 2, wqkv + tn * 144, (unsigned)((D_ * 3 * D_ - tn * 144) * 2), D_, 3 * D_, w - 4, tid & 63, ws, 0, ROT1);
    return;
  }
  if (w >= 6) {
    const int lane = tid & 63, p = w - 6, g = lane >> 4;
    f32x4 cs[5];
#pragma unroll
    for (int j = 0; j < 5; ++j) cs[j] = f32x4{0.f, 0.f, 0.f, 0.f};
    const int tb = C1::ABYTES + (16 * (g >> 1) + 4 * (g & 1) + ((lane & 15) >> 2)) * 288 + (lane & 3) * 8 + p * 32;
    for (int kt = 0; kt < 9; ++kt) {
      BAR();
      const char* sb = smem + (kt % C1::NST) * C1::STAGE + tb;
#pragma unroll
      for (int jj = 0; jj < 5; ++jj)
#pragma unroll
        for (int ks = 0; ks < 2; ++ks) {
          const char* q = sb + ks * (32 * 288) + jj * 64;
          cs[jj] = MFMA16(cat8(trread(q), trread(q + 8 * 288)), ones8(), cs[jj]);
        }
      WAITL0();
    }
    float* st = (float*)(smem + C1::CS_OFF);
#pragma unroll
    for (int jj = 0; jj < 5; ++jj) if ((lane & 15) == 0 && p + 2 * jj < 9) *(f32x4*)(st + 16 * (p + 2 * jj) + 4 * g) = cs[jj];
    WAITL0(); BAR();
    return;
  }
  f32x4 acc[2][9]; float rs[2], rq[2];
  consumer<C1, 2, 9, 0, false, true>(smem, w * 32, tid & 63, acc, rs, rq, ws, 0, 0, NoHook());
  int tz = threadIdx.x; VREG(tz);
  const int lane = tz & 63, g = lane >> 4, fr = lane & 15;
  const float* cs = (const float*)(smem + C1::CS_OFF);
  const int kind = tn >> 2, head0 = (tn & 3) * 8;
  const int tile32 = tm * 4 + w;
  const float sc = kind == 0 ? 0.23570226039551584f * 1.4426950408889634f : 1.f;
  float mu[2], rstd[2];
#pragma unroll
  for (int i = 0; i < 2; ++i) {
    const f32x2 st = ln_stats(rs[i], rq[i], 1.f / D_);
    mu[i] = st[0]; rstd[i] = st[1] * sc;
  }
  constexpr int RP = 336;
  char* reg = smem + w * (32 * RP);
  f32x4 c4a[9];
#pragma unroll
  for (int j = 0; j < 9; ++j) c4a[j] = *(const f32x4*)(cs + 16 * j + 4 * g);
#pragma unroll
  for (int j = 0; j < 9; ++j) {
    const f32x4 c4 = c4a[j];
#pragma unroll
    for (int rr = 0; rr < 2; ++rr) {
      const int c = 16 * j + 4 * g + 2 * rr, off = c * 2 + (c / 18) * 4;
#pragma unroll
      for (int i = 0; i < 2; ++i)
        *(unsigned*)(reg + (16 * i + fr) * RP + off) = cvtpk((acc[i][j][2 * rr] - mu[i] * c4[2 * rr]) * rstd[i], (acc[i][j][2 * rr + 1] - mu[i] * c4[2 * rr + 1]) * rstd[i]);
    }
  }
  WAITL0();
  if (kind < 2) {
    char* base = ws + (kind == 0 ? WS_Q + (size_t)tile32 * QK_TILE + (size_t)head0 * Q_HEAD : WS_KV + (size_t)tile32 * KV_TILE + (size_t)head0 * KV_HEAD);
    const int hstride = kind == 0 ? Q_HEAD : KV_HEAD;
    const char* rp = reg + (lane & 31) * RP + (lane >> 5) * 16;
#pragma unroll
    for (int hl = 0; hl < 8; ++hl) {
      const u32x2 lo = *(const u32x2*)(rp + hl * 40), hi = *(const u32x2*)(rp + hl * 40 + 8);
      *(u32x4*)(base + (size_t)hl * hstride + lane * 16) = u32x4{lo[0], lo[1], hi[0], hi[1]};
    }
#pragma unroll
    for (int q = 0; q < 4; ++q) {
      const int hl = 2 * q + (lane >> 5);
      *(unsigned*)(base + (size_t)hl * hstride + 1024 + (lane & 31) * 4) = *(const unsigned*)(reg + (lane & 31) * RP + hl * 40 + 32);
    }
  } else {
    char* base = ws + WS_KV + (size_t)tile32 * KV_TILE + V_OFF + (size_t)head0 * KV_HEAD;
    const int s = g >> 1, h = g & 1;
    const char* rp = reg + (16 * s + 4 * h + (fr >> 2)) * RP + (lane & 3) * 8;
#pragma unroll
    for (int j = 0; j < 10; ++j) {
      const bf16x8 v = cat8(trread(rp + j * 32), trread(rp + 8 * RP + j * 32));
      const int c = 16 * j + fr, hl = c / 20, d = c - hl * 20;
      if (d < 18) *(bf16x8*)(base + (size_t)hl * KV_HEAD + s * 608 + h * 304 + d * 16) = v;
    }
    if (lane < 32) {
      *(bf16x8*)(base + (size_t)(lane >> 2) * KV_HEAD + (lane & 3) * 304 + 18 * 16) = ones8();
    }
  }
  STAMP_END(0);
}

DI void spin_until(const unsigned* cnt, unsigned target) {
  for (unsigned spins = 0; spins < (1u << 24); ++spins) {
    if (__hip_atomic_load(cnt, __ATOMIC_RELAXED, __HIP_MEMORY_SCOPE_AGENT) >= target) break;
    __builtin_amdgcn_s_sleep(1);
  }
}
template <int KT> using CR = Cfg<64, 5, 4, 8, B_TILED, KT, false>;
constexpr int ATT_LDS = 152 * 1024;
__global__ __launch_bounds__(512, 2) void k_attn(char* __restrict__ ws, const u16* __restrict__ wsp, const u16* __restrict__ x) {
  extern __shared__ __attribute__((aligned(16))) char smem[];
  const int tid = threadIdx.x, lane = tid & 63, w = rfl(tid >> 6);
  const int b = blockIdx.x, xcd = b & 7, idx = b >> 3;
  const int head = xcd * 4 + (idx & 3), qb = idx >> 2;
  char* ws_dbg = ws; STAMP(1, 0);
  const int q31 = lane & 31, h = lane >> 5;
  const char* qp = ws + WS_Q + (size_t)head * Q_HEAD + (size_t)(qb * 8 + w) * QK_TILE;
  bf16x8 qf1; unsigned q2;
  asm volatile("global_load_dwordx4 %0, %1, off" : "=v"(qf1) : "v"(qp + lane * 16));
  asm volatile("global_load_dword %0, %1, off" : "=v"(q2) : "v"(qp + 1024 + q31 * 4));
  {
    __amdgpu_buffer_rsrc_t srd = mksrd(ws + WS_KV + (size_t)head * KV_HEAD, KV_HEAD);
    const unsigned ldsb = (unsigned)(size_t)smem + w * 1024, vo = lane * 16 + w * 1024;
#pragma unroll
    for (int j = 0; j < 19; ++j) DMAQ(ldsb, j * 8192, vo, srd, (unsigned)(j * 8192));
  }
  asm volatile("s_waitcnt vmcnt(16)" : "+v"(qf1), "+v"(q2) :: "memory");
  BAR();
  u32x4 q2v = {h == 0 ? q2 : 0u, 0u, 0u, 0u};
  const bf16x8 qf2 = __builtin_bit_cast(bf16x8, q2v);
  const int dcl = q31 < 19 ? q31 : (q31 >= 20 && q31 < 28 ? 12 : 16);
  const int koff = lane * 16, k2off = 1024 + q31 * 4, voff = V_OFF + h * 304 + dcl * 16;
  f32x16 o;
#pragma unroll
  for (int r = 0; r < 16; ++r) o[r] = 0.f;
  auto scores = [&](int t, const f32x16& c) {
    const char* tp = smem + t * KV_TILE;
    const bf16x8 k1 = *(const bf16x8*)(tp + koff);
    u32x4 k2v = {*(const unsigned*)(tp + k2off), 0u, 0u, 0u};
    f32x16 s = MFMA32(k1, qf1, c);
    return MFMA32(__builtin_bit_cast(bf16x8, k2v), qf2, s);
  };
  f32x16 zero;
#pragma unroll
  for (int r = 0; r < 16; ++r) zero[r] = 0.f;
  STAMP(1, 1);
  unsigned dummy_keep;
  f32x16 negm;
  {
    const f32x16 s = scores(0, zero);
    float m = s[0];
#pragma unroll
    for (int r = 1; r < 16; ++r) m = fmaxf(m, s[r]);
    m = fmaxf(m, xor32(m));
#pragma unroll
    for (int r = 0; r < 16; ++r) negm[r] = -m;
  }
  {
    bf16x8 k1, v0, v1; u32x4 k2 = {0u, 0u, 0u, 0u};
    f32x16 sA = scores(0, negm), sB;
    k1 = *(const bf16x8*)(smem + KV_TILE + koff); k2[0] = *(const unsigned*)(smem + KV_TILE + k2off);
    v0 = *(const bf16x8*)(smem + voff); v1 = *(const bf16x8*)(smem + voff + 608);
    auto step = [&](int t, f32x16& sc, f32x16& sn) {
      const char* tp = smem + t * KV_TILE;
      u32x4 p0, p1;
      sn = MFMA32(k1, qf1, negm);
      k1 = *(const bf16x8*)(tp + 2 * KV_TILE + koff);
#pragma unroll
      for (int e = 0; e < 4; ++e) sc[e] = __builtin_amdgcn_exp2f(sc[e]);
      SB();
      sn = MFMA32(__builtin_bit_cast(bf16x8, k2), qf2, sn);
      k2[0] = *(const unsigned*)(tp + 2 * KV_TILE + k2off);
#pragma unroll
      for (int e = 4; e < 8; ++e) sc[e] = __builtin_amdgcn_exp2f(sc[e]);
#pragma unroll
      for (int e = 0; e < 4; ++e) p0[e] = cvtpk(sc[2 * e], sc[2 * e + 1]);
      SB();
      o = MFMA32(v0, __builtin_bit_cast(bf16x8, p0), o);
      v0 = *(const bf16x8*)(tp + KV_TILE + voff);
#pragma unroll
      for (int e = 8; e < 16; ++e) sc[e] = __builtin_amdgcn_exp2f(sc[e]);
#pragma unroll
      for (int e = 0; e < 4; ++e) p1[e] = cvtpk(sc[8 + 2 * e], sc[8 + 2 * e + 1]);
      SB();
      o = MFMA32(v1, __builtin_bit_cast(bf16x8, p1), o);
      v1 = *(const bf16x8*)(tp + KV_TILE + voff + 608);
      SB();
    };
    unsigned dummy = 0;
    auto warm = [&]() { const unsigned T = b * 512 + tid; TOUCH(dummy, (const char*)wsp + (size_t)(T < 65536u ? T : 0u) * 128); };
#define CHUNK(c, nv) { if (c > 0) { WAITV(nv); BAR(); } if (c == 3) warm(); \
    for (int t = 8 * c; t < 8 * c + 8; t += 2) { step(t, sA, sB); step(t + 1, sB, sA); } }
    CHUNK(0, 16) CHUNK(1, 13) CHUNK(2, 11) CHUNK(3, 9) CHUNK(4, 7) CHUNK(5, 5) CHUNK(6, 3) CHUNK(7, 1)
#undef CHUNK
    VREG(dummy);
    dummy_keep = dummy;
  }
  STAMP(1, 2);
  float l = __shfl(o[10], q31);
  bool ok = l < 1.8446744e19f;
#pragma unroll
  for (int r = 0; r < 12; ++r) ok = ok && (fabsf(o[r]) <= 3.0e38f);
  if (!__all(ok)) {
    float m = -3.0e38f;
#pragma unroll
    for (int r = 0; r < 16; ++r) o[r] = 0.f;
    for (int t = 0; t < 64; ++t) {
      f32x16 s = scores(t, zero);
      float tmax = s[0];
#pragma unroll
      for (int r = 1; r < 16; ++r) tmax = fmaxf(tmax, s[r]);
      tmax = fmaxf(tmax, xor32(tmax));
      const float mn = fmaxf(m, tmax), alpha = __builtin_amdgcn_exp2f(m - mn);
      m = mn;
#pragma unroll
      for (int r = 0; r < 16; ++r) { o[r] *= alpha; s[r] = __builtin_amdgcn_exp2f(s[r] - mn); }
      const char* tp = smem + t * KV_TILE + voff;
      const bf16x8 v0 = *(const bf16x8*)(tp), v1 = *(const bf16x8*)(tp + 608);
      u32x4 p0, p1;
#pragma unroll
      for (int e = 0; e < 4; ++e) { p0[e] = cvtpk(s[2 * e], s[2 * e + 1]); p1[e] = cvtpk(s[8 + 2 * e], s[8 + 2 * e + 1]); }
      o = MFMA32(v0, __builtin_bit_cast(bf16x8, p0), o);
      o = MFMA32(v1, __builtin_bit_cast(bf16x8, p1), o);
    }
    l = __shfl(o[10], q31);
  }
  const float inv = 1.f / l;
  {
    unsigned a0 = cvtpk(o[0] * inv, o[1] * inv), a1 = cvtpk(o[2] * inv, o[3] * inv), b0 = cvtpk(o[4] * inv, o[5] * inv), b1 = cvtpk(o[6] * inv, o[7] * inv);
    { auto r = __builtin_amdgcn_permlane32_swap(a0, b0, false, false); a0 = r[0]; b0 = r[1]; }
    { auto r = __builtin_amdgcn_permlane32_swap(a1, b1, false, false); a1 = r[0]; b1 = r[1]; }
    const __amdgpu_buffer_rsrc_t so = mksrd(ws + WS_AO + (size_t)(qb * 32 + head) * 8192, 8192);
    __builtin_amdgcn_raw_buffer_store_b128(u32x4{a0, a1, b0, b1}, so, (w * 32 + q31) * 32 + h * 16, 0, 16);
    const __amdgpu_buffer_rsrc_t s2 = mksrd(ws + WS_AO + (size_t)S_ * 1024, S_ * 128);
    if (h == 0) __builtin_amdgcn_raw_buffer_store_b32(cvtpk(o[8] * inv, o[9] * inv), s2, (qb * 256 + w * 32 + q31) * 128 + head * 4, 0, 16);
  }
  asm volatile("s_waitcnt vmcnt(0)" : "+v"(dummy_keep) :: "memory");
  WAITL0(); BAR();
  STAMP(2, 0); STAMP(1, 3);
  using C = CR<9>;
  unsigned* cnt = (unsigned*)(ws + WS_CNT + (32 + qb) * 128);
  if (w == 0 && lane == 0) __hip_atomic_fetch_add(cnt, 1u, __ATOMIC_RELAXED, __HIP_MEMORY_SCOPE_AGENT);
  if (w >= 6) return;
  const int row0 = qb * 256 + (head >> 3) * 64, tn = head & 7;
  if (w >= 4) {
    const int p = w - 4;
    const __amdgpu_buffer_rsrc_t srdA = mksrd(ws + WS_AO, S_ * 1152), srdB = mksrd(ws + WS_WO + (size_t)tn * 9 * WR_TB, 9 * WR_TB);
    const unsigned ldsb = (unsigned)(size_t)smem, mA = ldsb + p * 4096, mB = ldsb + C::ABYTES + p * 5120;
    const int c0 = (lane & 7) ^ (lane >> 4), c1 = (lane & 7) ^ (4 + (lane >> 4));
    const int rl = (head >> 3) * 64 + p * 32 + (lane >> 3);
    const unsigned va0 = (unsigned)(qb * 32 + (c0 >> 1)) * 8192 + rl * 32 + (c0 & 1) * 16, va1 = (unsigned)(qb * 32 + (c1 >> 1)) * 8192 + rl * 32 + (c1 & 1) * 16;
    const unsigned vt0 = S_ * 1024 + (qb * 256 + rl) * 128 + c0 * 16, vt1 = S_ * 1024 + (qb * 256 + rl) * 128 + c1 * 16, vb = p * 5120 + lane * 16;
    auto issueA = [&](int kt) {
      const unsigned sa = mA + (kt & 7) * C::STAGE;
      if (kt < 8) {
#pragma unroll
        for (int j = 0; j < 4; ++j) { const unsigned v = ((j & 1) ? va1 : va0) + j * 8 * 32; DMAQ_SC1(sa, j * 1024, v, srdA, (unsigned)(kt * 4 * 8192)); }
      } else {
#pragma unroll
        for (int j = 0; j < 4; ++j) { const unsigned v = ((j & 1) ? vt1 : vt0) + j * 8 * 128; DMAQ_SC1(sa, j * 1024, v, srdA, 0u); }
      }
    };
    auto issueB = [&](int kt) {
      const unsigned sb = mB + (kt & 7) * C::STAGE, sk = kt * WR_TB;
#pragma unroll
      for (int j = 0; j < 5; ++j) { const unsigned v = vb + j * 1024; DMAQ(sb, j * 1024, v, srdB, sk); }
    };
#pragma unroll
    for (int s = 0; s < 7; ++s) issueB(s);
    if (lane == 0) spin_until(cnt, 32u);
    asm volatile("" ::: "memory");
#pragma unroll
    for (int s = 0; s < 7; ++s) issueA(s);
    WAITV(24); BAR(); issueA(7); issueB(7);
    WAITV(29); BAR(); issueA(8); issueB(8);
    WAITV(34); BAR(); WAITV(30); BAR(); WAITV(26); BAR(); WAITV(22); BAR(); WAITV(18); BAR(); WAITV(9); BAR(); WAITV(0); BAR();
    BAR();
    return;
  }
  {
    const int g = lane >> 4, fr = lane & 15;
    const int tok = row0 + w * 16 + fr;
    const size_t ro = (size_t)tok * D_ + tn * 72 + 4 * g;
    u32x2 r[5];
#pragma unroll
    for (int j = 0; j < 5; ++j) r[j] = *(const u32x2*)(x + ro + ((j < 4 || g < 2) ? 16 * j : 0));
    f32x4 acc[1][5]; float rs[1], rq[1];
    consumer<C, 1, 5, 0, false, false>(smem, w * 16, lane, acc, rs, rq, ws, 7, 0, NoHook());
    u16* out = (u16*)(ws + WS_H);
    float s1 = 0.f, s2 = 0.f;
#pragma unroll
    for (int j = 0; j < 5; ++j) {
      if (j < 4 || g < 2) {
        u32x2 v;
        const unsigned p0 = cvtpk(acc[0][j][0], acc[0][j][1]), p1 = cvtpk(acc[0][j][2], acc[0][j][3]);
        v[0] = cvtpk(bflo(r[j][0]) + bflo(p0), bfhi(r[j][0]) + bfhi(p0));
        v[1] = cvtpk(bflo(r[j][1]) + bflo(p1), bfhi(r[j][1]) + bfhi(p1));
        *(u32x2*)(out + ro + 16 * j) = v;
        const float a0 = bflo(v[0]), a1 = bfhi(v[0]), a2 = bflo(v[1]), a3 = bfhi(v[1]);
        s1 += (a0 + a1) + (a2 + a3);
        s2 = fmaf(a0, a0, fmaf(a1, a1, fmaf(a2, a2, fmaf(a3, a3, s2))));
      }
    }
    s1 += xor16(s1); s1 += xor32(s1); s2 += xor16(s2); s2 += xor32(s2);
    if (g == 0) *(f32x2*)(ws + WS_HST + ((size_t)tok * 8 + tn) * 8) = f32x2{s1, s2};
  }
  STAMP_END(2);
}

constexpr int MST = 36864, MNST = 4;
constexpr int M_CSA = MNST * MST, M_CSB = M_CSA + 576, M_RS = M_CSB + 576, M_S2 = M_RS + 1024, M_LDS = M_S2 + 1024;
__global__ __launch_bounds__(448, 2) void k_mix(const u16* __restrict__ wsp, const u16* __restrict__ bsp, char* __restrict__ ws, u16* __restrict__ outp) {
  extern __shared__ __attribute__((aligned(16))) char smem[];
  const int tid = threadIdx.x, w = rfl(tid >> 6);
  const int b = blockIdx.x, xcd = b & 7, idx = b >> 3;
  const int tm = (xcd & 1) * 8 + (idx & 7), tn = (xcd >> 1) * 4 + (idx >> 3);
  char* ws_dbg = ws;
  unsigned* cnt1 = (unsigned*)(ws + WS_CNT + tm * 128);
  unsigned* cnt2 = (unsigned*)(ws + WS_CNT + (16 + tn) * 128);
  unsigned* cnt3 = (unsigned*)(ws + WS_CNT + (40 + tm) * 128);
  using CD = CR<36>;
  const int drow0 = tm * 128 + (tn >> 3) * 64, dtn = tn & 7;
  if (__builtin_expect(w == 4 || w == 5, 1)) {
    const int lane = tid & 63, p = w - 4;
    if (p == 0) STAMPW(3, 4);
    const unsigned ldsb = (unsigned)(size_t)smem;
    const unsigned mA = ldsb + p * 8192, mB = ldsb + 16384 + p * 9216;
    const unsigned sw0 = ((lane & 7) ^ (lane >> 4)) << 4, sw1 = ((lane & 7) ^ (4 + (lane >> 4))) << 4;
    const int g = lane >> 4;
    constexpr int NQ = 17;
    {
      const __amdgpu_buffer_rsrc_t srdA = mksrd((const u16*)(ws + WS_H) + (size_t)tm * 128 * D_, 128 * D_ * 2), srdB = mksrd(ws + WS_WIN, 32 * 9 * WIN_TB);
      const unsigned va0 = (p * 64 + (lane >> 3)) * (D_ * 2) + sw0, va1 = (p * 64 + (lane >> 3)) * (D_ * 2) + sw1, vb = p * 9216 + lane * 16;
      auto issue = [&](int s) {
        const int ph = s >= 9, kt = s - 9 * ph;
        const unsigned so = (s & 3) * MST, sa = mA + so, sb = mB + so;
        const unsigned soA = kt * 128, soB = ((ph ? tn : tn + 16) * 9 + kt) * WIN_TB;
#pragma unroll
        for (int j = 0; j < 8; ++j) { const unsigned v = ((j & 1) ? va1 : va0) + j * 8 * (D_ * 2); DMAQ(sa, j * 1024, v, srdA, soA); }
#pragma unroll
        for (int j = 0; j < 9; ++j) { const unsigned v = vb + j * 1024; DMAQ(sb, j * 1024, v, srdB, soB); }
      };
      issue(0); issue(1); issue(2);
      if (p == 0) STAMPW(3, 5);
      for (int s = 0; s < 15; ++s) { WAITV(2 * NQ); BAR(); issue(s + 3); }
      WAITV(2 * NQ); BAR(); WAITV(NQ); BAR(); WAITV(0); BAR();
    }
    BAR();
    BAR();
    if (p == 0) STAMPW(5, 0);
    {
      const __amdgpu_buffer_rsrc_t srdA = mksrd(wsp + (size_t)tm * 128 * S_, 128 * S_ * 2), srdB = mksrd((const u16*)(ws + WS_Z2N) + (size_t)tn * 144 * S_, 144 * S_ * 2);
      const unsigned va0 = (p * 64 + (lane >> 3)) * (S_ * 2) + sw0, va1 = (p * 64 + (lane >> 3)) * (S_ * 2) + sw1;
      const unsigned vb0 = (p * 72 + (lane >> 3)) * (S_ * 2) + (p ? sw1 : sw0), vb1 = (p * 72 + (lane >> 3)) * (S_ * 2) + (p ? sw0 : sw1);
      auto issueA = [&](int kt) {
        const unsigned sa = mA + (kt & 3) * MST, sk = kt * 128;
#pragma unroll
        for (int j = 0; j < 8; ++j) { const unsigned v = ((j & 1) ? va1 : va0) + j * 8 * (S_ * 2); DMAQ(sa, j * 1024, v, srdA, sk); }
      };
      auto issueB = [&](int kt) {
        const unsigned sb = mB + (kt & 3) * MST, sk = kt * 128;
#pragma unroll
        for (int j = 0; j < 9; ++j) { const unsigned v = ((j & 1) ? vb1 : vb0) + j * 8 * (S_ * 2); DMAQ_SC1(sb, j * 1024, v, srdB, sk); }
      };
      auto issue = [&](int kt) { issueA(kt); issueB(kt); };
      issueA(0); issueA(1); issueA(2);
      if (lane == 0) spin_until(cnt2, 16u);
      asm volatile("" ::: "memory");
      if (p == 0) STAMPW(5, 1);
      if (p == 0) STAMPW(5, 2);
      issueB(0); issueB(1); issueB(2);
      if (p == 0) STAMPW(5, 3);
      WAITV(18); BAR(); issue(3);
      WAITV(9 + NQ); BAR(); issue(4);
      for (int kt = 2; kt < 29; ++kt) { WAITV(2 * NQ); BAR(); issue(kt + 3); }
      WAITV(2 * NQ); BAR(); WAITV(NQ); BAR(); WAITV(0); BAR();
    }
    BAR();
    BAR();
    {
      const __amdgpu_buffer_rsrc_t srdA = mksrd((const u16*)(ws + WS_U) + (size_t)drow0 * FH, 64 * FH * 2), srdB = mksrd(ws + WS_WOUT + (size_t)dtn * 36 * WR_TB, 36 * WR_TB);
      const unsigned mA2 = ldsb + p * 4096, mB2 = ldsb + CD::ABYTES + p * 5120;
      const unsigned va0 = (p * 32 + (lane >> 3)) * (FH * 2) + sw0, va1 = (p * 32 + (lane >> 3)) * (FH * 2) + sw1, vb = p * 5120 + lane * 16;
      auto issueA = [&](int kt) {
        const unsigned sa = mA2 + (kt & 7) * CD::STAGE, sk = kt * 128;
#pragma unroll
        for (int j = 0; j < 4; ++j) { const unsigned v = ((j & 1) ? va1 : va0) + j * 8 * (FH * 2); DMAQ_SC1(sa, j * 1024, v, srdA, sk); }
      };
      auto issueB = [&](int kt) {
        const unsigned sb = mB2 + (kt & 7) * CD::STAGE, sk = kt * WR_TB;
#pragma unroll
        for (int j = 0; j < 5; ++j) { const unsigned v = vb + j * 1024; DMAQ(sb, j * 1024, v, srdB, sk); }
      };
#pragma unroll
      for (int s = 0; s < 7; ++s) issueB(s);
      if (lane == 0) spin_until(cnt3, 16u);
      asm volatile("" ::: "memory");
#pragma unroll
      for (int s = 0; s < 7; ++s) issueA(s);
      WAITV(24); BAR(); issueA(7); issueB(7);
      WAITV(29); BAR(); issueA(8); issueB(8);
      WAITV(34); BAR(); issueA(9); issueB(9);
      WAITV(39); BAR(); issueA(10); issueB(10);
      WAITV(44); BAR(); issueA(11); issueB(11);
      WAITV(49); BAR(); issueA(12); issueB(12);
      for (int kt = 6; kt < 29; ++kt) { WAITV(54); BAR(); issueA(kt + 7); issueB(kt + 7); }
      WAITV(54); BAR(); WAITV(45); BAR(); WAITV(36); BAR(); WAITV(27); BAR(); WAITV(18); BAR(); WAITV(9); BAR(); WAITV(0); BAR();
      BAR();
    }
    return;
  }
  if (w == 6) {
    const int lane = tid & 63;
    f32x2* rst = (f32x2*)(smem + M_RS);
#pragma unroll
    for (int i = 0; i < 2; ++i) {
      const f32x4* q = (const f32x4*)(ws + WS_HST + (size_t)(tm * 128 + 64 * i + lane) * 64);
      const f32x4 a = q[0], bb = q[1], c = q[2], d = q[3];
      const float s1 = (a[0] + a[2]) + (bb[0] + bb[2]) + (c[0] + c[2]) + (d[0] + d[2]), s2 = (a[1] + a[3]) + (bb[1] + bb[3]) + (c[1] + c[3]) + (d[1] + d[3]);
      rst[64 * i + lane] = ln_stats(s1, s2, 1.f / D_);
    }
    if (lane < 36) {
      *(f32x4*)(smem + M_CSA + lane * 16) = *(const f32x4*)(ws + WS_CS + (size_t)((tn + 16) * 144 + lane * 4) * 4);
      *(f32x4*)(smem + M_CSB + lane * 16) = *(const f32x4*)(ws + WS_CS + (size_t)(tn * 144 + lane * 4) * 4);
    }
    WAITL0();
    for (int s = 0; s < 15; ++s) BAR();
    if (lane == 0) spin_until(cnt1, 16u);
    asm volatile("" ::: "memory");
    const __amdgpu_buffer_rsrc_t srdS = mksrd(ws + WS_ST + (size_t)tm * (16 * 128 * 8), 16 * 128 * 8);
    u32x4 pv[16];
#pragma unroll
    for (int q = 0; q < 16; ++q) pv[q] = __builtin_amdgcn_raw_buffer_load_b128(srdS, (q * 128 + 2 * lane) * 8, 0, 16);
    SB(); BAR(); BAR(); SB();
    f32x4 t4 = {0.f, 0.f, 0.f, 0.f};
#pragma unroll
    for (int q = 0; q < 16; ++q) t4 += __builtin_bit_cast(f32x4, pv[q]);
    {
      const f32x2 q0 = ln_stats(t4[0], t4[1], 1.f / FH), q1 = ln_stats(t4[2], t4[3], 1.f / FH);
      *(f32x4*)(smem + M_S2 + lane * 16) = f32x4{q0[1], -q0[0] * q0[1], q1[1], -q1[0] * q1[1]};
    }
    WAITL0();
    BAR();
    BAR();
    return;
  }
  const int lane = tid & 63, g = lane >> 4, fr = lane & 15, f = fr >> 1;
  const int l0 = fr * 128 + ((g ^ f) << 4), l1 = fr * 128 + (((4 + g) ^ f) << 4);
  const int tb = 16384 + (16 * (g >> 1) + 4 * (g & 1) + (fr >> 2)) * 288 + (lane & 3) * 8;
  const int tok0 = tm * 128 + w * 32;
  f32x4 acc[2][9];
  bf16x8 a0[2], b0[9], a1[2], b1[9];
  auto zero_acc = [&]() {
#pragma unroll
    for (int i = 0; i < 2; ++i)
#pragma unroll
      for (int j = 0; j < 9; ++j) acc[i][j] = f32x4{0.f, 0.f, 0.f, 0.f};
  };
  auto mm = [&](bf16x8* a, bf16x8* bb) {
#pragma unroll
    for (int j = 0; j < 9; ++j)
#pragma unroll
      for (int i = 0; i < 2; ++i) acc[i][j] = MFMA16(bb[j], a[i], acc[i][j]);
  };
  auto rdKN = [&](bf16x8* a, bf16x8* bb, int s, int ks) {
    const char* sb = smem + (s & 3) * MST;
#pragma unroll
    for (int i = 0; i < 2; ++i) a[i] = *(const bf16x8*)(sb + w * 4096 + i * 2048 + (ks ? l1 : l0));
#pragma unroll
    for (int j = 0; j < 9; ++j) { const char* q = sb + tb + ks * (32 * 288) + j * 32; bb[j] = cat8(trread(q), trread(q + 8 * 288)); }
  };
  auto rdNK = [&](bf16x8* a, bf16x8* bb, int s, int ks) {
    const char* sb = smem + (s & 3) * MST + (ks ? l1 : l0);
#pragma unroll
    for (int i = 0; i < 2; ++i) a[i] = *(const bf16x8*)(sb + w * 4096 + i * 2048);
#pragma unroll
    for (int j = 0; j < 9; ++j) bb[j] = *(const bf16x8*)(sb + 16384 + j * 2048);
  };
#define SCHED(id, ND, RPG) _Pragma("unroll") for (int q = 0; q < 18; ++q) { __builtin_amdgcn_sched_group_barrier(0x008, 1, id); \
    if (q * RPG < ND) __builtin_amdgcn_sched_group_barrier(0x080, RPG, id); }
  auto gelu = [&](u32x2 (&zp)[2][9], int csoff, f32x2 (&sum)[2]) {
    const float c1 = -2.f * 0.7978845608028654f * 1.4426950408889634f, c2 = c1 * 0.044715f;
    const float* cs = (const float*)(smem + csoff);
    const f32x2* rst = (const f32x2*)(smem + M_RS) + w * 32;
    float nmu[2], k1[2], k3[2], irs[2];
    f32x2 s1[2], s2[2];
#pragma unroll
    for (int i = 0; i < 2; ++i) {
      const f32x2 st = rst[16 * i + fr];
      nmu[i] = -st[0]; k1[i] = c1 * st[1]; k3[i] = c2 * st[1] * st[1] * st[1]; irs[i] = __builtin_amdgcn_rcpf(st[1]);
      s1[i] = f32x2{0.f, 0.f}; s2[i] = f32x2{0.f, 0.f};
    }
    f32x4 c4 = *(const f32x4*)(cs + 4 * g);
#pragma unroll
    for (int j = 0; j < 9; ++j) {
      const f32x4 c4n = *(const f32x4*)(cs + 16 * (j < 8 ? j + 1 : 8) + 4 * g);
      SB();
#pragma unroll
      for (int i = 0; i < 2; ++i)
#pragma unroll
        for (int hh = 0; hh < 2; ++hh) {
          const f32x2 a2 = {acc[i][j][2 * hh], acc[i][j][2 * hh + 1]}, c2v = {c4[2 * hh], c4[2 * hh + 1]};
          const f32x2 t = f32x2{nmu[i], nmu[i]} * c2v + a2;
          const f32x2 y = t * (t * t * f32x2{k3[i], k3[i]} + f32x2{k1[i], k1[i]});
          const f32x2 e = {__builtin_amdgcn_exp2f(y[0]), __builtin_amdgcn_exp2f(y[1])};
          const f32x2 d = e * f32x2{irs[i], irs[i]} + f32x2{irs[i], irs[i]};
          const f32x2 z = t * f32x2{__builtin_amdgcn_rcpf(d[0]), __builtin_amdgcn_rcpf(d[1])};
          s1[i] += z; s2[i] += z * z;
          zp[i][j][hh] = cvtpk(z[0], z[1]);
        }
      VREG(zp[0][j]); VREG(zp[1][j]);
      c4 = c4n;
      SB();
    }
#pragma unroll
    for (int i = 0; i < 2; ++i) sum[i] = f32x2{s1[i][0] + s1[i][1], s2[i][0] + s2[i][1]};
  };
  const __amdgpu_buffer_rsrc_t srdS = mksrd(ws + WS_ST + (size_t)tm * (16 * 128 * 8), 16 * 128 * 8);
  zero_acc();
  STAMP(3, 0);
  BAR();
  STAMP(3, 1);
  rdKN(a0, b0, 0, 0);
  u32x2 z2p[2][9];
  auto stageAB = [&](int s) {
    SB(); rdKN(a1, b1, s, 1); mm(a0, b0); SCHED(0, 20, 2) SB();
    WAITL0(); BAR();
    SB(); rdKN(a0, b0, s + 1, 0); mm(a1, b1); SCHED(1, 20, 2)
  };
  for (int s = 0; s < 9; ++s) stageAB(s);
  SB();
  STAMP(5, 6);
  {
    f32x2 sum[2];
    gelu(z2p, M_CSA, sum);
#pragma unroll
    for (int i = 0; i < 2; ++i) {
      f32x2 v = sum[i];
      v[0] += xor16(v[0]); v[0] += xor32(v[0]); v[1] += xor16(v[1]); v[1] += xor32(v[1]);
      if (g == 0) __builtin_amdgcn_raw_buffer_store_b64(__builtin_bit_cast(u32x2, v), srdS, (tn * 128 + w * 32 + 16 * i + fr) * 8, 0, 16);
    }
    asm volatile("s_waitcnt vmcnt(0)" ::: "memory");
    zero_acc();
  }
  SB();
  STAMP(5, 4);
  stageAB(9);
  if (w == 0 && lane == 0) __hip_atomic_fetch_add(cnt1, 1u, __ATOMIC_RELAXED, __HIP_MEMORY_SCOPE_AGENT);
  for (int s = 10; s < 17; ++s) stageAB(s);
  SB(); rdKN(a1, b1, 17, 1); mm(a0, b0); mm(a1, b1); SB();
  WAITL0(); BAR();
  STAMP(3, 2);
  {
    const f32x2* st2 = (const f32x2*)(smem + M_S2) + w * 32;
    char* reg = smem + w * (32 * 304);
#pragma unroll
    for (int i = 0; i < 2; ++i) {
      const f32x2 st = st2[16 * i + fr];
#pragma unroll
      for (int j = 0; j < 9; ++j) {
        u32x2 v;
        v[0] = cvtpk(fmaf(bflo(z2p[i][j][0]), st[0], st[1]), fmaf(bfhi(z2p[i][j][0]), st[0], st[1]));
        v[1] = cvtpk(fmaf(bflo(z2p[i][j][1]), st[0], st[1]), fmaf(bfhi(z2p[i][j][1]), st[0], st[1]));
        *(u32x2*)(reg + (16 * i + fr) * 304 + (16 * j + 4 * g) * 2) = v;
      }
    }
    WAITL0();
    const __amdgpu_buffer_rsrc_t so = mksrd((u16*)(ws + WS_Z2N) + (size_t)(tn * 144) * S_ + tok0, 144 * S_ * 2);
    const char* rp = reg + (8 * g + (fr >> 2)) * 304 + (lane & 3) * 8;
#pragma unroll
    for (int j = 0; j < 9; ++j)
      __builtin_amdgcn_raw_buffer_store_b128(__builtin_bit_cast(u32x4, cat8(trread(rp + j * 32), trread(rp + 4 * 304 + j * 32))), so, (j * 16 + fr) * (S_ * 2) + 8 * g * 2, 0, 16);
    asm volatile("s_waitcnt vmcnt(0)" ::: "memory");
    WAITL0(); BAR();
    if (w == 0 && lane == 0) __hip_atomic_fetch_add(cnt2, 1u, __ATOMIC_RELAXED, __HIP_MEMORY_SCOPE_AGENT);
  }
  u32x2 z1p[2][9];
  { f32x2 sum[2]; gelu(z1p, M_CSB, sum); }
  const unsigned bias0 = bsp[tok0 + fr], bias1 = bsp[tok0 + 16 + fr];
  zero_acc();
  STAMP(4, 0);
  BAR();
  STAMP(4, 1);
  rdNK(a0, b0, 0, 0);
  for (int kt = 0; kt < 31; ++kt) {
    SB(); rdNK(a1, b1, kt, 1); mm(a0, b0); SCHED(0, 11, 1) SB();
    WAITL0(); BAR();
    SB(); rdNK(a0, b0, kt + 1, 0); mm(a1, b1); SCHED(1, 11, 1)
  }
  SB(); rdNK(a1, b1, 31, 1); mm(a0, b0); mm(a1, b1); SB();
  WAITL0(); BAR();
  SB();
  STAMP(4, 2);
#undef SCHED
  char* reg = smem + w * (32 * 304);
#pragma unroll
  for (int i = 0; i < 2; ++i) {
    const float bi = bflo(i ? bias1 : bias0);
#pragma unroll
    for (int j = 0; j < 9; ++j) {
      u32x2 v;
      const unsigned e0 = cvtpk(acc[i][j][0], acc[i][j][1]), e1 = cvtpk(acc[i][j][2], acc[i][j][3]);
      const unsigned g0 = cvtpk(bflo(e0) + bi, bfhi(e0) + bi), g1 = cvtpk(bflo(e1) + bi, bfhi(e1) + bi);
      v[0] = cvtpk(bflo(z1p[i][j][0]) * bflo(g0), bfhi(z1p[i][j][0]) * bfhi(g0));
      v[1] = cvtpk(bflo(z1p[i][j][1]) * bflo(g1), bfhi(z1p[i][j][1]) * bfhi(g1));
      *(u32x2*)(reg + (16 * i + fr) * 304 + (16 * j + 4 * g) * 2) = v;
    }
  }
  WAITL0();
  const __amdgpu_buffer_rsrc_t so = mksrd((u16*)(ws + WS_U) + (size_t)tok0 * FH + tn * 144, 32 * FH * 2);
#pragma unroll
  for (int i = 0; i < 9; ++i) {
    const int c = i * 64 + lane, row = c / 18, ch = c - row * 18;
    __builtin_amdgcn_raw_buffer_store_b128(*(const u32x4*)(reg + row * 304 + ch * 16), so, row * (FH * 2) + ch * 16, 0, 16);
  }
  asm volatile("s_waitcnt vmcnt(0)" ::: "memory");
  WAITL0(); BAR();
  STAMP(4, 3);
  if (w == 0 && lane == 0) __hip_atomic_fetch_add(cnt3, 1u, __ATOMIC_RELAXED, __HIP_MEMORY_SCOPE_AGENT);
  {
    const int tok = drow0 + w * 16 + fr;
    const size_t ro = (size_t)tok * D_ + dtn * 72 + 4 * g;
    const u16* res = (const u16*)(ws + WS_H);
    u32x2 r[5];
#pragma unroll
    for (int j = 0; j < 5; ++j) r[j] = *(const u32x2*)(res + ro + ((j < 4 || g < 2) ? 16 * j : 0));
    f32x4 acd[1][5]; float rs[1], rq[1];
    consumer<CD, 1, 5, 0, false, false>(smem, w * 16, lane, acd, rs, rq, ws, 6, 0, NoHook());
#pragma unroll
    for (int j = 0; j < 5; ++j) {
      if (j < 4 || g < 2) {
        const unsigned p0 = cvtpk(acd[0][j][0], acd[0][j][1]), p1 = cvtpk(acd[0][j][2], acd[0][j][3]);
        u32x2 v;
        v[0] = cvtpk(bflo(r[j][0]) + bflo(p0), bfhi(r[j][0]) + bfhi(p0));
        v[1] = cvtpk(bflo(r[j][1]) + bflo(p1), bfhi(r[j][1]) + bfhi(p1));
        *(u32x2*)(outp + ro + 16 * j) = v;
      }
    }
  }
}

extern "C" void kernel_launch(void* const* d_in, const int* in_sizes, int n_in, void* d_out, int out_size, void* d_ws, size_t ws_size, hipStream_t stream) {
  const u16* x = (const u16*)d_in[0];
  const u16* wqkv = (const u16*)d_in[1];
  const u16* wo = (const u16*)d_in[2];
  const u16* win = (const u16*)d_in[4];
  const u16* wsp = (const u16*)d_in[5];
  const u16* bsp = (const u16*)d_in[6];
  const u16* wout = (const u16*)d_in[7];
  char* ws = (char*)d_ws;
  (void)hipFuncSetAttribute((const void*)k_qkv, hipFuncAttributeMaxDynamicSharedMemorySize, C1::LDS);
  (void)hipFuncSetAttribute((const void*)k_attn, hipFuncAttributeMaxDynamicSharedMemorySize, ATT_LDS);
  (void)hipFuncSetAttribute((const void*)k_mix, hipFuncAttributeMaxDynamicSharedMemorySize, M_LDS);
  hipLaunchKernelGGL(k_qkv, dim3(256), dim3(512), C1::LDS, stream, x, wqkv, win, wout, wo, ws);
  hipLaunchKernelGGL(k_attn, dim3(256), dim3(512), ATT_LDS, stream, ws, wsp, x);
  hipLaunchKernelGGL(k_mix, dim3(256), dim3(448), M_LDS, stream, wsp, bsp, ws, (u16*)d_out);
}
```

```cpp
#include <hip/hip_runtime.h>
#include <stdint.h>

typedef unsigned short u16;
typedef short bf16x8 __attribute__((ext_vector_type(8)));
typedef short s16x4 __attribute__((ext_vector_type(4)));
typedef float f32x2 __attribute__((ext_vector_type(2)));
typedef float f32x4 __attribute__((ext_vector_type(4)));
typedef float f32x16 __attribute__((ext_vector_type(16)));
typedef unsigned u32x2 __attribute__((ext_vector_type(2)));
typedef unsigned u32x4 __attribute__((ext_vector_type(4)));
typedef __bf16 bf16x2_t __attribute__((ext_vector_type(2)));
typedef __attribute__((address_space(3))) s16x4* lds_s16x4_ptr;
#define DI __device__ __forceinline__

constexpr int S_ = 2048, D_ = 576, H_ = 32, DH = 18, FH = 2304, F2 = 4608;
constexpr int QK_TILE = 1152;
constexpr int V_OFF = 1152;
constexpr int KV_TILE = 1152 + 4 * 304;
constexpr int KV_HEAD = 64 * KV_TILE;
constexpr int Q_HEAD = 64 * QK_TILE;
constexpr int WIN_TB = 64 * 288, WR_TB = 64 * 160;
constexpr size_t WS_Q = 0;
constexpr size_t WS_KV = WS_Q + (size_t)H_ * Q_HEAD;
constexpr size_t WS_AO = WS_KV + (size_t)H_ * KV_HEAD + 8192;
constexpr size_t WS_H = WS_AO + (size_t)S_ * D_ * 2;
constexpr size_t WS_Z1 = WS_H + (size_t)S_ * D_ * 2;
constexpr size_t WS_Z2T = WS_Z1 + (size_t)S_ * FH * 2;
constexpr size_t WS_Z2N = WS_Z2T + (size_t)S_ * FH * 2;
constexpr size_t WS_ST = WS_Z2N + (size_t)S_ * FH * 2;
constexpr size_t WS_CNT = WS_ST + (size_t)S_ * 16 * 2 * 4;
constexpr size_t WS_HST = WS_ST + (size_t)S_ * 32 * 2 * 4;
constexpr size_t WS_U = WS_HST + (size_t)S_ * 8 * 2 * 4;
constexpr size_t WS_WIN = WS_U + (size_t)S_ * FH * 2;
constexpr size_t WS_WOUT = WS_WIN + (size_t)32 * 9 * WIN_TB;
constexpr size_t WS_WO = WS_WOUT + (size_t)8 * 36 * WR_TB;
constexpr size_t WS_CS = WS_WO + (size_t)8 * 9 * WR_TB;
constexpr size_t WS_END = WS_CS + (size_t)F2 * 4;

DI unsigned cvtpk(float lo, float hi) { f32x2 v = {lo, hi}; bf16x2_t b = __builtin_convertvector(v, bf16x2_t); return __builtin_bit_cast(unsigned, b); }
DI float bflo(unsigned u) { return __uint_as_float(u << 16); }
DI float bfhi(unsigned u) { return __uint_as_float(u & 0xffff0000u); }
DI float bfr(float x) { return __uint_as_float(cvtpk(x, x) << 16); }
DI f32x2 ln_stats(float s1, float s2, float inv_n) {
  const float mu = s1 * inv_n, var = fmaxf(s2 * inv_n - mu * mu, 0.f);
  return f32x2{bfr(mu), 1.f / bfr(sqrtf(bfr(bfr(var) + 1e-5f)))};
}
DI unsigned rfl(unsigned x) { return __builtin_amdgcn_readfirstlane(x); }
DI float xor16(float v) { return __shfl_xor(v, 16); }
DI float xor32(float v) { return __shfl_xor(v, 32); }
#define DMAQ(sb, imm, voff, srd, soff) asm volatile("s_add_u32 m0, %0, %1\n\ts_nop 0\n\tbuffer_load_dwordx4 %2, %3, %4 offen lds" :: "s"(sb), "i"(imm), "v"(voff), "s"(srd), "s"(soff) : "scc")
#define DMAQ_SC1(sb, imm, voff, srd, soff) asm volatile("s_add_u32 m0, %0, %1\n\ts_nop 0\n\tbuffer_load_dwordx4 %2, %3, %4 offen sc1 lds" :: "s"(sb), "i"(imm), "v"(voff), "s"(srd), "s"(soff) : "scc")
#define WAITV(n) asm volatile("s_waitcnt vmcnt(%0)" :: "i"(n) : "memory")
#define WAITL0() asm volatile("s_waitcnt lgkmcnt(0)" ::: "memory")
#define BAR() asm volatile("s_barrier" ::: "memory")
#define SB() __builtin_amdgcn_sched_barrier(0)
#define MFMA16(a, b, c) __builtin_amdgcn_mfma_f32_16x16x32_bf16((a), (b), (c), 0, 0, 0)
#define MFMA32(a, b, c) __builtin_amdgcn_mfma_f32_32x32x16_bf16((a), (b), (c), 0, 0, 0)
#define VREG(x) asm volatile("" : "+v"(x))
DI __amdgpu_buffer_rsrc_t mksrd(const void* p, unsigned bytes) { return __builtin_amdgcn_make_buffer_rsrc((void*)p, (short)0, (int)bytes, 0x00020000); }
DI s16x4 trread(const char* p) { return __builtin_amdgcn_ds_read_tr16_b64_v4i16((lds_s16x4_ptr)(p)); }
DI bf16x8 cat8(s16x4 lo, s16x4 hi) { return __builtin_shufflevector(lo, hi, 0, 1, 2, 3, 4, 5, 6, 7); }
DI bf16x8 ones8() { const u32x4 o = {0x3F803F80u, 0x3F803F80u, 0x3F803F80u, 0x3F803F80u}; return __builtin_bit_cast(bf16x8, o); }
#define ROT1 (3 * (((idx >> 2) + (idx & 3)) % 3))
#define ST_AUX 16
DI void st16(__amdgpu_buffer_rsrc_t srd, unsigned off, u32x4 v) { __builtin_amdgcn_raw_buffer_store_b128(v, srd, off, 0, ST_AUX); }
#define TOUCH(dummy, ptr) asm volatile("global_load_dword %0, %1, off" : "+v"(dummy) : "v"(ptr))
#define STAMP(kid, slot)
#define STAMPW(kid, slot)
#define STAMP_END(kid)

enum { B_NK = 0, B_KN = 1, B_TILED = 2 };
template <int BM_, int NT_, int NP_, int NST_, int BMODE_, int KT_, bool COLSUM_>
struct Cfg {
  static constexpr int BM = BM_, NT = NT_, NP = NP_, NST = NST_, KT = KT_, BMODE = BMODE_;
  static constexpr bool BKN = BMODE_ != B_NK, COLSUM = COLSUM_;
  static constexpr int BN = NT * 16;
  static constexpr int NA = BM / 8, NB = BN / 8, NI = NA + NB;
  static constexpr int ABYTES = BM * 128, BBYTES = BN * 128, STAGE = ABYTES + BBYTES;
  static constexpr int RING = NST * STAGE;
  static constexpr int CS_OFF = RING;
  static constexpr int RS_OFF = RING + BN * 4;
  static constexpr int LDS = RS_OFF + BM * 8;
};

template <class C, int TAILN = 0, int TCPR = 1>
DI void run_producer(char* smem, const void* Ab, unsigned abytes, const void* Bb, unsigned bbytes, int lda, int ldb, int p, int lane, char* ws_dbg, int kid,
                     int rot = 0, const void* Tb = nullptr, unsigned tbytes = 0, int ldt = 0) {
  if (p == 0) STAMPW(kid, 4);
  constexpr int HA = C::NA / 2, HB = C::NB / 2, NQ = HA + HB;
  static_assert(C::NA % 4 == 0 && C::NB % 2 == 0, "piece split");
  const __amdgpu_buffer_rsrc_t srdA = mksrd(Ab, abytes), srdB = mksrd(Bb, bbytes);
  const unsigned ldsb = (unsigned)(size_t)smem;
  const unsigned mA = ldsb + p * (HA * 1024), mB = ldsb + C::ABYTES + p * (HB * 1024);
  const unsigned sw0 = ((lane & 7) ^ (lane >> 4)) << 4, sw1 = ((lane & 7) ^ (4 + (lane >> 4))) << 4;
  const unsigned va0 = (p * HA * 8 + (lane >> 3)) * (lda * 2) + sw0, va1 = (p * HA * 8 + (lane >> 3)) * (lda * 2) + sw1;
  unsigned vb[HB];
#pragma unroll
  for (int j = 0; j < HB; ++j) {
    if (C::BMODE == B_NK) {
      const int q = p * HB + j;
      vb[j] = (8 * q + (lane >> 3)) * (ldb * 2) + ((q & 1) ? sw1 : sw0);
    } else if (C::BMODE == B_KN) {
      constexpr int CPR = C::BN / 8;
      const int c = 64 * (p * HB + j) + lane, rho = c / CPR, ch = c - rho * CPR;
      const int k = (rho & ~12) | ((rho & 4) << 1) | ((rho & 8) >> 1);
      vb[j] = k * (ldb * 2) + ch * 16;
    } else {
      vb[j] = (p * HB + j) * 1024 + lane * 16;
    }
  }
  auto issue = [&](int kt) {
    const unsigned so = (kt % C::NST) * C::STAGE, sa = mA + so, sb = mB + so;
    int kr = kt + rot; if (kr >= C::KT) kr -= C::KT;
    const unsigned soA = kr * 128, soB = C::BMODE == B_NK ? kr * 128 : (C::BMODE == B_KN ? kr * 64 * ldb * 2 : kr * C::BBYTES);
#pragma unroll
    for (int j = 0; j < HA; ++j) { const unsigned v = ((j & 1) ? va1 : va0) + j * 8 * (lda * 2); DMAQ(sa, j * 1024, v, srdA, soA); }
#pragma unroll
    for (int j = 0; j < HB; ++j) DMAQ(sb, j * 1024, vb[j], srdB, soB);
  };
  constexpr int PF = C::NST - 1;
#pragma unroll
  for (int s = 0; s < PF; ++s) issue(s);
  if (p == 0) STAMPW(kid, 5);
  constexpr int NJ = C::COLSUM ? (C::NT + 1) / 2 : 0;
  f32x4 cs[NJ > 0 ? NJ : 1];
#pragma unroll
  for (int j = 0; j < NJ; ++j) cs[j] = f32x4{0.f, 0.f, 0.f, 0.f};
  const int g = lane >> 4;
  const int tb = C::ABYTES + (16 * (g >> 1) + 4 * (g & 1) + ((lane & 15) >> 2)) * (C::BN * 2) + (lane & 3) * 8 + p * 32;
  auto colsum = [&](int kt) {
    if constexpr (C::COLSUM) {
      const char* sb = smem + (kt % C::NST) * C::STAGE + tb;
#pragma unroll
      for (int jj = 0; jj < NJ; ++jj) {
#pragma unroll
        for (int ks = 0; ks < 2; ++ks) {
          const char* q = sb + ks * (32 * C::BN * 2) + jj * 64;
          cs[jj] = MFMA16(cat8(trread(q), trread(q + 8 * C::BN * 2)), ones8(), cs[jj]);
        }
      }
      WAITL0();
    }
  };
  for (int kt = 0; kt < C::KT - PF; ++kt) { WAITV((PF - 1) * NQ); BAR(); issue(kt + PF); colsum(kt); }
  constexpr int NTL = TAILN / 4;
  auto tail = [&](int half) {
    const unsigned sb = ldsb + ((C::KT + half) % C::NST) * C::STAGE + p * (NTL * 1024);
#pragma unroll
    for (int j = 0; j < NTL; ++j) {
      const int c = 64 * (half * 2 * NTL + p * NTL + j) + lane, row = c / TCPR, ch = c - row * TCPR;
      const unsigned vo2 = row * ldt + ch * 16;
      DMAQ(sb, j * 1024, vo2, mksrd(Tb, tbytes), 0u);
    }
  };
#pragma unroll
  for (int r = PF - 1; r >= 0; --r) {
    WAITV(r * NQ + (TAILN > 0 ? (r < PF - 1 ? NTL : 0) + (r < PF - 2 ? NTL : 0) : 0)); BAR();
    if (TAILN > 0 && r == PF - 1) tail(0);
    if (TAILN > 0 && r == PF - 2) tail(1);
    colsum(C::KT - 1 - r);
  }
  if (TAILN > 0) WAITV(0);
  if constexpr (C::COLSUM) {
    float* st = (float*)(smem + C::CS_OFF);
#pragma unroll
    for (int jj = 0; jj < NJ; ++jj) {
      if ((lane & 15) == 0 && p + 2 * jj < C::NT) *(f32x4*)(st + 16 * (p + 2 * jj) + 4 * g) = cs[jj];
    }
    WAITL0();
  }
  BAR();
}

struct NoHook { DI void operator()() const {} };
template <class C, int MI, int NTW, int J0, bool ROLL, bool ROWSTAT, class Hook>
DI void consumer(char* smem, int row0, int lane, f32x4 (&acc)[MI][NTW], float (&rs)[MI], float (&rq)[MI], char* ws_dbg, int kid, int hook_kt, Hook hook) {
  const int g = lane >> 4, fr = lane & 15, f = fr >> 1;
  const int la0 = row0 * 128 + fr * 128 + ((g ^ f) << 4), la1 = row0 * 128 + fr * 128 + (((4 + g) ^ f) << 4);
  const int lb0 = C::ABYTES + J0 * 2048 + fr * 128 + ((g ^ f) << 4), lb1 = C::ABYTES + J0 * 2048 + fr * 128 + (((4 + g) ^ f) << 4);
  const int tb = C::ABYTES + J0 * 32 + (16 * (g >> 1) + 4 * (g & 1) + (fr >> 2)) * (C::BN * 2) + (lane & 3) * 8;
  f32x4 ars[MI], arq[MI];
#pragma unroll
  for (int i = 0; i < MI; ++i) {
    ars[i] = f32x4{0.f, 0.f, 0.f, 0.f}; arq[i] = f32x4{0.f, 0.f, 0.f, 0.f};
#pragma unroll
    for (int j = 0; j < NTW; ++j) acc[i][j] = f32x4{0.f, 0.f, 0.f, 0.f};
  }
  bf16x8 a0[MI], b0[NTW], a1[MI], b1[ROLL ? 1 : NTW];
  auto rda = [&](bf16x8* a, int kt, int ks) {
    const char* sb = smem + (kt % C::NST) * C::STAGE + (ks ? la1 : la0);
#pragma unroll
    for (int i = 0; i < MI; ++i) a[i] = *(const bf16x8*)(sb + i * 2048);
  };
  auto rdb1 = [&](int j, int kt, int ks) -> bf16x8 {
    const char* sb = smem + (kt % C::NST) * C::STAGE;
    if constexpr (C::BKN) {
      const char* p = sb + tb + ks * (32 * C::BN * 2) + j * 32;
      return cat8(trread(p), trread(p + 8 * C::BN * 2));
    } else {
      return *(const bf16x8*)(sb + (ks ? lb1 : lb0) + j * 2048);
    }
  };
  auto stats = [&](bf16x8* a) {
    if constexpr (ROWSTAT) {
#pragma unroll
      for (int i = 0; i < MI; ++i) { ars[i] = MFMA16(ones8(), a[i], ars[i]); arq[i] = MFMA16(a[i], a[i], arq[i]); }
    }
  };
  STAMP(kid, 0);
  BAR();
  STAMP(kid, 1);
  if constexpr (!ROLL) {
    constexpr int NM = MI * NTW + (ROWSTAT ? 2 * MI : 0), ND = MI + NTW * (C::BKN ? 2 : 1), RPG = (ND + (NM * 2 / 3) - 1) / (NM * 2 / 3);
#define SCHED(id) _Pragma("unroll") for (int q = 0; q < NM; ++q) { __builtin_amdgcn_sched_group_barrier(0x008, 1, id); \
    if (q * RPG < ND) __builtin_amdgcn_sched_group_barrier(0x080, RPG, id); }
    auto rd = [&](bf16x8* a, bf16x8* bb, int kt, int ks) {
      rda(a, kt, ks);
#pragma unroll
      for (int j = 0; j < NTW; ++j) bb[j] = rdb1(j, kt, ks);
    };
    auto mm = [&](bf16x8* a, bf16x8* bb) {
#pragma unroll
      for (int j = 0; j < NTW; ++j)
#pragma unroll
        for (int i = 0; i < MI; ++i) acc[i][j] = MFMA16(bb[j], a[i], acc[i][j]);
      stats(a);
    };
    auto body = [&](int kt) {
      SB();
      rd(a1, b1, kt, 1);
      mm(a0, b0);
      SCHED(0)
      SB();
      WAITL0(); BAR();
      SB();
      rd(a0, b0, kt + 1, 0);
      mm(a1, b1);
      SCHED(1)
    };
    rd(a0, b0, 0, 0);
    if (hook_kt > 0) {
      for (int kt = 0; kt < hook_kt; ++kt) body(kt);
      SB(); hook(); SB();
      for (int kt = hook_kt; kt < C::KT - 1; ++kt) body(kt);
    } else {
      for (int kt = 0; kt < C::KT - 1; ++kt) body(kt);
    }
    SB();
    rd(a1, b1, C::KT - 1, 1);
    mm(a0, b0);
    mm(a1, b1);
#undef SCHED
  } else {
    auto sub = [&](bf16x8* a, int kt, int ks, bool more) {
#pragma unroll
      for (int j = 0; j < NTW; ++j) {
#pragma unroll
        for (int i = 0; i < MI; ++i) acc[i][j] = MFMA16(b0[j], a[i], acc[i][j]);
        if (more) b0[j] = rdb1(j, kt, ks);
        SB();
      }
      stats(a);
    };
    rda(a0, 0, 0);
#pragma unroll
    for (int j = 0; j < NTW; ++j) b0[j] = rdb1(j, 0, 0);
    for (int kt = 0; kt < C::KT - 1; ++kt) {
      rda(a1, kt, 1);
      sub(a0, kt, 1, true);
      WAITL0(); BAR();
      rda(a0, kt + 1, 0);
      sub(a1, kt + 1, 0, true);
    }
    rda(a1, C::KT - 1, 1);
    sub(a0, C::KT - 1, 1, true);
    sub(a1, 0, 0, false);
  }
  if constexpr (ROWSTAT) {
#pragma unroll
    for (int i = 0; i < MI; ++i) {
      rs[i] = ars[i][0];
      const float d = (fr & 2) ? ((fr & 1) ? arq[i][3] : arq[i][2]) : ((fr & 1) ? arq[i][1] : arq[i][0]);
      rq[i] = __shfl(d, fr + 16 * (fr >> 2));
    }
  }
  SB();
  WAITL0(); BAR();
  SB();
  STAMP(kid, 2);
}

using C1 = Cfg<128, 9, 4, 4, B_KN, 9, false>;
constexpr int RU1 = 32 * 9 * 18, RU2 = RU1 + 8 * 36 * 10, RU3 = RU2 + 8 * 9 * 10;
DI const u16* retile_src(const u16* __restrict__ win, const u16* __restrict__ wout, const u16* __restrict__ wo, int u, int lane) {
  if (u < RU1) {
    const int tile = u / 18, q = u - tile * 18, tn = tile / 9, kt = tile - tn * 9;
    const int c = 64 * q + lane, rho = c / 18, ch = c - rho * 18;
    const int k = (rho & ~12) | ((rho & 4) << 1) | ((rho & 8) >> 1);
    return win + (size_t)(kt * 64 + k) * F2 + tn * 144 + ch * 8;
  }
  const bool o = u < RU2; const int v = u - (o ? RU1 : RU2), KT = o ? 36 : 9;
  const int tile = v / 10, q = v - tile * 10, tn = tile / KT, kt = tile - tn * KT;
  const int c = 64 * q + lane, rho = c / 10, ch = c - rho * 10;
  const int k = (rho & ~12) | ((rho & 4) << 1) | ((rho & 8) >> 1);
  int col = tn * 72 + ch * 8; if (col > D_ - 8) col = D_ - 8;
  int kr = kt * 64 + k;
  if (!o) kr = kr < 512 ? 18 * (kr >> 4) + (kr & 15) : 18 * ((kr - 512) >> 1) + 16 + (kr & 1);
  return (o ? wout : wo) + (size_t)kr * D_ + col;
}
__global__ __launch_bounds__(512, 2) void k_qkv(const u16* __restrict__ x, const u16* __restrict__ wqkv, const u16* __restrict__ win, const u16* __restrict__ wout,
                                                const u16* __restrict__ wo, char* __restrict__ ws) {
  extern __shared__ __attribute__((aligned(16))) char smem[];
  const int tid = threadIdx.x, w = rfl(tid >> 6);
  const int b = blockIdx.x, xcd = b & 7, idx = b >> 3;
  char* ws_dbg = ws;
  if (b == 255 && tid < 56) *(unsigned*)(ws + WS_CNT + tid * 128) = 0u;
  if (idx >= 24) {
    STAMP(0, 0); STAMP(0, 1); STAMP(0, 2);
    const int pw = (idx - 24) * 8 + xcd, lane = tid & 63, gw = pw * 8 + w;
    constexpr int NU = (RU3 + 511) / 512;
    u32x4 v[NU];
#pragma unroll
    for (int i = 0; i < NU; ++i) { const int u = gw + 512 * i; v[i] = *(const u32x4*)retile_src(win, wout, wo, u < RU3 ? u : gw, lane); }
    const int cc = tid % 9, rg = tid / 9;
    u32x4 cv[11];
    if (rg < 56) {
#pragma unroll
      for (int i = 0; i < 11; ++i) { const int r = rg + 56 * i; cv[i] = *(const u32x4*)(win + (size_t)(r < D_ ? r : rg) * F2 + pw * 72 + cc * 8); }
    }
    const __amdgpu_buffer_rsrc_t so = mksrd(ws + WS_WIN, RU3 * 1024);
#pragma unroll
    for (int i = 0; i < NU; ++i) { const int u = gw + 512 * i; if (u < RU3) st16(so, u * 1024 + lane * 16, v[i]); }
    float* red = (float*)smem;
    if (rg < 56) {
      f32x4 sa = {0.f, 0.f, 0.f, 0.f}, sb = {0.f, 0.f, 0.f, 0.f};
#pragma unroll
      for (int i = 0; i < 11; ++i) if (rg + 56 * i < D_) {
        sa += f32x4{bflo(cv[i][0]), bfhi(cv[i][0]), bflo(cv[i][1]), bfhi(cv[i][1])};
        sb += f32x4{bflo(cv[i][2]), bfhi(cv[i][2]), bflo(cv[i][3]), bfhi(cv[i][3])};
      }
      *(f32x4*)(red + rg * 72 + cc * 8) = sa; *(f32x4*)(red + rg * 72 + cc * 8 + 4) = sb;
    }
    __syncthreads();
    if (tid < 288) {
      const int c = tid % 72, q = tid / 72;
      float t = 0.f;
#pragma unroll
      for (int r = 0; r < 14; ++r) t += red[(q * 14 + r) * 72 + c];
      red[56 * 72 + tid] = t;
    }
    __syncthreads();
    if (tid < 72) ((float*)(ws + WS_CS))[pw * 72 + tid] = (red[56 * 72 + tid] + red[56 * 72 + 72 + tid]) + (red[56 * 72 + 144 + tid] + red[56 * 72 + 216 + tid]);
    STAMP_END(0);
    return;
  }
  const int tm = (xcd & 3) * 4 + (idx & 3), tn = (xcd >> 2) * 6 + (idx >> 2);
  if (__builtin_expect(w == 4 || w == 5, 1)) {
    run_producer<C1>(smem, x + (size_t)tm * 128 * D_, 128 * D_ * 2, wqkv + tn * 144, (unsigned)((D_ * 3 * D_ - tn * 144) * 2), D_, 3 * D_, w - 4, tid & 63, ws, 0, ROT1);
    return;
  }
  if (w >= 6) {
    const int lane = tid & 63, p = w - 6, g = lane >> 4;
    f32x4 cs[5];
#pragma unroll
    for (int j = 0; j < 5; ++j) cs[j] = f32x4{0.f, 0.f, 0.f, 0.f};
    const int tb = C1::ABYTES + (16 * (g >> 1) + 4 * (g & 1) + ((lane & 15) >> 2)) * 288 + (lane & 3) * 8 + p * 32;
    for (int kt = 0; kt < 9; ++kt) {
      BAR();
      const char* sb = smem + (kt % C1::NST) * C1::STAGE + tb;
#pragma unroll
      for (int jj = 0; jj < 5; ++jj)
#pragma unroll
        for (int ks = 0; ks < 2; ++ks) {
          const char* q = sb + ks * (32 * 288) + jj * 64;
          cs[jj] = MFMA16(cat8(trread(q), trread(q + 8 * 288)), ones8(), cs[jj]);
        }
      WAITL0();
    }
    float* st = (float*)(smem + C1::CS_OFF);
#pragma unroll
    for (int jj = 0; jj < 5; ++jj) if ((lane & 15) == 0 && p + 2 * jj < 9) *(f32x4*)(st + 16 * (p + 2 * jj) + 4 * g) = cs[jj];
    WAITL0(); BAR();
    return;
  }
  f32x4 acc[2][9]; float rs[2], rq[2];
  consumer<C1, 2, 9, 0, false, true>(smem, w * 32, tid & 63, acc, rs, rq, ws, 0, 0, NoHook());
  int tz = threadIdx.x; VREG(tz);
  const int lane = tz & 63, g = lane >> 4, fr = lane & 15;
  const float* cs = (const float*)(smem + C1::CS_OFF);
  const int kind = tn >> 2, head0 = (tn & 3) * 8;
  const int tile32 = tm * 4 + w;
  const float sc = kind == 0 ? 0.23570226039551584f * 1.4426950408889634f : 1.f;
  float mu[2], rstd[2];
#pragma unroll
  for (int i = 0; i < 2; ++i) {
    const f32x2 st = ln_stats(rs[i], rq[i], 1.f / D_);
    mu[i] = st[0]; rstd[i] = st[1] * sc;
  }
  constexpr int RP = 336;
  char* reg = smem + w * (32 * RP);
  f32x4 c4a[9];
#pragma unroll
  for (int j = 0; j < 9; ++j) c4a[j] = *(const f32x4*)(cs + 16 * j + 4 * g);
#pragma unroll
  for (int j = 0; j < 9; ++j) {
    const f32x4 c4 = c4a[j];
#pragma unroll
    for (int rr = 0; rr < 2; ++rr) {
      const int c = 16 * j + 4 * g + 2 * rr, off = c * 2 + (c / 18) * 4;
#pragma unroll
      for (int i = 0; i < 2; ++i)
        *(unsigned*)(reg + (16 * i + fr) * RP + off) = cvtpk((acc[i][j][2 * rr] - mu[i] * c4[2 * rr]) * rstd[i], (acc[i][j][2 * rr + 1] - mu[i] * c4[2 * rr + 1]) * rstd[i]);
    }
  }
  WAITL0();
  if (kind < 2) {
    char* base = ws + (kind == 0 ? WS_Q + (size_t)tile32 * QK_TILE + (size_t)head0 * Q_HEAD : WS_KV + (size_t)tile32 * KV_TILE + (size_t)head0 * KV_HEAD);
    const int hstride = kind == 0 ? Q_HEAD : KV_HEAD;
    const char* rp = reg + (lane & 31) * RP + (lane >> 5) * 16;
#pragma unroll
    for (int hl = 0; hl < 8; ++hl) {
      const u32x2 lo = *(const u32x2*)(rp + hl * 40), hi = *(const u32x2*)(rp + hl * 40 + 8);
      *(u32x4*)(base + (size_t)hl * hstride + lane * 16) = u32x4{lo[0], lo[1], hi[0], hi[1]};
    }
#pragma unroll
    for (int q = 0; q < 4; ++q) {
      const int hl = 2 * q + (lane >> 5);
      *(unsigned*)(base + (size_t)hl * hstride + 1024 + (lane & 31) * 4) = *(const unsigned*)(reg + (lane & 31) * RP + hl * 40 + 32);
    }
  } else {
    char* base = ws + WS_KV + (size_t)tile32 * KV_TILE + V_OFF + (size_t)head0 * KV_HEAD;
    const int s = g >> 1, h = g & 1;
    const char* rp = reg + (16 * s + 4 * h + (fr >> 2)) * RP + (lane & 3) * 8;
#pragma unroll
    for (int j = 0; j < 10; ++j) {
      const bf16x8 v = cat8(trread(rp + j * 32), trread(rp + 8 * RP + j * 32));
      const int c = 16 * j + fr, hl = c / 20, d = c - hl * 20;
      if (d < 18) *(bf16x8*)(base + (size_t)hl * KV_HEAD + s * 608 + h * 304 + d * 16) = v;
    }
    if (lane < 32) {
      *(bf16x8*)(base + (size_t)(lane >> 2) * KV_HEAD + (lane & 3) * 304 + 18 * 16) = ones8();
    }
  }
  STAMP_END(0);
}

DI void spin_until(const unsigned* cnt, unsigned target) {
  for (unsigned spins = 0; spins < (1u << 24); ++spins) {
    if (__hip_atomic_load(cnt, __ATOMIC_RELAXED, __HIP_MEMORY_SCOPE_AGENT) >= target) break;
    __builtin_amdgcn_s_sleep(1);
  }
}
template <int KT> using CR = Cfg<64, 5, 4, 8, B_TILED, KT, false>;
constexpr int ATT_LDS = 152 * 1024;
__global__ __launch_bounds__(512, 2) void k_attn(char* __restrict__ ws, const u16* __restrict__ wsp, const u16* __restrict__ x) {
  extern __shared__ __attribute__((aligned(16))) char smem[];
  const int tid = threadIdx.x, lane = tid & 63, w = rfl(tid >> 6);
  const int b = blockIdx.x, xcd = b & 7, idx = b >> 3;
  const int head = xcd * 4 + (idx & 3), qb = idx >> 2;
  char* ws_dbg = ws; STAMP(1, 0);
  const int q31 = lane & 31, h = lane >> 5;
  const char* qp = ws + WS_Q + (size_t)head * Q_HEAD + (size_t)(qb * 8 + w) * QK_TILE;
  bf16x8 qf1; unsigned q2;
  asm volatile("global_load_dwordx4 %0, %1, off" : "=v"(qf1) : "v"(qp + lane * 16));
  asm volatile("global_load_dword %0, %1, off" : "=v"(q2) : "v"(qp + 1024 + q31 * 4));
  {
    __amdgpu_buffer_rsrc_t srd = mksrd(ws + WS_KV + (size_t)head * KV_HEAD, KV_HEAD);
    const unsigned ldsb = (unsigned)(size_t)smem + w * 1024, vo = lane * 16 + w * 1024;
#pragma unroll
    for (int j = 0; j < 19; ++j) DMAQ(ldsb, j * 8192, vo, srd, (unsigned)(j * 8192));
  }
  asm volatile("s_waitcnt vmcnt(16)" : "+v"(qf1), "+v"(q2) :: "memory");
  BAR();
  u32x4 q2v = {h == 0 ? q2 : 0u, 0u, 0u, 0u};
  const bf16x8 qf2 = __builtin_bit_cast(bf16x8, q2v);
  const int dcl = q31 < 19 ? q31 : (q31 >= 20 && q31 < 28 ? 12 : 16);
  const int koff = lane * 16, k2off = 1024 + q31 * 4, voff = V_OFF + h * 304 + dcl * 16;
  f32x16 o;
#pragma unroll
  for (int r = 0; r < 16; ++r) o[r] = 0.f;
  auto scores = [&](int t, const f32x16& c) {
    const char* tp = smem + t * KV_TILE;
    const bf16x8 k1 = *(const bf16x8*)(tp + koff);
    u32x4 k2v = {*(const unsigned*)(tp + k2off), 0u, 0u, 0u};
    f32x16 s = MFMA32(k1, qf1, c);
    return MFMA32(__builtin_bit_cast(bf16x8, k2v), qf2, s);
  };
  f32x16 zero;
#pragma unroll
  for (int r = 0; r < 16; ++r) zero[r] = 0.f;
  STAMP(1, 1);
  unsigned dummy_keep;
  f32x16 negm;
  {
    const f32x16 s = scores(0, zero);
    float m = s[0];
#pragma unroll
    for (int r = 1; r < 16; ++r) m = fmaxf(m, s[r]);
    m = fmaxf(m, xor32(m));
#pragma unroll
    for (int r = 0; r < 16; ++r) negm[r] = -m;
  }
  {
    struct Frag { bf16x8 k1, v0, v1; u32x4 k2; };
    Frag fA, fB;
    fA.k2 = u32x4{0u, 0u, 0u, 0u}; fB.k2 = u32x4{0u, 0u, 0u, 0u};
    f32x16 sA = scores(0, negm), sB;
    fA.k1 = *(const bf16x8*)(smem + KV_TILE + koff); fA.k2[0] = *(const unsigned*)(smem + KV_TILE + k2off);
    fA.v0 = *(const bf16x8*)(smem + voff); fA.v1 = *(const bf16x8*)(smem + voff + 608);
    sA[0] = __builtin_amdgcn_exp2f(sA[0]); sA[1] = __builtin_amdgcn_exp2f(sA[1]);
    auto step = [&](int t, f32x16& sc, f32x16& sn, Frag& fc, Frag& fn) {
      const char* tp = smem + t * KV_TILE;
      u32x4 p0, p1;
      fn.v1 = *(const bf16x8*)(tp + KV_TILE + voff + 608); SB();
      fn.v0 = *(const bf16x8*)(tp + KV_TILE + voff); SB();
      fn.k2[0] = *(const unsigned*)(tp + 2 * KV_TILE + k2off); SB();
      fn.k1 = *(const bf16x8*)(tp + 2 * KV_TILE + koff);
      SB();
      sn = MFMA32(fc.k1, qf1, negm);
#pragma unroll
      for (int e = 2; e < 5; ++e) sc[e] = __builtin_amdgcn_exp2f(sc[e]);
      SB();
      sn = MFMA32(__builtin_bit_cast(bf16x8, fc.k2), qf2, sn);
#pragma unroll
      for (int e = 5; e < 8; ++e) sc[e] = __builtin_amdgcn_exp2f(sc[e]);
#pragma unroll
      for (int e = 0; e < 4; ++e) p0[e] = cvtpk(sc[2 * e], sc[2 * e + 1]);
      SB();
      sc[8] = __builtin_amdgcn_exp2f(sc[8]); sc[9] = __builtin_amdgcn_exp2f(sc[9]);
      SB();
      o = MFMA32(fc.v0, __builtin_bit_cast(bf16x8, p0), o);
#pragma unroll
      for (int e = 10; e < 16; ++e) sc[e] = __builtin_amdgcn_exp2f(sc[e]);
#pragma unroll
      for (int e = 0; e < 4; ++e) p1[e] = cvtpk(sc[8 + 2 * e], sc[8 + 2 * e + 1]);
      SB();
      sn[0] = __builtin_amdgcn_exp2f(sn[0]); sn[1] = __builtin_amdgcn_exp2f(sn[1]);
      SB();
      o = MFMA32(fc.v1, __builtin_bit_cast(bf16x8, p1), o);
      SB();
    };
    unsigned dummy = 0;
    auto warm = [&]() { const unsigned T = b * 512 + tid; TOUCH(dummy, (const char*)wsp + (size_t)(T < 65536u ? T : 0u) * 128); };
#define CHUNK(c, nv) { if (c > 0) { WAITV(nv); BAR(); } if (c == 3) warm(); \
    for (int t = 8 * c; t < 8 * c + 8; t += 2) { step(t, sA, sB, fA, fB); step(t + 1, sB, sA, fB, fA); } }
    CHUNK(0, 16) CHUNK(1, 13) CHUNK(2, 11) CHUNK(3, 9) CHUNK(4, 7) CHUNK(5, 5) CHUNK(6, 3) CHUNK(7, 1)
#undef CHUNK
    VREG(dummy);
    dummy_keep = dummy;
  }
  STAMP(1, 2);
  float l = __shfl(o[10], q31);
  bool ok = l < 1.8446744e19f;
#pragma unroll
  for (int r = 0; r < 12; ++r) ok = ok && (fabsf(o[r]) <= 3.0e38f);
  if (!__all(ok)) {
    float m = -3.0e38f;
#pragma unroll
    for (int r = 0; r < 16; ++r) o[r] = 0.f;
    for (int t = 0; t < 64; ++t) {
      f32x16 s = scores(t, zero);
      float tmax = s[0];
#pragma unroll
      for (int r = 1; r < 16; ++r) tmax = fmaxf(tmax, s[r]);
      tmax = fmaxf(tmax, xor32(tmax));
      const float mn = fmaxf(m, tmax), alpha = __builtin_amdgcn_exp2f(m - mn);
      m = mn;
#pragma unroll
      for (int r = 0; r < 16; ++r) { o[r] *= alpha; s[r] = __builtin_amdgcn_exp2f(s[r] - mn); }
      const char* tp = smem + t * KV_TILE + voff;
      const bf16x8 v0 = *(const bf16x8*)(tp), v1 = *(const bf16x8*)(tp + 608);
      u32x4 p0, p1;
#pragma unroll
      for (int e = 0; e < 4; ++e) { p0[e] = cvtpk(s[2 * e], s[2 * e + 1]); p1[e] = cvtpk(s[8 + 2 * e], s[8 + 2 * e + 1]); }
      o = MFMA32(v0, __builtin_bit_cast(bf16x8, p0), o);
      o = MFMA32(v1, __builtin_bit_cast(bf16x8, p1), o);
    }
    l = __shfl(o[10], q31);
  }
  const float inv = 1.f / l;
  {
    unsigned a0 = cvtpk(o[0] * inv, o[1] * inv), a1 = cvtpk(o[2] * inv, o[3] * inv), b0 = cvtpk(o[4] * inv, o[5] * inv), b1 = cvtpk(o[6] * inv, o[7] * inv);
    { auto r = __builtin_amdgcn_permlane32_swap(a0, b0, false, false); a0 = r[0]; b0 = r[1]; }
    { auto r = __builtin_amdgcn_permlane32_swap(a1, b1, false, false); a1 = r[0]; b1 = r[1]; }
    const __amdgpu_buffer_rsrc_t so = mksrd(ws + WS_AO + (size_t)(qb * 32 + head) * 8192, 8192);
    __builtin_amdgcn_raw_buffer_store_b128(u32x4{a0, a1, b0, b1}, so, (w * 32 + q31) * 32 + h * 16, 0, 16);
    const __amdgpu_buffer_rsrc_t s2 = mksrd(ws + WS_AO + (size_t)S_ * 1024, S_ * 128);
    if (h == 0) __builtin_amdgcn_raw_buffer_store_b32(cvtpk(o[8] * inv, o[9] * inv), s2, (qb * 256 + w * 32 + q31) * 128 + head * 4, 0, 16);
  }
  asm volatile("s_waitcnt vmcnt(0)" : "+v"(dummy_keep) :: "memory");
  WAITL0(); BAR();
  STAMP(2, 0); STAMP(1, 3);
  using C = CR<9>;
  unsigned* cnt = (unsigned*)(ws + WS_CNT + (32 + qb) * 128);
  if (w == 0 && lane == 0) __hip_atomic_fetch_add(cnt, 1u, __ATOMIC_RELAXED, __HIP_MEMORY_SCOPE_AGENT);
  if (w >= 6) return;
  const int row0 = qb * 256 + (head >> 3) * 64, tn = head & 7;
  if (w >= 4) {
    const int p = w - 4;
    const __amdgpu_buffer_rsrc_t srdA = mksrd(ws + WS_AO, S_ * 1152), srdB = mksrd(ws + WS_WO + (size_t)tn * 9 * WR_TB, 9 * WR_TB);
    const unsigned ldsb = (unsigned)(size_t)smem, mA = ldsb + p * 4096, mB = ldsb + C::ABYTES + p * 5120;
    const int c0 = (lane & 7) ^ (lane >> 4), c1 = (lane & 7) ^ (4 + (lane >> 4));
    const int rl = (head >> 3) * 64 + p * 32 + (lane >> 3);
    const unsigned va0 = (unsigned)(qb * 32 + (c0 >> 1)) * 8192 + rl * 32 + (c0 & 1) * 16, va1 = (unsigned)(qb * 32 + (c1 >> 1)) * 8192 + rl * 32 + (c1 & 1) * 16;
    const unsigned vt0 = S_ * 1024 + (qb * 256 + rl) * 128 + c0 * 16, vt1 = S_ * 1024 + (qb * 256 + rl) * 128 + c1 * 16, vb = p * 5120 + lane * 16;
    auto issueA = [&](int kt) {
      const unsigned sa = mA + (kt & 7) * C::STAGE;
      if (kt < 8) {
#pragma unroll
        for (int j = 0; j < 4; ++j) { const unsigned v = ((j & 1) ? va1 : va0) + j * 8 * 32; DMAQ_SC1(sa, j * 1024, v, srdA, (unsigned)(kt * 4 * 8192)); }
      } else {
#pragma unroll
        for (int j = 0; j < 4; ++j) { const unsigned v = ((j & 1) ? vt1 : vt0) + j * 8 * 128; DMAQ_SC1(sa, j * 1024, v, srdA, 0u); }
      }
    };
    auto issueB = [&](int kt) {
      const unsigned sb = mB + (kt & 7) * C::STAGE, sk = kt * WR_TB;
#pragma unroll
      for (int j = 0; j < 5; ++j) { const unsigned v = vb + j * 1024; DMAQ(sb, j * 1024, v, srdB, sk); }
    };
#pragma unroll
    for (int s = 0; s < 7; ++s) issueB(s);
    if (lane == 0) spin_until(cnt, 32u);
    asm volatile("" ::: "memory");
#pragma unroll
    for (int s = 0; s < 7; ++s) issueA(s);
    WAITV(24); BAR(); issueA(7); issueB(7);
    WAITV(29); BAR(); issueA(8); issueB(8);
    WAITV(34); BAR(); WAITV(30); BAR(); WAITV(26); BAR(); WAITV(22); BAR(); WAITV(18); BAR(); WAITV(9); BAR(); WAITV(0); BAR();
    BAR();
    return;
  }
  {
    const int g = lane >> 4, fr = lane & 15;
    const int tok = row0 + w * 16 + fr;
    const size_t ro = (size_t)tok * D_ + tn * 72 + 4 * g;
    u32x2 r[5];
#pragma unroll
    for (int j = 0; j < 5; ++j) r[j] = *(const u32x2*)(x + ro + ((j < 4 || g < 2) ? 16 * j : 0));
    f32x4 acc[1][5]; float rs[1], rq[1];
    consumer<C, 1, 5, 0, false, false>(smem, w * 16, lane, acc, rs, rq, ws, 7, 0, NoHook());
    u16* out = (u16*)(ws + WS_H);
    float s1 = 0.f, s2 = 0.f;
#pragma unroll
    for (int j = 0; j < 5; ++j) {
      if (j < 4 || g < 2) {
        u32x2 v;
        const unsigned p0 = cvtpk(acc[0][j][0], acc[0][j][1]), p1 = cvtpk(acc[0][j][2], acc[0][j][3]);
        v[0] = cvtpk(bflo(r[j][0]) + bflo(p0), bfhi(r[j][0]) + bfhi(p0));
        v[1] = cvtpk(bflo(r[j][1]) + bflo(p1), bfhi(r[j][1]) + bfhi(p1));
        *(u32x2*)(out + ro + 16 * j) = v;
        const float a0 = bflo(v[0]), a1 = bfhi(v[0]), a2 = bflo(v[1]), a3 = bfhi(v[1]);
        s1 += (a0 + a1) + (a2 + a3);
        s2 = fmaf(a0, a0, fmaf(a1, a1, fmaf(a2, a2, fmaf(a3, a3, s2))));
      }
    }
    s1 += xor16(s1); s1 += xor32(s1); s2 += xor16(s2); s2 += xor32(s2);
    if (g == 0) *(f32x2*)(ws + WS_HST + ((size_t)tok * 8 + tn) * 8) = f32x2{s1, s2};
  }
  STAMP_END(2);
}

constexpr int MST = 36864, MNST = 4;
constexpr int M_CSA = MNST * MST, M_CSB = M_CSA + 576, M_RS = M_CSB + 576, M_S2 = M_RS + 1024, M_LDS = M_S2 + 1024;
__global__ __launch_bounds__(448, 2) void k_mix(const u16* __restrict__ wsp, const u16* __restrict__ bsp, char* __restrict__ ws, u16* __restrict__ outp) {
  extern __shared__ __attribute__((aligned(16))) char smem[];
  const int tid = threadIdx.x, w = rfl(tid >> 6);
  const int b = blockIdx.x, xcd = b & 7, idx = b >> 3;
  const int tm = (xcd & 1) * 8 + (idx & 7), tn = (xcd >> 1) * 4 + (idx >> 3);
  char* ws_dbg = ws;
  unsigned* cnt1 = (unsigned*)(ws + WS_CNT + tm * 128);
  unsigned* cnt2 = (unsigned*)(ws + WS_CNT + (16 + tn) * 128);
  unsigned* cnt3 = (unsigned*)(ws + WS_CNT + (40 + tm) * 128);
  using CD = CR<36>;
  const int drow0 = tm * 128 + (tn >> 3) * 64, dtn = tn & 7;
  if (__builtin_expect(w == 4 || w == 5, 1)) {
    const int lane = tid & 63, p = w - 4;
    if (p == 0) STAMPW(3, 4);
    const unsigned ldsb = (unsigned)(size_t)smem;
    const unsigned mA = ldsb + p * 8192, mB = ldsb + 16384 + p * 9216;
    const unsigned sw0 = ((lane & 7) ^ (lane >> 4)) << 4, sw1 = ((lane & 7) ^ (4 + (lane >> 4))) << 4;
    const int g = lane >> 4;
    constexpr int NQ = 17;
    {
      const __amdgpu_buffer_rsrc_t srdA = mksrd((const u16*)(ws + WS_H) + (size_t)tm * 128 * D_, 128 * D_ * 2), srdB = mksrd(ws + WS_WIN, 32 * 9 * WIN_TB);
      const unsigned va0 = (p * 64 + (lane >> 3)) * (D_ * 2) + sw0, va1 = (p * 64 + (lane >> 3)) * (D_ * 2) + sw1, vb = p * 9216 + lane * 16;
      auto issue = [&](int s) {
        const int ph = s >= 9, kt = s - 9 * ph;
        const unsigned so = (s & 3) * MST, sa = mA + so, sb = mB + so;
        const unsigned soA = kt * 128, soB = ((ph ? tn : tn + 16) * 9 + kt) * WIN_TB;
#pragma unroll
        for (int j = 0; j < 8; ++j) { const unsigned v = ((j & 1) ? va1 : va0) + j * 8 * (D_ * 2); DMAQ(sa, j * 1024, v, srdA, soA); }
#pragma unroll
        for (int j = 0; j < 9; ++j) { const unsigned v = vb + j * 1024; DMAQ(sb, j * 1024, v, srdB, soB); }
      };
      issue(0); issue(1); issue(2);
      if (p == 0) STAMPW(3, 5);
      for (int s = 0; s < 15; ++s) { WAITV(2 * NQ); BAR(); issue(s + 3); }
      WAITV(2 * NQ); BAR(); WAITV(NQ); BAR(); WAITV(0); BAR();
    }
    BAR();
    BAR();
    if (p == 0) STAMPW(5, 0);
    {
      const __amdgpu_buffer_rsrc_t srdA = mksrd(wsp + (size_t)tm * 128 * S_, 128 * S_ * 2), srdB = mksrd((const u16*)(ws + WS_Z2N) + (size_t)tn * 144 * S_, 144 * S_ * 2);
      const unsigned va0 = (p * 64 + (lane >> 3)) * (S_ * 2) + sw0, va1 = (p * 64 + (lane >> 3)) * (S_ * 2) + sw1;
      const unsigned vb0 = (p * 72 + (lane >> 3)) * (S_ * 2) + (p ? sw1 : sw0), vb1 = (p * 72 + (lane >> 3)) * (S_ * 2) + (p ? sw0 : sw1);
      auto issueA = [&](int kt) {
        const unsigned sa = mA + (kt & 3) * MST, sk = kt * 128;
#pragma unroll
        for (int j = 0; j < 8; ++j) { const unsigned v = ((j & 1) ? va1 : va0) + j * 8 * (S_ * 2); DMAQ(sa, j * 1024, v, srdA, sk); }
      };
      auto issueB = [&](int kt) {
        const unsigned sb = mB + (kt & 3) * MST, sk = kt * 128;
#pragma unroll
        for (int j = 0; j < 9; ++j) { const unsigned v = ((j & 1) ? vb1 : vb0) + j * 8 * (S_ * 2); DMAQ_SC1(sb, j * 1024, v, srdB, sk); }
      };
      auto issue = [&](int kt) { issueA(kt); issueB(kt); };
      issueA(0); issueA(1); issueA(2);
      if (lane == 0) spin_until(cnt2, 16u);
      asm volatile("" ::: "memory");
      if (p == 0) STAMPW(5, 1);
      if (p == 0) STAMPW(5, 2);
      issueB(0); issueB(1); issueB(2);
      if (p == 0) STAMPW(5, 3);
      WAITV(18); BAR(); issue(3);
      WAITV(9 + NQ); BAR(); issue(4);
      for (int kt = 2; kt < 29; ++kt) { WAITV(2 * NQ); BAR(); issue(kt + 3); }
      WAITV(2 * NQ); BAR(); WAITV(NQ); BAR(); WAITV(0); BAR();
    }
    BAR();
    BAR();
    {
      const __amdgpu_buffer_rsrc_t srdA = mksrd((const u16*)(ws + WS_U) + (size_t)drow0 * FH, 64 * FH * 2), srdB = mksrd(ws + WS_WOUT + (size_t)dtn * 36 * WR_TB, 36 * WR_TB);
      const unsigned mA2 = ldsb + p * 4096, mB2 = ldsb + CD::ABYTES + p * 5120;
      const unsigned va0 = (p * 32 + (lane >> 3)) * (FH * 2) + sw0, va1 = (p * 32 + (lane >> 3)) * (FH * 2) + sw1, vb = p * 5120 + lane * 16;
      auto issueA = [&](int kt) {
        const unsigned sa = mA2 + (kt & 7) * CD::STAGE, sk = kt * 128;
#pragma unroll
        for (int j = 0; j < 4; ++j) { const unsigned v = ((j & 1) ? va1 : va0) + j * 8 * (FH * 2); DMAQ_SC1(sa, j * 1024, v, srdA, sk); }
      };
      auto issueB = [&](int kt) {
        const unsigned sb = mB2 + (kt & 7) * CD::STAGE, sk = kt * WR_TB;
#pragma unroll
        for (int j = 0; j < 5; ++j) { const unsigned v = vb + j * 1024; DMAQ(sb, j * 1024, v, srdB, sk); }
      };
#pragma unroll
      for (int s = 0; s < 7; ++s) issueB(s);
      if (lane == 0) spin_until(cnt3, 16u);
      asm volatile("" ::: "memory");
#pragma unroll
      for (int s = 0; s < 7; ++s) issueA(s);
      WAITV(24); BAR(); issueA(7); issueB(7);
      WAITV(29); BAR(); issueA(8); issueB(8);
      WAITV(34); BAR(); issueA(9); issueB(9);
      WAITV(39); BAR(); issueA(10); issueB(10);
      WAITV(44); BAR(); issueA(11); issueB(11);
      WAITV(49); BAR(); issueA(12); issueB(12);
      for (int kt = 6; kt < 29; ++kt) { WAITV(54); BAR(); issueA(kt + 7); issueB(kt + 7); }
      WAITV(54); BAR(); WAITV(45); BAR(); WAITV(36); BAR(); WAITV(27); BAR(); WAITV(18); BAR(); WAITV(9); BAR(); WAITV(0); BAR();
      BAR();
    }
    return;
  }
  if (w == 6) {
    const int lane = tid & 63;
    f32x2* rst = (f32x2*)(smem + M_RS);
#pragma unroll
    for (int i = 0; i < 2; ++i) {
      const f32x4* q = (const f32x4*)(ws + WS_HST + (size_t)(tm * 128 + 64 * i + lane) * 64);
      const f32x4 a = q[0], bb = q[1], c = q[2], d = q[3];
      const float s1 = (a[0] + a[2]) + (bb[0] + bb[2]) + (c[0] + c[2]) + (d[0] + d[2]), s2 = (a[1] + a[3]) + (bb[1] + bb[3]) + (c[1] + c[3]) + (d[1] + d[3]);
      rst[64 * i + lane] = ln_stats(s1, s2, 1.f / D_);
    }
    if (lane < 36) {
      *(f32x4*)(smem + M_CSA + lane * 16) = *(const f32x4*)(ws + WS_CS + (size_t)((tn + 16) * 144 + lane * 4) * 4);
      *(f32x4*)(smem + M_CSB + lane * 16) = *(const f32x4*)(ws + WS_CS + (size_t)(tn * 144 + lane * 4) * 4);
    }
    WAITL0();
    for (int s = 0; s < 15; ++s) BAR();
    if (lane == 0) spin_until(cnt1, 16u);
    asm volatile("" ::: "memory");
    const __amdgpu_buffer_rsrc_t srdS = mksrd(ws + WS_ST + (size_t)tm * (16 * 128 * 8), 16 * 128 * 8);
    u32x4 pv[16];
#pragma unroll
    for (int q = 0; q < 16; ++q) pv[q] = __builtin_amdgcn_raw_buffer_load_b128(srdS, (q * 128 + 2 * lane) * 8, 0, 16);
    SB(); BAR(); BAR(); SB();
    f32x4 t4 = {0.f, 0.f, 0.f, 0.f};
#pragma unroll
    for (int q = 0; q < 16; ++q) t4 += __builtin_bit_cast(f32x4, pv[q]);
    {
      const f32x2 q0 = ln_stats(t4[0], t4[1], 1.f / FH), q1 = ln_stats(t4[2], t4[3], 1.f / FH);
      *(f32x4*)(smem + M_S2 + lane * 16) = f32x4{q0[1], -q0[0] * q0[1], q1[1], -q1[0] * q1[1]};
    }
    WAITL0();
    BAR();
    BAR();
    return;
  }
  const int lane = tid & 63, g = lane >> 4, fr = lane & 15, f = fr >> 1;
  const int l0 = fr * 128 + ((g ^ f) << 4), l1 = fr * 128 + (((4 + g) ^ f) << 4);
  const int tb = 16384 + (16 * (g >> 1) + 4 * (g & 1) + (fr >> 2)) * 288 + (lane & 3) * 8;
  const int tok0 = tm * 128 + w * 32;
  f32x4 acc[2][9];
  bf16x8 a0[2], b0[9], a1[2], b1[9];
  auto zero_acc = [&]() {
#pragma unroll
    for (int i = 0; i < 2; ++i)
#pragma unroll
      for (int j = 0; j < 9; ++j) acc[i][j] = f32x4{0.f, 0.f, 0.f, 0.f};
  };
  auto mm = [&](bf16x8* a, bf16x8* bb) {
#pragma unroll
    for (int j = 0; j < 9; ++j)
#pragma unroll
      for (int i = 0; i < 2; ++i) acc[i][j] = MFMA16(bb[j], a[i], acc[i][j]);
  };
  auto rdKN = [&](bf16x8* a, bf16x8* bb, int s, int ks) {
    const char* sb = smem + (s & 3) * MST;
#pragma unroll
    for (int i = 0; i < 2; ++i) a[i] = *(const bf16x8*)(sb + w * 4096 + i * 2048 + (ks ? l1 : l0));
#pragma unroll
    for (int j = 0; j < 9; ++j) { const char* q = sb + tb + ks * (32 * 288) + j * 32; bb[j] = cat8(trread(q), trread(q + 8 * 288)); }
  };
  auto rdNK = [&](bf16x8* a, bf16x8* bb, int s, int ks) {
    const char* sb = smem + (s & 3) * MST + (ks ? l1 : l0);
#pragma unroll
    for (int i = 0; i < 2; ++i) a[i] = *(const bf16x8*)(sb + w * 4096 + i * 2048);
#pragma unroll
    for (int j = 0; j < 9; ++j) bb[j] = *(const bf16x8*)(sb + 16384 + j * 2048);
  };
#define SCHED(id, ND, RPG) _Pragma("unroll") for (int q = 0; q < 18; ++q) { __builtin_amdgcn_sched_group_barrier(0x008, 1, id); \
    if (q * RPG < ND) __builtin_amdgcn_sched_group_barrier(0x080, RPG, id); }
  auto gelu = [&](u32x2 (&zp)[2][9], int csoff, f32x2 (&sum)[2]) {
    const float c1 = -2.f * 0.7978845608028654f * 1.4426950408889634f, c2 = c1 * 0.044715f;
    const float* cs = (const float*)(smem + csoff);
    const f32x2* rst = (const f32x2*)(smem + M_RS) + w * 32;
    float nmu[2], k1[2], k3[2], irs[2];
    f32x2 s1[2], s2[2];
#pragma unroll
    for (int i = 0; i < 2; ++i) {
      const f32x2 st = rst[16 * i + fr];
      nmu[i] = -st[0]; k1[i] = c1 * st[1]; k3[i] = c2 * st[1] * st[1] * st[1]; irs[i] = __builtin_amdgcn_rcpf(st[1]);
      s1[i] = f32x2{0.f, 0.f}; s2[i] = f32x2{0.f, 0.f};
    }
    f32x4 c4 = *(const f32x4*)(cs + 4 * g);
#pragma unroll
    for (int j = 0; j < 9; ++j) {
      const f32x4 c4n = *(const f32x4*)(cs + 16 * (j < 8 ? j + 1 : 8) + 4 * g);
      SB();
#pragma unroll
      for (int i = 0; i < 2; ++i)
#pragma unroll
        for (int hh = 0; hh < 2; ++hh) {
          const f32x2 a2 = {acc[i][j][2 * hh], acc[i][j][2 * hh + 1]}, c2v = {c4[2 * hh], c4[2 * hh + 1]};
          const f32x2 t = f32x2{nmu[i], nmu[i]} * c2v + a2;
          const f32x2 y = t * (t * t * f32x2{k3[i], k3[i]} + f32x2{k1[i], k1[i]});
          const f32x2 e = {__builtin_amdgcn_exp2f(y[0]), __builtin_amdgcn_exp2f(y[1])};
          const f32x2 d = e * f32x2{irs[i], irs[i]} + f32x2{irs[i], irs[i]};
          const f32x2 z = t * f32x2{__builtin_amdgcn_rcpf(d[0]), __builtin_amdgcn_rcpf(d[1])};
          s1[i] += z; s2[i] += z * z;
          zp[i][j][hh] = cvtpk(z[0], z[1]);
        }
      VREG(zp[0][j]); VREG(zp[1][j]);
      c4 = c4n;
      SB();
    }
#pragma unroll
    for (int i = 0; i < 2; ++i) sum[i] = f32x2{s1[i][0] + s1[i][1], s2[i][0] + s2[i][1]};
  };
  const __amdgpu_buffer_rsrc_t srdS = mksrd(ws + WS_ST + (size_t)tm * (16 * 128 * 8), 16 * 128 * 8);
  zero_acc();
  STAMP(3, 0);
  BAR();
  STAMP(3, 1);
  rdKN(a0, b0, 0, 0);
  u32x2 z2p[2][9];
  auto stageAB = [&](int s) {
    SB(); rdKN(a1, b1, s, 1); mm(a0, b0); SCHED(0, 20, 2) SB();
    WAITL0(); BAR();
    SB(); rdKN(a0, b0, s + 1, 0); mm(a1, b1); SCHED(1, 20, 2)
  };
  for (int s = 0; s < 9; ++s) stageAB(s);
  SB();
  STAMP(5, 6);
  {
    f32x2 sum[2];
    gelu(z2p, M_CSA, sum);
#pragma unroll
    for (int i = 0; i < 2; ++i) {
      f32x2 v = sum[i];
      v[0] += xor16(v[0]); v[0] += xor32(v[0]); v[1] += xor16(v[1]); v[1] += xor32(v[1]);
      if (g == 0) __builtin_amdgcn_raw_buffer_store_b64(__builtin_bit_cast(u32x2, v), srdS, (tn * 128 + w * 32 + 16 * i + fr) * 8, 0, 16);
    }
    asm volatile("s_waitcnt vmcnt(0)" ::: "memory");
    zero_acc();
  }
  SB();
  STAMP(5, 4);
  stageAB(9);
  if (w == 0 && lane == 0) __hip_atomic_fetch_add(cnt1, 1u, __ATOMIC_RELAXED, __HIP_MEMORY_SCOPE_AGENT);
  for (int s = 10; s < 17; ++s) stageAB(s);
  SB(); rdKN(a1, b1, 17, 1); mm(a0, b0); mm(a1, b1); SB();
  WAITL0(); BAR();
  STAMP(3, 2);
  {
    const f32x2* st2 = (const f32x2*)(smem + M_S2) + w * 32;
    char* reg = smem + w * (32 * 304);
#pragma unroll
    for (int i = 0; i < 2; ++i) {
      const f32x2 st = st2[16 * i + fr];
#pragma unroll
      for (int j = 0; j < 9; ++j) {
        u32x2 v;
        v[0] = cvtpk(fmaf(bflo(z2p[i][j][0]), st[0], st[1]), fmaf(bfhi(z2p[i][j][0]), st[0], st[1]));
        v[1] = cvtpk(fmaf(bflo(z2p[i][j][1]), st[0], st[1]), fmaf(bfhi(z2p[i][j][1]), st[0], st[1]));
        *(u32x2*)(reg + (16 * i + fr) * 304 + (16 * j + 4 * g) * 2) = v;
      }
    }
    WAITL0();
    const __amdgpu_buffer_rsrc_t so = mksrd((u16*)(ws + WS_Z2N) + (size_t)(tn * 144) * S_ + tok0, 144 * S_ * 2);
    const char* rp = reg + (8 * g + (fr >> 2)) * 304 + (lane & 3) * 8;
#pragma unroll
    for (int j = 0; j < 9; ++j)
      __builtin_amdgcn_raw_buffer_store_b128(__builtin_bit_cast(u32x4, cat8(trread(rp + j * 32), trread(rp + 4 * 304 + j * 32))), so, (j * 16 + fr) * (S_ * 2) + 8 * g * 2, 0, 16);
    asm volatile("s_waitcnt vmcnt(0)" ::: "memory");
    WAITL0(); BAR();
    if (w == 0 && lane == 0) __hip_atomic_fetch_add(cnt2, 1u, __ATOMIC_RELAXED, __HIP_MEMORY_SCOPE_AGENT);
  }
  u32x2 z1p[2][9];
  { f32x2 sum[2]; gelu(z1p, M_CSB, sum); }
  const unsigned bias0 = bsp[tok0 + fr], bias1 = bsp[tok0 + 16 + fr];
  zero_acc();
  STAMP(4, 0);
  BAR();
  STAMP(4, 1);
  rdNK(a0, b0, 0, 0);
  for (int kt = 0; kt < 31; ++kt) {
    SB(); rdNK(a1, b1, kt, 1); mm(a0, b0); SCHED(0, 11, 1) SB();
    WAITL0(); BAR();
    SB(); rdNK(a0, b0, kt + 1, 0); mm(a1, b1); SCHED(1, 11, 1)
  }
  SB(); rdNK(a1, b1, 31, 1); mm(a0, b0); mm(a1, b1); SB();
  WAITL0(); BAR();
  SB();
  STAMP(4, 2);
#undef SCHED
  char* reg = smem + w * (32 * 304);
#pragma unroll
  for (int i = 0; i < 2; ++i) {
    const float bi = bflo(i ? bias1 : bias0);
#pragma unroll
    for (int j = 0; j < 9; ++j) {
      u32x2 v;
      const unsigned e0 = cvtpk(acc[i][j][0], acc[i][j][1]), e1 = cvtpk(acc[i][j][2], acc[i][j][3]);
      const unsigned g0 = cvtpk(bflo(e0) + bi, bfhi(e0) + bi), g1 = cvtpk(bflo(e1) + bi, bfhi(e1) + bi);
      v[0] = cvtpk(bflo(z1p[i][j][0]) * bflo(g0), bfhi(z1p[i][j][0]) * bfhi(g0));
      v[1] = cvtpk(bflo(z1p[i][j][1]) * bflo(g1), bfhi(z1p[i][j][1]) * bfhi(g1));
      *(u32x2*)(reg + (16 * i + fr) * 304 + (16 * j + 4 * g) * 2) = v;
    }
  }
  WAITL0();
  const __amdgpu_buffer_rsrc_t so = mksrd((u16*)(ws + WS_U) + (size_t)tok0 * FH + tn * 144, 32 * FH * 2);
#pragma unroll
  for (int i = 0; i < 9; ++i) {
    const int c = i * 64 + lane, row = c / 18, ch = c - row * 18;
    __builtin_amdgcn_raw_buffer_store_b128(*(const u32x4*)(reg + row * 304 + ch * 16), so, row * (FH * 2) + ch * 16, 0, 16);
  }
  asm volatile("s_waitcnt vmcnt(0)" ::: "memory");
  WAITL0(); BAR();
  STAMP(4, 3);
  if (w == 0 && lane == 0) __hip_atomic_fetch_add(cnt3, 1u, __ATOMIC_RELAXED, __HIP_MEMORY_SCOPE_AGENT);
  {
    const int tok = drow0 + w * 16 + fr;
    const size_t ro = (size_t)tok * D_ + dtn * 72 + 4 * g;
    const u16* res = (const u16*)(ws + WS_H);
    u32x2 r[5];
#pragma unroll
    for (int j = 0; j < 5; ++j) r[j] = *(const u32x2*)(res + ro + ((j < 4 || g < 2) ? 16 * j : 0));
    f32x4 acd[1][5]; float rs[1], rq[1];
    consumer<CD, 1, 5, 0, false, false>(smem, w * 16, lane, acd, rs, rq, ws, 6, 0, NoHook());
#pragma unroll
    for (int j = 0; j < 5; ++j) {
      if (j < 4 || g < 2) {
        const unsigned p0 = cvtpk(acd[0][j][0], acd[0][j][1]), p1 = cvtpk(acd[0][j][2], acd[0][j][3]);
        u32x2 v;
        v[0] = cvtpk(bflo(r[j][0]) + bflo(p0), bfhi(r[j][0]) + bfhi(p0));
        v[1] = cvtpk(bflo(r[j][1]) + bflo(p1), bfhi(r[j][1]) + bfhi(p1));
        *(u32x2*)(outp + ro + 16 * j) = v;
      }
    }
  }
}

extern "C" void kernel_launch(void* const* d_in, const int* in_sizes, int n_in, void* d_out, int out_size, void* d_ws, size_t ws_size, hipStream_t stream) {
  const u16* x = (const u16*)d_in[0];
  const u16* wqkv = (const u16*)d_in[1];
  const u16* wo = (const u16*)d_in[2];
  const u16* win = (const u16*)d_in[4];
  const u16* wsp = (const u16*)d_in[5];
  const u16* bsp = (const u16*)d_in[6];
  const u16* wout = (const u16*)d_in[7];
  char* ws = (char*)d_ws;
  (void)hipFuncSetAttribute((const void*)k_qkv, hipFuncAttributeMaxDynamicSharedMemorySize, C1::LDS);
  (void)hipFuncSetAttribute((const void*)k_attn, hipFuncAttributeMaxDynamicSharedMemorySize, ATT_LDS);
  (void)hipFuncSetAttribute((const void*)k_mix, hipFuncAttributeMaxDynamicSharedMemorySize, M_LDS);
  hipLaunchKernelGGL(k_qkv, dim3(256), dim3(512), C1::LDS, stream, x, wqkv, win, wout, wo, ws);
  hipLaunchKernelGGL(k_attn, dim3(256), dim3(512), ATT_LDS, stream, ws, wsp, x);
  hipLaunchKernelGGL(k_mix, dim3(256), dim3(448), M_LDS, stream, wsp, bsp, ws, (u16*)d_out);
}
```

```cpp
#include <hip/hip_runtime.h>
#include <stdint.h>

typedef unsigned short u16;
typedef short bf16x8 __attribute__((ext_vector_type(8)));
typedef short s16x4 __attribute__((ext_vector_type(4)));
typedef float f32x2 __attribute__((ext_vector_type(2)));
typedef float f32x4 __attribute__((ext_vector_type(4)));
typedef float f32x16 __attribute__((ext_vector_type(16)));
typedef unsigned u32x2 __attribute__((ext_vector_type(2)));
typedef unsigned u32x4 __attribute__((ext_vector_type(4)));
typedef __bf16 bf16x2_t __attribute__((ext_vector_type(2)));
typedef __attribute__((address_space(3))) s16x4* lds_s16x4_ptr;
#define DI __device__ __forceinline__

constexpr int S_ = 2048, D_ = 576, H_ = 32, DH = 18, FH = 2304, F2 = 4608;
constexpr int QK_TILE = 1152;
constexpr int V_OFF = 1152;
constexpr int KV_TILE = 1152 + 4 * 304;
constexpr int KV_HEAD = 64 * KV_TILE;
constexpr int Q_HEAD = 64 * QK_TILE;
constexpr int WIN_TB = 64 * 288, WR_TB = 64 * 160;
constexpr size_t WS_Q = 0;
constexpr size_t WS_KV = WS_Q + (size_t)H_ * Q_HEAD;
constexpr size_t WS_AO = WS_KV + (size_t)H_ * KV_HEAD + 8192;
constexpr size_t WS_H = WS_AO + (size_t)S_ * D_ * 2;
constexpr size_t WS_Z1 = WS_H + (size_t)S_ * D_ * 2;
constexpr size_t WS_Z2T = WS_Z1 + (size_t)S_ * FH * 2;
constexpr size_t WS_Z2N = WS_Z2T + (size_t)S_ * FH * 2;
constexpr size_t WS_ST = WS_Z2N + (size_t)S_ * FH * 2;
constexpr size_t WS_CNT = WS_ST + (size_t)S_ * 16 * 2 * 4;
constexpr size_t WS_HST = WS_ST + (size_t)S_ * 32 * 2 * 4;
constexpr size_t WS_U0 = WS_HST + (size_t)S_ * 8 * 2 * 4;
constexpr size_t WS_U = 0;
static_assert((size_t)S_ * FH * 2 <= WS_H, "u must fit below h");
constexpr size_t WS_WIN = WS_U0;
constexpr size_t WS_WOUT = WS_WIN + (size_t)32 * 9 * WIN_TB;
constexpr size_t WS_WO = WS_WOUT + (size_t)8 * 36 * WR_TB;
constexpr size_t WS_CS = WS_WO + (size_t)8 * 9 * WR_TB;
constexpr size_t WS_END = WS_CS + (size_t)F2 * 4;

DI unsigned cvtpk(float lo, float hi) { f32x2 v = {lo, hi}; bf16x2_t b = __builtin_convertvector(v, bf16x2_t); return __builtin_bit_cast(unsigned, b); }
DI float bflo(unsigned u) { return __uint_as_float(u << 16); }
DI float bfhi(unsigned u) { return __uint_as_float(u & 0xffff0000u); }
DI float bfr(float x) { return __uint_as_float(cvtpk(x, x) << 16); }
DI f32x2 ln_stats(float s1, float s2, float inv_n) {
  const float mu = s1 * inv_n, var = fmaxf(s2 * inv_n - mu * mu, 0.f);
  return f32x2{bfr(mu), 1.f / bfr(sqrtf(bfr(bfr(var) + 1e-5f)))};
}
DI unsigned rfl(unsigned x) { return __builtin_amdgcn_readfirstlane(x); }
DI float xor16(float v) { return __shfl_xor(v, 16); }
DI float xor32(float v) { return __shfl_xor(v, 32); }
#define DMAQ(sb, imm, voff, srd, soff) asm volatile("s_add_u32 m0, %0, %1\n\ts_nop 0\n\tbuffer_load_dwordx4 %2, %3, %4 offen lds" :: "s"(sb), "i"(imm), "v"(voff), "s"(srd), "s"(soff) : "scc")
#define DMAQ_SC1(sb, imm, voff, srd, soff) asm volatile("s_add_u32 m0, %0, %1\n\ts_nop 0\n\tbuffer_load_dwordx4 %2, %3, %4 offen sc1 lds" :: "s"(sb), "i"(imm), "v"(voff), "s"(srd), "s"(soff) : "scc")
#define WAITV(n) asm volatile("s_waitcnt vmcnt(%0)" :: "i"(n) : "memory")
#define WAITL0() asm volatile("s_waitcnt lgkmcnt(0)" ::: "memory")
#define BAR() asm volatile("s_barrier" ::: "memory")
#define SB() __builtin_amdgcn_sched_barrier(0)
#define MFMA16(a, b, c) __builtin_amdgcn_mfma_f32_16x16x32_bf16((a), (b), (c), 0, 0, 0)
#define MFMA32(a, b, c) __builtin_amdgcn_mfma_f32_32x32x16_bf16((a), (b), (c), 0, 0, 0)
#define VREG(x) asm volatile("" : "+v"(x))
DI __amdgpu_buffer_rsrc_t mksrd(const void* p, unsigned bytes) { return __builtin_amdgcn_make_buffer_rsrc((void*)p, (short)0, (int)bytes, 0x00020000); }
DI s16x4 trread(const char* p) { return __builtin_amdgcn_ds_read_tr16_b64_v4i16((lds_s16x4_ptr)(p)); }
DI bf16x8 cat8(s16x4 lo, s16x4 hi) { return __builtin_shufflevector(lo, hi, 0, 1, 2, 3, 4, 5, 6, 7); }
DI bf16x8 ones8() { const u32x4 o = {0x3F803F80u, 0x3F803F80u, 0x3F803F80u, 0x3F803F80u}; return __builtin_bit_cast(bf16x8, o); }
#define ROT1 (3 * (((idx >> 2) + (idx & 3)) % 3))
#define ST_AUX 16
DI void st16(__amdgpu_buffer_rsrc_t srd, unsigned off, u32x4 v) { __builtin_amdgcn_raw_buffer_store_b128(v, srd, off, 0, ST_AUX); }
#define TOUCH(dummy, ptr) asm volatile("global_load_dword %0, %1, off" : "+v"(dummy) : "v"(ptr))
#define STAMP(kid, slot)
#define STAMPW(kid, slot)
#define STAMP_END(kid)

enum { B_NK = 0, B_KN = 1, B_TILED = 2 };
template <int BM_, int NT_, int NP_, int NST_, int BMODE_, int KT_, bool COLSUM_>
struct Cfg {
  static constexpr int BM = BM_, NT = NT_, NP = NP_, NST = NST_, KT = KT_, BMODE = BMODE_;
  static constexpr bool BKN = BMODE_ != B_NK, COLSUM = COLSUM_;
  static constexpr int BN = NT * 16;
  static constexpr int NA = BM / 8, NB = BN / 8, NI = NA + NB;
  static constexpr int ABYTES = BM * 128, BBYTES = BN * 128, STAGE = ABYTES + BBYTES;
  static constexpr int RING = NST * STAGE;
  static constexpr int CS_OFF = RING;
  static constexpr int RS_OFF = RING + BN * 4;
  static constexpr int LDS = RS_OFF + BM * 8;
};

template <class C, int TAILN = 0, int TCPR = 1>
DI void run_producer(char* smem, const void* Ab, unsigned abytes, const void* Bb, unsigned bbytes, int lda, int ldb, int p, int lane, char* ws_dbg, int kid,
                     int rot = 0, const void* Tb = nullptr, unsigned tbytes = 0, int ldt = 0) {
  if (p == 0) STAMPW(kid, 4);
  constexpr int HA = C::NA / 2, HB = C::NB / 2, NQ = HA + HB;
  static_assert(C::NA % 4 == 0 && C::NB % 2 == 0, "piece split");
  const __amdgpu_buffer_rsrc_t srdA = mksrd(Ab, abytes), srdB = mksrd(Bb, bbytes);
  const unsigned ldsb = (unsigned)(size_t)smem;
  const unsigned mA = ldsb + p * (HA * 1024), mB = ldsb + C::ABYTES + p * (HB * 1024);
  const unsigned sw0 = ((lane & 7) ^ (lane >> 4)) << 4, sw1 = ((lane & 7) ^ (4 + (lane >> 4))) << 4;
  const unsigned va0 = (p * HA * 8 + (lane >> 3)) * (lda * 2) + sw0, va1 = (p * HA * 8 + (lane >> 3)) * (lda * 2) + sw1;
  unsigned vb[HB];
#pragma unroll
  for (int j = 0; j < HB; ++j) {
    if (C::BMODE == B_NK) {
      const int q = p * HB + j;
      vb[j] = (8 * q + (lane >> 3)) * (ldb * 2) + ((q & 1) ? sw1 : sw0);
    } else if (C::BMODE == B_KN) {
      constexpr int CPR = C::BN / 8;
      const int c = 64 * (p * HB + j) + lane, rho = c / CPR, ch = c - rho * CPR;
      const int k = (rho & ~12) | ((rho & 4) << 1) | ((rho & 8) >> 1);
      vb[j] = k * (ldb * 2) + ch * 16;
    } else {
      vb[j] = (p * HB + j) * 1024 + lane * 16;
    }
  }
  auto issue = [&](int kt) {
    const unsigned so = (kt % C::NST) * C::STAGE, sa = mA + so, sb = mB + so;
    int kr = kt + rot; if (kr >= C::KT) kr -= C::KT;
    const unsigned soA = kr * 128, soB = C::BMODE == B_NK ? kr * 128 : (C::BMODE == B_KN ? kr * 64 * ldb * 2 : kr * C::BBYTES);
#pragma unroll
    for (int j = 0; j < HA; ++j) { const unsigned v = ((j & 1) ? va1 : va0) + j * 8 * (lda * 2); DMAQ(sa, j * 1024, v, srdA, soA); }
#pragma unroll
    for (int j = 0; j < HB; ++j) DMAQ(sb, j * 1024, vb[j], srdB, soB);
  };
  constexpr int PF = C::NST - 1;
#pragma unroll
  for (int s = 0; s < PF; ++s) issue(s);
  if (p == 0) STAMPW(kid, 5);
  constexpr int NJ = C::COLSUM ? (C::NT + 1) / 2 : 0;
  f32x4 cs[NJ > 0 ? NJ : 1];
#pragma unroll
  for (int j = 0; j < NJ; ++j) cs[j] = f32x4{0.f, 0.f, 0.f, 0.f};
  const int g = lane >> 4;
  const int tb = C::ABYTES + (16 * (g >> 1) + 4 * (g & 1) + ((lane & 15) >> 2)) * (C::BN * 2) + (lane & 3) * 8 + p * 32;
  auto colsum = [&](int kt) {
    if constexpr (C::COLSUM) {
      const char* sb = smem + (kt % C::NST) * C::STAGE + tb;
#pragma unroll
      for (int jj = 0; jj < NJ; ++jj) {
#pragma unroll
        for (int ks = 0; ks < 2; ++ks) {
          const char* q = sb + ks * (32 * C::BN * 2) + jj * 64;
          cs[jj] = MFMA16(cat8(trread(q), trread(q + 8 * C::BN * 2)), ones8(), cs[jj]);
        }
      }
      WAITL0();
    }
  };
  for (int kt = 0; kt < C::KT - PF; ++kt) { WAITV((PF - 1) * NQ); BAR(); issue(kt + PF); colsum(kt); }
  constexpr int NTL = TAILN / 4;
  auto tail = [&](int half) {
    const unsigned sb = ldsb + ((C::KT + half) % C::NST) * C::STAGE + p * (NTL * 1024);
#pragma unroll
    for (int j = 0; j < NTL; ++j) {
      const int c = 64 * (half * 2 * NTL + p * NTL + j) + lane, row = c / TCPR, ch = c - row * TCPR;
      const unsigned vo2 = row * ldt + ch * 16;
      DMAQ(sb, j * 1024, vo2, mksrd(Tb, tbytes), 0u);
    }
  };
#pragma unroll
  for (int r = PF - 1; r >= 0; --r) {
    WAITV(r * NQ + (TAILN > 0 ? (r < PF - 1 ? NTL : 0) + (r < PF - 2 ? NTL : 0) : 0)); BAR();
    if (TAILN > 0 && r == PF - 1) tail(0);
    if (TAILN > 0 && r == PF - 2) tail(1);
    colsum(C::KT - 1 - r);
  }
  if (TAILN > 0) WAITV(0);
  if constexpr (C::COLSUM) {
    float* st = (float*)(smem + C::CS_OFF);
#pragma unroll
    for (int jj = 0; jj < NJ; ++jj) {
      if ((lane & 15) == 0 && p + 2 * jj < C::NT) *(f32x4*)(st + 16 * (p + 2 * jj) + 4 * g) = cs[jj];
    }
    WAITL0();
  }
  BAR();
}

struct NoHook { DI void operator()() const {} };
template <class C, int MI, int NTW, int J0, bool ROLL, bool ROWSTAT, class Hook>
DI void consumer(char* smem, int row0, int lane, f32x4 (&acc)[MI][NTW], float (&rs)[MI], float (&rq)[MI], char* ws_dbg, int kid, int hook_kt, Hook hook) {
  const int g = lane >> 4, fr = lane & 15, f = fr >> 1;
  const int la0 = row0 * 128 + fr * 128 + ((g ^ f) << 4), la1 = row0 * 128 + fr * 128 + (((4 + g) ^ f) << 4);
  const int lb0 = C::ABYTES + J0 * 2048 + fr * 128 + ((g ^ f) << 4), lb1 = C::ABYTES + J0 * 2048 + fr * 128 + (((4 + g) ^ f) << 4);
  const int tb = C::ABYTES + J0 * 32 + (16 * (g >> 1) + 4 * (g & 1) + (fr >> 2)) * (C::BN * 2) + (lane & 3) * 8;
  f32x4 ars[MI], arq[MI];
#pragma unroll
  for (int i = 0; i < MI; ++i) {
    ars[i] = f32x4{0.f, 0.f, 0.f, 0.f}; arq[i] = f32x4{0.f, 0.f, 0.f, 0.f};
#pragma unroll
    for (int j = 0; j < NTW; ++j) acc[i][j] = f32x4{0.f, 0.f, 0.f, 0.f};
  }
  bf16x8 a0[MI], b0[NTW], a1[MI], b1[ROLL ? 1 : NTW];
  auto rda = [&](bf16x8* a, int kt, int ks) {
    const char* sb = smem + (kt % C::NST) * C::STAGE + (ks ? la1 : la0);
#pragma unroll
    for (int i = 0; i < MI; ++i) a[i] = *(const bf16x8*)(sb + i * 2048);
  };
  auto rdb1 = [&](int j, int kt, int ks) -> bf16x8 {
    const char* sb = smem + (kt % C::NST) * C::STAGE;
    if constexpr (C::BKN) {
      const char* p = sb + tb + ks * (32 * C::BN * 2) + j * 32;
      return cat8(trread(p), trread(p + 8 * C::BN * 2));
    } else {
      return *(const bf16x8*)(sb + (ks ? lb1 : lb0) + j * 2048);
    }
  };
  auto stats = [&](bf16x8* a) {
    if constexpr (ROWSTAT) {
#pragma unroll
      for (int i = 0; i < MI; ++i) { ars[i] = MFMA16(ones8(), a[i], ars[i]); arq[i] = MFMA16(a[i], a[i], arq[i]); }
    }
  };
  STAMP(kid, 0);
  BAR();
  STAMP(kid, 1);
  if constexpr (!ROLL) {
    constexpr int NM = MI * NTW + (ROWSTAT ? 2 * MI : 0), ND = MI + NTW * (C::BKN ? 2 : 1), RPG = (ND + (NM * 2 / 3) - 1) / (NM * 2 / 3);
#define SCHED(id) _Pragma("unroll") for (int q = 0; q < NM; ++q) { __builtin_amdgcn_sched_group_barrier(0x008, 1, id); \
    if (q * RPG < ND) __builtin_amdgcn_sched_group_barrier(0x080, RPG, id); }
    auto rd = [&](bf16x8* a, bf16x8* bb, int kt, int ks) {
      rda(a, kt, ks);
#pragma unroll
      for (int j = 0; j < NTW; ++j) bb[j] = rdb1(j, kt, ks);
    };
    auto mm = [&](bf16x8* a, bf16x8* bb) {
#pragma unroll
      for (int j = 0; j < NTW; ++j)
#pragma unroll
        for (int i = 0; i < MI; ++i) acc[i][j] = MFMA16(bb[j], a[i], acc[i][j]);
      stats(a);
    };
    auto body = [&](int kt) {
      SB();
      rd(a1, b1, kt, 1);
      mm(a0, b0);
      SCHED(0)
      SB();
      WAITL0(); BAR();
      SB();
      rd(a0, b0, kt + 1, 0);
      mm(a1, b1);
      SCHED(1)
    };
    rd(a0, b0, 0, 0);
    if (hook_kt > 0) {
      for (int kt = 0; kt < hook_kt; ++kt) body(kt);
      SB(); hook(); SB();
      for (int kt = hook_kt; kt < C::KT - 1; ++kt) body(kt);
    } else {
      for (int kt = 0; kt < C::KT - 1; ++kt) body(kt);
    }
    SB();
    rd(a1, b1, C::KT - 1, 1);
    mm(a0, b0);
    mm(a1, b1);
#undef SCHED
  } else {
    auto sub = [&](bf16x8* a, int kt, int ks, bool more) {
#pragma unroll
      for (int j = 0; j < NTW; ++j) {
#pragma unroll
        for (int i = 0; i < MI; ++i) acc[i][j] = MFMA16(b0[j], a[i], acc[i][j]);
        if (more) b0[j] = rdb1(j, kt, ks);
        SB();
      }
      stats(a);
    };
    rda(a0, 0, 0);
#pragma unroll
    for (int j = 0; j < NTW; ++j) b0[j] = rdb1(j, 0, 0);
    for (int kt = 0; kt < C::KT - 1; ++kt) {
      rda(a1, kt, 1);
      sub(a0, kt, 1, true);
      WAITL0(); BAR();
      rda(a0, kt + 1, 0);
      sub(a1, kt + 1, 0, true);
    }
    rda(a1, C::KT - 1, 1);
    sub(a0, C::KT - 1, 1, true);
    sub(a1, 0, 0, false);
  }
  if constexpr (ROWSTAT) {
#pragma unroll
    for (int i = 0; i < MI; ++i) {
      rs[i] = ars[i][0];
      const float d = (fr & 2) ? ((fr & 1) ? arq[i][3] : arq[i][2]) : ((fr & 1) ? arq[i][1] : arq[i][0]);
      rq[i] = __shfl(d, fr + 16 * (fr >> 2));
    }
  }
  SB();
  WAITL0(); BAR();
  SB();
  STAMP(kid, 2);
}

using C1 = Cfg<128, 9, 4, 4, B_KN, 9, false>;
constexpr int RU1 = 32 * 9 * 18, RU2 = RU1 + 8 * 36 * 10, RU3 = RU2 + 8 * 9 * 10;
DI const u16* retile_src(const u16* __restrict__ win, const u16* __restrict__ wout, const u16* __restrict__ wo, int u, int lane) {
  if (u < RU1) {
    const int tile = u / 18, q = u - tile * 18, tn = tile / 9, kt = tile - tn * 9;
    const int c = 64 * q + lane, rho = c / 18, ch = c - rho * 18;
    const int k = (rho & ~12) | ((rho & 4) << 1) | ((rho & 8) >> 1);
    return win + (size_t)(kt * 64 + k) * F2 + tn * 144 + ch * 8;
  }
  const bool o = u < RU2; const int v = u - (o ? RU1 : RU2), KT = o ? 36 : 9;
  const int tile = v / 10, q = v - tile * 10, tn = tile / KT, kt = tile - tn * KT;
  const int c = 64 * q + lane, rho = c / 10, ch = c - rho * 10;
  const int k = (rho & ~12) | ((rho & 4) << 1) | ((rho & 8) >> 1);
  int col = tn * 72 + ch * 8; if (col > D_ - 8) col = D_ - 8;
  int kr = kt * 64 + k;
  if (!o) kr = kr < 512 ? 18 * (kr >> 4) + (kr & 15) : 18 * ((kr - 512) >> 1) + 16 + (kr & 1);
  return (o ? wout : wo) + (size_t)kr * D_ + col;
}
__global__ __launch_bounds__(512, 2) void k_qkv(const u16* __restrict__ x, const u16* __restrict__ wqkv, const u16* __restrict__ win, const u16* __restrict__ wout,
                                                const u16* __restrict__ wo, char* __restrict__ ws) {
  extern __shared__ __attribute__((aligned(16))) char smem[];
  const int tid = threadIdx.x, w = rfl(tid >> 6);
  const int b = blockIdx.x, xcd = b & 7, idx = b >> 3;
  char* ws_dbg = ws;
  if (b == 255 && tid < 56) *(unsigned*)(ws + WS_CNT + tid * 128) = 0u;
  if (idx >= 24) {
    STAMP(0, 0); STAMP(0, 1); STAMP(0, 2);
    const int pw = (idx - 24) * 8 + xcd, lane = tid & 63, gw = pw * 8 + w;
    constexpr int NU = (RU3 + 511) / 512;
    u32x4 v[NU];
#pragma unroll
    for (int i = 0; i < NU; ++i) { const int u = gw + 512 * i; v[i] = *(const u32x4*)retile_src(win, wout, wo, u < RU3 ? u : gw, lane); }
    const int cc = tid % 9, rg = tid / 9;
    u32x4 cv[11];
    if (rg < 56) {
#pragma unroll
      for (int i = 0; i < 11; ++i) { const int r = rg + 56 * i; cv[i] = *(const u32x4*)(win + (size_t)(r < D_ ? r : rg) * F2 + pw * 72 + cc * 8); }
    }
    const __amdgpu_buffer_rsrc_t so = mksrd(ws + WS_WIN, RU3 * 1024);
#pragma unroll
    for (int i = 0; i < NU; ++i) { const int u = gw + 512 * i; if (u < RU3) st16(so, u * 1024 + lane * 16, v[i]); }
    float* red = (float*)smem;
    if (rg < 56) {
      f32x4 sa = {0.f, 0.f, 0.f, 0.f}, sb = {0.f, 0.f, 0.f, 0.f};
#pragma unroll
      for (int i = 0; i < 11; ++i) if (rg + 56 * i < D_) {
        sa += f32x4{bflo(cv[i][0]), bfhi(cv[i][0]), bflo(cv[i][1]), bfhi(cv[i][1])};
        sb += f32x4{bflo(cv[i][2]), bfhi(cv[i][2]), bflo(cv[i][3]), bfhi(cv[i][3])};
      }
      *(f32x4*)(red + rg * 72 + cc * 8) = sa; *(f32x4*)(red + rg * 72 + cc * 8 + 4) = sb;
    }
    __syncthreads();
    if (tid < 288) {
      const int c = tid % 72, q = tid / 72;
      float t = 0.f;
#pragma unroll
      for (int r = 0; r < 14; ++r) t += red[(q * 14 + r) * 72 + c];
      red[56 * 72 + tid] = t;
    }
    __syncthreads();
    if (tid < 72) ((float*)(ws + WS_CS))[pw * 72 + tid] = (red[56 * 72 + tid] + red[56 * 72 + 72 + tid]) + (red[56 * 72 + 144 + tid] + red[56 * 72 + 216 + tid]);
    STAMP_END(0);
    return;
  }
  const int tm = (xcd & 3) * 4 + (idx & 3), tn = (xcd >> 2) * 6 + (idx >> 2);
  if (__builtin_expect(w == 4 || w == 5, 1)) {
    run_producer<C1>(smem, x + (size_t)tm * 128 * D_, 128 * D_ * 2, wqkv + tn * 144, (unsigned)((D_ * 3 * D_ - tn * 144) * 2), D_, 3 * D_, w - 4, tid & 63, ws, 0, ROT1);
    return;
  }
  if (w >= 6) {
    const int lane = tid & 63, p = w - 6, g = lane >> 4;
    f32x4 cs[5];
#pragma unroll
    for (int j = 0; j < 5; ++j) cs[j] = f32x4{0.f, 0.f, 0.f, 0.f};
    const int tb = C1::ABYTES + (16 * (g >> 1) + 4 * (g & 1) + ((lane & 15) >> 2)) * 288 + (lane & 3) * 8 + p * 32;
    for (int kt = 0; kt < 9; ++kt) {
      BAR();
      const char* sb = smem + (kt % C1::NST) * C1::STAGE + tb;
#pragma unroll
      for (int jj = 0; jj < 5; ++jj)
#pragma unroll
        for (int ks = 0; ks < 2; ++ks) {
          const char* q = sb + ks * (32 * 288) + jj * 64;
          cs[jj] = MFMA16(cat8(trread(q), trread(q + 8 * 288)), ones8(), cs[jj]);
        }
      WAITL0();
    }
    float* st = (float*)(smem + C1::CS_OFF);
#pragma unroll
    for (int jj = 0; jj < 5; ++jj) if ((lane & 15) == 0 && p + 2 * jj < 9) *(f32x4*)(st + 16 * (p + 2 * jj) + 4 * g) = cs[jj];
    WAITL0(); BAR();
    return;
  }
  f32x4 acc[2][9]; float rs[2], rq[2];
  consumer<C1, 2, 9, 0, false, true>(smem, w * 32, tid & 63, acc, rs, rq, ws, 0, 0, NoHook());
  int tz = threadIdx.x; VREG(tz);
  const int lane = tz & 63, g = lane >> 4, fr = lane & 15;
  const float* cs = (const float*)(smem + C1::CS_OFF);
  const int kind = tn >> 2, head0 = (tn & 3) * 8;
  const int tile32 = tm * 4 + w;
  const float sc = kind == 0 ? 0.23570226039551584f * 1.4426950408889634f : 1.f;
  float mu[2], rstd[2];
#pragma unroll
  for (int i = 0; i < 2; ++i) {
    const f32x2 st = ln_stats(rs[i], rq[i], 1.f / D_);
    mu[i] = st[0]; rstd[i] = st[1] * sc;
  }
  constexpr int RP = 336;
  char* reg = smem + w * (32 * RP);
  f32x4 c4a[9];
#pragma unroll
  for (int j = 0; j < 9; ++j) c4a[j] = *(const f32x4*)(cs + 16 * j + 4 * g);
#pragma unroll
  for (int j = 0; j < 9; ++j) {
    const f32x4 c4 = c4a[j];
#pragma unroll
    for (int rr = 0; rr < 2; ++rr) {
      const int c = 16 * j + 4 * g + 2 * rr, off = c * 2 + (c / 18) * 4;
#pragma unroll
      for (int i = 0; i < 2; ++i)
        *(unsigned*)(reg + (16 * i + fr) * RP + off) = cvtpk((acc[i][j][2 * rr] - mu[i] * c4[2 * rr]) * rstd[i], (acc[i][j][2 * rr + 1] - mu[i] * c4[2 * rr + 1]) * rstd[i]);
    }
  }
  WAITL0();
  if (kind < 2) {
    char* base = ws + (kind == 0 ? WS_Q + (size_t)tile32 * QK_TILE + (size_t)head0 * Q_HEAD : WS_KV + (size_t)tile32 * KV_TILE + (size_t)head0 * KV_HEAD);
    const int hstride = kind == 0 ? Q_HEAD : KV_HEAD;
    const char* rp = reg + (lane & 31) * RP + (lane >> 5) * 16;
#pragma unroll
    for (int hl = 0; hl < 8; ++hl) {
      const u32x2 lo = *(const u32x2*)(rp + hl * 40), hi = *(const u32x2*)(rp + hl * 40 + 8);
      *(u32x4*)(base + (size_t)hl * hstride + lane * 16) = u32x4{lo[0], lo[1], hi[0], hi[1]};
    }
#pragma unroll
    for (int q = 0; q < 4; ++q) {
      const int hl = 2 * q + (lane >> 5);
      *(unsigned*)(base + (size_t)hl * hstride + 1024 + (lane & 31) * 4) = *(const unsigned*)(reg + (lane & 31) * RP + hl * 40 + 32);
    }
  } else {
    char* base = ws + WS_KV + (size_t)tile32 * KV_TILE + V_OFF + (size_t)head0 * KV_HEAD;
    const int s = g >> 1, h = g & 1;
    const char* rp = reg + (16 * s + 4 * h + (fr >> 2)) * RP + (lane & 3) * 8;
#pragma unroll
    for (int j = 0; j < 10; ++j) {
      const bf16x8 v = cat8(trread(rp + j * 32), trread(rp + 8 * RP + j * 32));
      const int c = 16 * j + fr, hl = c / 20, d = c - hl * 20;
      if (d < 18) *(bf16x8*)(base + (size_t)hl * KV_HEAD + s * 608 + h * 304 + d * 16) = v;
    }
    if (lane < 32) {
      *(bf16x8*)(base + (size_t)(lane >> 2) * KV_HEAD + (lane & 3) * 304 + 18 * 16) = ones8();
    }
  }
  STAMP_END(0);
}

DI void spin_until(const unsigned* cnt, unsigned target) {
  for (unsigned spins = 0; spins < (1u << 24); ++spins) {
    if (__hip_atomic_load(cnt, __ATOMIC_RELAXED, __HIP_MEMORY_SCOPE_AGENT) >= target) break;
    __builtin_amdgcn_s_sleep(1);
  }
}
template <int KT> using CR = Cfg<64, 5, 4, 8, B_TILED, KT, false>;
constexpr int ATT_LDS = 152 * 1024;
__global__ __launch_bounds__(512, 2) void k_attn(char* __restrict__ ws, const u16* __restrict__ wsp, const u16* __restrict__ x) {
  extern __shared__ __attribute__((aligned(16))) char smem[];
  const int tid = threadIdx.x, lane = tid & 63, w = rfl(tid >> 6);
  const int b = blockIdx.x, xcd = b & 7, idx = b >> 3;
  const int head = xcd * 4 + (idx & 3), qb = idx >> 2;
  char* ws_dbg = ws; STAMP(1, 0);
  const int q31 = lane & 31, h = lane >> 5;
  const char* qp = ws + WS_Q + (size_t)head * Q_HEAD + (size_t)(qb * 8 + w) * QK_TILE;
  bf16x8 qf1; unsigned q2;
  asm volatile("global_load_dwordx4 %0, %1, off" : "=v"(qf1) : "v"(qp + lane * 16));
  asm volatile("global_load_dword %0, %1, off" : "=v"(q2) : "v"(qp + 1024 + q31 * 4));
  {
    __amdgpu_buffer_rsrc_t srd = mksrd(ws + WS_KV + (size_t)head * KV_HEAD, KV_HEAD);
    const unsigned ldsb = (unsigned)(size_t)smem + w * 1024, vo = lane * 16 + w * 1024;
#pragma unroll
    for (int j = 0; j < 19; ++j) DMAQ(ldsb, j * 8192, vo, srd, (unsigned)(j * 8192));
  }
  asm volatile("s_waitcnt vmcnt(16)" : "+v"(qf1), "+v"(q2) :: "memory");
  BAR();
  u32x4 q2v = {h == 0 ? q2 : 0u, 0u, 0u, 0u};
  const bf16x8 qf2 = __builtin_bit_cast(bf16x8, q2v);
  const int dcl = q31 < 19 ? q31 : (q31 >= 20 && q31 < 28 ? 12 : 16);
  const int koff = lane * 16, k2off = 1024 + q31 * 4, voff = V_OFF + h * 304 + dcl * 16;
  f32x16 o;
#pragma unroll
  for (int r = 0; r < 16; ++r) o[r] = 0.f;
  auto scores = [&](int t, const f32x16& c) {
    const char* tp = smem + t * KV_TILE;
    const bf16x8 k1 = *(const bf16x8*)(tp + koff);
    u32x4 k2v = {*(const unsigned*)(tp + k2off), 0u, 0u, 0u};
    f32x16 s = MFMA32(k1, qf1, c);
    return MFMA32(__builtin_bit_cast(bf16x8, k2v), qf2, s);
  };
  f32x16 zero;
#pragma unroll
  for (int r = 0; r < 16; ++r) zero[r] = 0.f;
  STAMP(1, 1);
  unsigned dummy_keep;
  f32x16 negm;
  {
    const f32x16 s = scores(0, zero);
    float m = s[0];
#pragma unroll
    for (int r = 1; r < 16; ++r) m = fmaxf(m, s[r]);
    m = fmaxf(m, xor32(m));
#pragma unroll
    for (int r = 0; r < 16; ++r) negm[r] = -m;
  }
  {
    struct Frag { bf16x8 k1, v0, v1; u32x4 k2; };
    Frag fA, fB;
    fA.k2 = u32x4{0u, 0u, 0u, 0u}; fB.k2 = u32x4{0u, 0u, 0u, 0u};
    f32x16 sA = scores(0, negm), sB;
    fA.k1 = *(const bf16x8*)(smem + KV_TILE + koff); fA.k2[0] = *(const unsigned*)(smem + KV_TILE + k2off);
    fA.v0 = *(const bf16x8*)(smem + voff); fA.v1 = *(const bf16x8*)(smem + voff + 608);
    sA[0] = __builtin_amdgcn_exp2f(sA[0]); sA[1] = __builtin_amdgcn_exp2f(sA[1]);
    auto step = [&](int t, f32x16& sc, f32x16& sn, Frag& fc, Frag& fn) {
      const char* tp = smem + t * KV_TILE;
      u32x4 p0, p1;
      fn.v1 = *(const bf16x8*)(tp + KV_TILE + voff + 608); SB();
      fn.v0 = *(const bf16x8*)(tp + KV_TILE + voff); SB();
      fn.k2[0] = *(const unsigned*)(tp + 2 * KV_TILE + k2off); SB();
      fn.k1 = *(const bf16x8*)(tp + 2 * KV_TILE + koff);
      SB();
      sn = MFMA32(fc.k1, qf1, negm);
#pragma unroll
      for (int e = 2; e < 5; ++e) sc[e] = __builtin_amdgcn_exp2f(sc[e]);
      SB();
      sn = MFMA32(__builtin_bit_cast(bf16x8, fc.k2), qf2, sn);
#pragma unroll
      for (int e = 5; e < 8; ++e) sc[e] = __builtin_amdgcn_exp2f(sc[e]);
#pragma unroll
      for (int e = 0; e < 4; ++e) p0[e] = cvtpk(sc[2 * e], sc[2 * e + 1]);
      SB();
      sc[8] = __builtin_amdgcn_exp2f(sc[8]); sc[9] = __builtin_amdgcn_exp2f(sc[9]);
      SB();
      o = MFMA32(fc.v0, __builtin_bit_cast(bf16x8, p0), o);
#pragma unroll
      for (int e = 10; e < 16; ++e) sc[e] = __builtin_amdgcn_exp2f(sc[e]);
#pragma unroll
      for (int e = 0; e < 4; ++e) p1[e] = cvtpk(sc[8 + 2 * e], sc[8 + 2 * e + 1]);
      SB();
      sn[0] = __builtin_amdgcn_exp2f(sn[0]); sn[1] = __builtin_amdgcn_exp2f(sn[1]);
      SB();
      o = MFMA32(fc.v1, __builtin_bit_cast(bf16x8, p1), o);
      SB();
    };
    unsigned dummy = 0;
    auto warm = [&]() { const unsigned T = b * 512 + tid; TOUCH(dummy, (const char*)wsp + (size_t)(T < 65536u ? T : 0u) * 128); };
#define CHUNK(c, nv) { if (c > 0) { WAITV(nv); BAR(); } if (c == 3) warm(); \
    for (int t = 8 * c; t < 8 * c + 8; t += 2) { step(t, sA, sB, fA, fB); step(t + 1, sB, sA, fB, fA); } }
    CHUNK(0, 16) CHUNK(1, 13) CHUNK(2, 11) CHUNK(3, 9) CHUNK(4, 7) CHUNK(5, 5) CHUNK(6, 3) CHUNK(7, 1)
#undef CHUNK
    VREG(dummy);
    dummy_keep = dummy;
  }
  STAMP(1, 2);
  float l = __shfl(o[10], q31);
  bool ok = l < 1.8446744e19f;
#pragma unroll
  for (int r = 0; r < 12; ++r) ok = ok && (fabsf(o[r]) <= 3.0e38f);
  if (!__all(ok)) {
    float m = -3.0e38f;
#pragma unroll
    for (int r = 0; r < 16; ++r) o[r] = 0.f;
    for (int t = 0; t < 64; ++t) {
      f32x16 s = scores(t, zero);
      float tmax = s[0];
#pragma unroll
      for (int r = 1; r < 16; ++r) tmax = fmaxf(tmax, s[r]);
      tmax = fmaxf(tmax, xor32(tmax));
      const float mn = fmaxf(m, tmax), alpha = __builtin_amdgcn_exp2f(m - mn);
      m = mn;
#pragma unroll
      for (int r = 0; r < 16; ++r) { o[r] *= alpha; s[r] = __builtin_amdgcn_exp2f(s[r] - mn); }
      const char* tp = smem + t * KV_TILE + voff;
      const bf16x8 v0 = *(const bf16x8*)(tp), v1 = *(const bf16x8*)(tp + 608);
      u32x4 p0, p1;
#pragma unroll
      for (int e = 0; e < 4; ++e) { p0[e] = cvtpk(s[2 * e], s[2 * e + 1]); p1[e] = cvtpk(s[8 + 2 * e], s[8 + 2 * e + 1]); }
      o = MFMA32(v0, __builtin_bit_cast(bf16x8, p0), o);
      o = MFMA32(v1, __builtin_bit_cast(bf16x8, p1), o);
    }
    l = __shfl(o[10], q31);
  }
  const float inv = 1.f / l;
  {
    unsigned a0 = cvtpk(o[0] * inv, o[1] * inv), a1 = cvtpk(o[2] * inv, o[3] * inv), b0 = cvtpk(o[4] * inv, o[5] * inv), b1 = cvtpk(o[6] * inv, o[7] * inv);
    { auto r = __builtin_amdgcn_permlane32_swap(a0, b0, false, false); a0 = r[0]; b0 = r[1]; }
    { auto r = __builtin_amdgcn_permlane32_swap(a1, b1, false, false); a1 = r[0]; b1 = r[1]; }
    const __amdgpu_buffer_rsrc_t so = mksrd(ws + WS_AO + (size_t)(qb * 32 + head) * 8192, 8192);
    __builtin_amdgcn_raw_buffer_store_b128(u32x4{a0, a1, b0, b1}, so, (w * 32 + q31) * 32 + h * 16, 0, 16);
    const __amdgpu_buffer_rsrc_t s2 = mksrd(ws + WS_AO + (size_t)S_ * 1024, S_ * 128);
    if (h == 0) __builtin_amdgcn_raw_buffer_store_b32(cvtpk(o[8] * inv, o[9] * inv), s2, (qb * 256 + w * 32 + q31) * 128 + head * 4, 0, 16);
  }
  asm volatile("s_waitcnt vmcnt(0)" : "+v"(dummy_keep) :: "memory");
  WAITL0(); BAR();
  STAMP(2, 0); STAMP(1, 3);
  using C = CR<9>;
  unsigned* cnt = (unsigned*)(ws + WS_CNT + (32 + qb) * 128);
  if (w == 0 && lane == 0) __hip_atomic_fetch_add(cnt, 1u, __ATOMIC_RELAXED, __HIP_MEMORY_SCOPE_AGENT);
  if (w >= 6) return;
  const int row0 = qb * 256 + (head >> 3) * 64, tn = head & 7;
  if (w >= 4) {
    const int p = w - 4;
    const __amdgpu_buffer_rsrc_t srdA = mksrd(ws + WS_AO, S_ * 1152), srdB = mksrd(ws + WS_WO + (size_t)tn * 9 * WR_TB, 9 * WR_TB);
    const unsigned ldsb = (unsigned)(size_t)smem, mA = ldsb + p * 4096, mB = ldsb + C::ABYTES + p * 5120;
    const int c0 = (lane & 7) ^ (lane >> 4), c1 = (lane & 7) ^ (4 + (lane >> 4));
    const int rl = (head >> 3) * 64 + p * 32 + (lane >> 3);
    const unsigned va0 = (unsigned)(qb * 32 + (c0 >> 1)) * 8192 + rl * 32 + (c0 & 1) * 16, va1 = (unsigned)(qb * 32 + (c1 >> 1)) * 8192 + rl * 32 + (c1 & 1) * 16;
    const unsigned vt0 = S_ * 1024 + (qb * 256 + rl) * 128 + c0 * 16, vt1 = S_ * 1024 + (qb * 256 + rl) * 128 + c1 * 16, vb = p * 5120 + lane * 16;
    auto issueA = [&](int kt) {
      const unsigned sa = mA + (kt & 7) * C::STAGE;
      if (kt < 8) {
#pragma unroll
        for (int j = 0; j < 4; ++j) { const unsigned v = ((j & 1) ? va1 : va0) + j * 8 * 32; DMAQ_SC1(sa, j * 1024, v, srdA, (unsigned)(kt * 4 * 8192)); }
      } else {
#pragma unroll
        for (int j = 0; j < 4; ++j) { const unsigned v = ((j & 1) ? vt1 : vt0) + j * 8 * 128; DMAQ_SC1(sa, j * 1024, v, srdA, 0u); }
      }
    };
    auto issueB = [&](int kt) {
      const unsigned sb = mB + (kt & 7) * C::STAGE, sk = kt * WR_TB;
#pragma unroll
      for (int j = 0; j < 5; ++j) { const unsigned v = vb + j * 1024; DMAQ(sb, j * 1024, v, srdB, sk); }
    };
#pragma unroll
    for (int s = 0; s < 7; ++s) issueB(s);
    if (lane == 0) spin_until(cnt, 32u);
    asm volatile("" ::: "memory");
#pragma unroll
    for (int s = 0; s < 7; ++s) issueA(s);
    WAITV(24); BAR(); issueA(7); issueB(7);
    WAITV(29); BAR(); issueA(8); issueB(8);
    WAITV(34); BAR(); WAITV(30); BAR(); WAITV(26); BAR(); WAITV(22); BAR(); WAITV(18); BAR(); WAITV(9); BAR(); WAITV(0); BAR();
    BAR();
    return;
  }
  {
    const int g = lane >> 4, fr = lane & 15;
    const int tok = row0 + w * 16 + fr;
    const size_t ro = (size_t)tok * D_ + tn * 72 + 4 * g;
    u32x2 r[5];
#pragma unroll
    for (int j = 0; j < 5; ++j) r[j] = *(const u32x2*)(x + ro + ((j < 4 || g < 2) ? 16 * j : 0));
    f32x4 acc[1][5]; float rs[1], rq[1];
    consumer<C, 1, 5, 0, false, false>(smem, w * 16, lane, acc, rs, rq, ws, 7, 0, NoHook());
    u16* out = (u16*)(ws + WS_H);
    float s1 = 0.f, s2 = 0.f;
#pragma unroll
    for (int j = 0; j < 5; ++j) {
      if (j < 4 || g < 2) {
        u32x2 v;
        const unsigned p0 = cvtpk(acc[0][j][0], acc[0][j][1]), p1 = cvtpk(acc[0][j][2], acc[0][j][3]);
        v[0] = cvtpk(bflo(r[j][0]) + bflo(p0), bfhi(r[j][0]) + bfhi(p0));
        v[1] = cvtpk(bflo(r[j][1]) + bflo(p1), bfhi(r[j][1]) + bfhi(p1));
        *(u32x2*)(out + ro + 16 * j) = v;
        const float a0 = bflo(v[0]), a1 = bfhi(v[0]), a2 = bflo(v[1]), a3 = bfhi(v[1]);
        s1 += (a0 + a1) + (a2 + a3);
        s2 = fmaf(a0, a0, fmaf(a1, a1, fmaf(a2, a2, fmaf(a3, a3, s2))));
      }
    }
    s1 += xor16(s1); s1 += xor32(s1); s2 += xor16(s2); s2 += xor32(s2);
    if (g == 0) *(f32x2*)(ws + WS_HST + ((size_t)tok * 8 + tn) * 8) = f32x2{s1, s2};
  }
  STAMP_END(2);
}

constexpr int MST = 36864, MNST = 4;
constexpr int M_CSA = MNST * MST, M_CSB = M_CSA + 576, M_RS = M_CSB + 576, M_S2 = M_RS + 1024, M_LDS = M_S2 + 1024;
__global__ __launch_bounds__(448, 2) void k_mix(const u16* __restrict__ wsp, const u16* __restrict__ bsp, char* __restrict__ ws, u16* __restrict__ outp) {
  extern __shared__ __attribute__((aligned(16))) char smem[];
  const int tid = threadIdx.x, w = rfl(tid >> 6);
  const int b = blockIdx.x, xcd = b & 7, idx = b >> 3;
  const int tm = (xcd & 1) * 8 + (idx & 7), tn = (xcd >> 1) * 4 + (idx >> 3);
  char* ws_dbg = ws;
  unsigned* cnt1 = (unsigned*)(ws + WS_CNT + tm * 128);
  unsigned* cnt2 = (unsigned*)(ws + WS_CNT + (16 + tn) * 128);
  unsigned* cnt3 = (unsigned*)(ws + WS_CNT + (40 + tm) * 128);
  using CD = CR<36>;
  const int drow0 = tm * 128 + (tn >> 3) * 64, dtn = tn & 7;
  if (__builtin_expect(w == 4 || w == 5, 1)) {
    const int lane = tid & 63, p = w - 4;
    if (p == 0) STAMPW(3, 4);
    const unsigned ldsb = (unsigned)(size_t)smem;
    const unsigned mA = ldsb + p * 8192, mB = ldsb + 16384 + p * 9216;
    const unsigned sw0 = ((lane & 7) ^ (lane >> 4)) << 4, sw1 = ((lane & 7) ^ (4 + (lane >> 4))) << 4;
    const int g = lane >> 4;
    constexpr int NQ = 17;
    {
      const __amdgpu_buffer_rsrc_t srdA = mksrd((const u16*)(ws + WS_H) + (size_t)tm * 128 * D_, 128 * D_ * 2), srdB = mksrd(ws + WS_WIN, 32 * 9 * WIN_TB);
      const unsigned va0 = (p * 64 + (lane >> 3)) * (D_ * 2) + sw0, va1 = (p * 64 + (lane >> 3)) * (D_ * 2) + sw1, vb = p * 9216 + lane * 16;
      auto issue = [&](int s) {
        const int ph = s >= 9, kt = s - 9 * ph;
        const unsigned so = (s & 3) * MST, sa = mA + so, sb = mB + so;
        const unsigned soA = kt * 128, soB = ((ph ? tn : tn + 16) * 9 + kt) * WIN_TB;
#pragma unroll
        for (int j = 0; j < 8; ++j) { const unsigned v = ((j & 1) ? va1 : va0) + j * 8 * (D_ * 2); DMAQ(sa, j * 1024, v, srdA, soA); }
#pragma unroll
        for (int j = 0; j < 9; ++j) { const unsigned v = vb + j * 1024; DMAQ(sb, j * 1024, v, srdB, soB); }
      };
      issue(0); issue(1); issue(2);
      if (p == 0) STAMPW(3, 5);
      for (int s = 0; s < 15; ++s) { WAITV(2 * NQ); BAR(); issue(s + 3); }
      WAITV(2 * NQ); BAR(); WAITV(NQ); BAR(); WAITV(0); BAR();
    }
    BAR();
    BAR();
    if (p == 0) STAMPW(5, 0);
    {
      const __amdgpu_buffer_rsrc_t srdA = mksrd(wsp + (size_t)tm * 128 * S_, 128 * S_ * 2), srdB = mksrd((const u16*)(ws + WS_Z2N) + (size_t)tn * 144 * S_, 144 * S_ * 2);
      const unsigned va0 = (p * 64 + (lane >> 3)) * (S_ * 2) + sw0, va1 = (p * 64 + (lane >> 3)) * (S_ * 2) + sw1;
      const unsigned vb0 = (p * 72 + (lane >> 3)) * (S_ * 2) + (p ? sw1 : sw0), vb1 = (p * 72 + (lane >> 3)) * (S_ * 2) + (p ? sw0 : sw1);
      auto issueA = [&](int kt) {
        const unsigned sa = mA + (kt & 3) * MST, sk = kt * 128;
#pragma unroll
        for (int j = 0; j < 8; ++j) { const unsigned v = ((j & 1) ? va1 : va0) + j * 8 * (S_ * 2); DMAQ(sa, j * 1024, v, srdA, sk); }
      };
      auto issueB = [&](int kt) {
        const unsigned sb = mB + (kt & 3) * MST, sk = kt * 128;
#pragma unroll
        for (int j = 0; j < 9; ++j) { const unsigned v = ((j & 1) ? vb1 : vb0) + j * 8 * (S_ * 2); DMAQ_SC1(sb, j * 1024, v, srdB, sk); }
      };
      auto issue = [&](int kt) { issueA(kt); issueB(kt); };
      issueA(0); issueA(1); issueA(2);
      if (lane == 0) spin_until(cnt2, 16u);
      asm volatile("" ::: "memory");
      if (p == 0) STAMPW(5, 1);
      if (p == 0) STAMPW(5, 2);
      issueB(0); issueB(1); issueB(2);
      if (p == 0) STAMPW(5, 3);
      WAITV(18); BAR(); issue(3);
      WAITV(9 + NQ); BAR(); issue(4);
      for (int kt = 2; kt < 29; ++kt) { WAITV(2 * NQ); BAR(); issue(kt + 3); }
      WAITV(2 * NQ); BAR(); WAITV(NQ); BAR(); WAITV(0); BAR();
    }
    BAR();
    BAR();
    {
      const __amdgpu_buffer_rsrc_t srdA = mksrd((const u16*)(ws + WS_U) + (size_t)drow0 * FH, 64 * FH * 2), srdB = mksrd(ws + WS_WOUT + (size_t)dtn * 36 * WR_TB, 36 * WR_TB);
      const unsigned mA2 = ldsb + p * 4096, mB2 = ldsb + CD::ABYTES + p * 5120;
      const unsigned va0 = (p * 32 + (lane >> 3)) * (FH * 2) + sw0, va1 = (p * 32 + (lane >> 3)) * (FH * 2) + sw1, vb = p * 5120 + lane * 16;
      auto issueA = [&](int kt) {
        const unsigned sa = mA2 + (kt & 7) * CD::STAGE, sk = kt * 128;
#pragma unroll
        for (int j = 0; j < 4; ++j) { const unsigned v = ((j & 1) ? va1 : va0) + j * 8 * (FH * 2); DMAQ_SC1(sa, j * 1024, v, srdA, sk); }
      };
      auto issueB = [&](int kt) {
        const unsigned sb = mB2 + (kt & 7) * CD::STAGE, sk = kt * WR_TB;
#pragma unroll
        for (int j = 0; j < 5; ++j) { const unsigned v = vb + j * 1024; DMAQ(sb, j * 1024, v, srdB, sk); }
      };
#pragma unroll
      for (int s = 0; s < 7; ++s) issueB(s);
      if (lane == 0) spin_until(cnt3, 16u);
      asm volatile("" ::: "memory");
#pragma unroll
      for (int s = 0; s < 7; ++s) issueA(s);
      WAITV(24); BAR(); issueA(7); issueB(7);
      WAITV(29); BAR(); issueA(8); issueB(8);
      WAITV(34); BAR(); issueA(9); issueB(9);
      WAITV(39); BAR(); issueA(10); issueB(10);
      WAITV(44); BAR(); issueA(11); issueB(11);
      WAITV(49); BAR(); issueA(12); issueB(12);
      for (int kt = 6; kt < 29; ++kt) { WAITV(54); BAR(); issueA(kt + 7); issueB(kt + 7); }
      WAITV(54); BAR(); WAITV(45); BAR(); WAITV(36); BAR(); WAITV(27); BAR(); WAITV(18); BAR(); WAITV(9); BAR(); WAITV(0); BAR();
      BAR();
    }
    return;
  }
  if (w == 6) {
    const int lane = tid & 63;
    f32x2* rst = (f32x2*)(smem + M_RS);
#pragma unroll
    for (int i = 0; i < 2; ++i) {
      const f32x4* q = (const f32x4*)(ws + WS_HST + (size_t)(tm * 128 + 64 * i + lane) * 64);
      const f32x4 a = q[0], bb = q[1], c = q[2], d = q[3];
      const float s1 = (a[0] + a[2]) + (bb[0] + bb[2]) + (c[0] + c[2]) + (d[0] + d[2]), s2 = (a[1] + a[3]) + (bb[1] + bb[3]) + (c[1] + c[3]) + (d[1] + d[3]);
      rst[64 * i + lane] = ln_stats(s1, s2, 1.f / D_);
    }
    if (lane < 36) {
      *(f32x4*)(smem + M_CSA + lane * 16) = *(const f32x4*)(ws + WS_CS + (size_t)((tn + 16) * 144 + lane * 4) * 4);
      *(f32x4*)(smem + M_CSB + lane * 16) = *(const f32x4*)(ws + WS_CS + (size_t)(tn * 144 + lane * 4) * 4);
    }
    WAITL0();
    for (int s = 0; s < 15; ++s) BAR();
    if (lane == 0) spin_until(cnt1, 16u);
    asm volatile("" ::: "memory");
    const __amdgpu_buffer_rsrc_t srdS = mksrd(ws + WS_ST + (size_t)tm * (16 * 128 * 8), 16 * 128 * 8);
    u32x4 pv[16];
#pragma unroll
    for (int q = 0; q < 16; ++q) pv[q] = __builtin_amdgcn_raw_buffer_load_b128(srdS, (q * 128 + 2 * lane) * 8, 0, 16);
    SB(); BAR(); BAR(); SB();
    f32x4 t4 = {0.f, 0.f, 0.f, 0.f};
#pragma unroll
    for (int q = 0; q < 16; ++q) t4 += __builtin_bit_cast(f32x4, pv[q]);
    {
      const f32x2 q0 = ln_stats(t4[0], t4[1], 1.f / FH), q1 = ln_stats(t4[2], t4[3], 1.f / FH);
      *(f32x4*)(smem + M_S2 + lane * 16) = f32x4{q0[1], -q0[0] * q0[1], q1[1], -q1[0] * q1[1]};
    }
    WAITL0();
    BAR();
    BAR();
    return;
  }
  const int lane = tid & 63, g = lane >> 4, fr = lane & 15, f = fr >> 1;
  const int l0 = fr * 128 + ((g ^ f) << 4), l1 = fr * 128 + (((4 + g) ^ f) << 4);
  const int tb = 16384 + (16 * (g >> 1) + 4 * (g & 1) + (fr >> 2)) * 288 + (lane & 3) * 8;
  const int tok0 = tm * 128 + w * 32;
  f32x4 acc[2][9];
  bf16x8 a0[2], b0[9], a1[2], b1[9];
  auto zero_acc = [&]() {
#pragma unroll
    for (int i = 0; i < 2; ++i)
#pragma unroll
      for (int j = 0; j < 9; ++j) acc[i][j] = f32x4{0.f, 0.f, 0.f, 0.f};
  };
  auto mm = [&](bf16x8* a, bf16x8* bb) {
#pragma unroll
    for (int j = 0; j < 9; ++j)
#pragma unroll
      for (int i = 0; i < 2; ++i) acc[i][j] = MFMA16(bb[j], a[i], acc[i][j]);
  };
  auto rdKN = [&](bf16x8* a, bf16x8* bb, int s, int ks) {
    const char* sb = smem + (s & 3) * MST;
#pragma unroll
    for (int i = 0; i < 2; ++i) a[i] = *(const bf16x8*)(sb + w * 4096 + i * 2048 + (ks ? l1 : l0));
#pragma unroll
    for (int j = 0; j < 9; ++j) { const char* q = sb + tb + ks * (32 * 288) + j * 32; bb[j] = cat8(trread(q), trread(q + 8 * 288)); }
  };
  auto rdNK = [&](bf16x8* a, bf16x8* bb, int s, int ks) {
    const char* sb = smem + (s & 3) * MST + (ks ? l1 : l0);
#pragma unroll
    for (int i = 0; i < 2; ++i) a[i] = *(const bf16x8*)(sb + w * 4096 + i * 2048);
#pragma unroll
    for (int j = 0; j < 9; ++j) bb[j] = *(const bf16x8*)(sb + 16384 + j * 2048);
  };
#define SCHED(id, ND, RPG) _Pragma("unroll") for (int q = 0; q < 18; ++q) { __builtin_amdgcn_sched_group_barrier(0x008, 1, id); \
    if (q * RPG < ND) __builtin_amdgcn_sched_group_barrier(0x080, RPG, id); }
  auto gelu = [&](u32x2 (&zp)[2][9], int csoff, f32x2 (&sum)[2]) {
    const float c1 = -2.f * 0.7978845608028654f * 1.4426950408889634f, c2 = c1 * 0.044715f;
    const float* cs = (const float*)(smem + csoff);
    const f32x2* rst = (const f32x2*)(smem + M_RS) + w * 32;
    float nmu[2], k1[2], k3[2], irs[2];
    f32x2 s1[2], s2[2];
#pragma unroll
    for (int i = 0; i < 2; ++i) {
      const f32x2 st = rst[16 * i + fr];
      nmu[i] = -st[0]; k1[i] = c1 * st[1]; k3[i] = c2 * st[1] * st[1] * st[1]; irs[i] = __builtin_amdgcn_rcpf(st[1]);
      s1[i] = f32x2{0.f, 0.f}; s2[i] = f32x2{0.f, 0.f};
    }
    f32x4 c4 = *(const f32x4*)(cs + 4 * g);
#pragma unroll
    for (int j = 0; j < 9; ++j) {
      const f32x4 c4n = *(const f32x4*)(cs + 16 * (j < 8 ? j + 1 : 8) + 4 * g);
      SB();
#pragma unroll
      for (int i = 0; i < 2; ++i)
#pragma unroll
        for (int hh = 0; hh < 2; ++hh) {
          const f32x2 a2 = {acc[i][j][2 * hh], acc[i][j][2 * hh + 1]}, c2v = {c4[2 * hh], c4[2 * hh + 1]};
          const f32x2 t = f32x2{nmu[i], nmu[i]} * c2v + a2;
          const f32x2 y = t * (t * t * f32x2{k3[i], k3[i]} + f32x2{k1[i], k1[i]});
          const f32x2 e = {__builtin_amdgcn_exp2f(y[0]), __builtin_amdgcn_exp2f(y[1])};
          const f32x2 d = e * f32x2{irs[i], irs[i]} + f32x2{irs[i], irs[i]};
          const f32x2 z = t * f32x2{__builtin_amdgcn_rcpf(d[0]), __builtin_amdgcn_rcpf(d[1])};
          s1[i] += z; s2[i] += z * z;
          zp[i][j][hh] = cvtpk(z[0], z[1]);
        }
      VREG(zp[0][j]); VREG(zp[1][j]);
      c4 = c4n;
      SB();
    }
#pragma unroll
    for (int i = 0; i < 2; ++i) sum[i] = f32x2{s1[i][0] + s1[i][1], s2[i][0] + s2[i][1]};
  };
  const __amdgpu_buffer_rsrc_t srdS = mksrd(ws + WS_ST + (size_t)tm * (16 * 128 * 8), 16 * 128 * 8);
  zero_acc();
  STAMP(3, 0);
  BAR();
  STAMP(3, 1);
  rdKN(a0, b0, 0, 0);
  u32x2 z2p[2][9];
  auto stageAB = [&](int s) {
    SB(); rdKN(a1, b1, s, 1); mm(a0, b0); SCHED(0, 20, 2) SB();
    WAITL0(); BAR();
    SB(); rdKN(a0, b0, s + 1, 0); mm(a1, b1); SCHED(1, 20, 2)
  };
  for (int s = 0; s < 9; ++s) stageAB(s);
  SB();
  STAMP(5, 6);
  {
    f32x2 sum[2];
    gelu(z2p, M_CSA, sum);
#pragma unroll
    for (int i = 0; i < 2; ++i) {
      f32x2 v = sum[i];
      v[0] += xor16(v[0]); v[0] += xor32(v[0]); v[1] += xor16(v[1]); v[1] += xor32(v[1]);
      if (g == 0) __builtin_amdgcn_raw_buffer_store_b64(__builtin_bit_cast(u32x2, v), srdS, (tn * 128 + w * 32 + 16 * i + fr) * 8, 0, 16);
    }
    asm volatile("s_waitcnt vmcnt(0)" ::: "memory");
    zero_acc();
  }
  SB();
  STAMP(5, 4);
  stageAB(9);
  if (w == 0 && lane == 0) __hip_atomic_fetch_add(cnt1, 1u, __ATOMIC_RELAXED, __HIP_MEMORY_SCOPE_AGENT);
  for (int s = 10; s < 17; ++s) stageAB(s);
  SB(); rdKN(a1, b1, 17, 1); mm(a0, b0); mm(a1, b1); SB();
  WAITL0(); BAR();
  STAMP(3, 2);
  {
    const f32x2* st2 = (const f32x2*)(smem + M_S2) + w * 32;
    char* reg = smem + w * (32 * 304);
#pragma unroll
    for (int i = 0; i < 2; ++i) {
      const f32x2 st = st2[16 * i + fr];
#pragma unroll
      for (int j = 0; j < 9; ++j) {
        u32x2 v;
        v[0] = cvtpk(fmaf(bflo(z2p[i][j][0]), st[0], st[1]), fmaf(bfhi(z2p[i][j][0]), st[0], st[1]));
        v[1] = cvtpk(fmaf(bflo(z2p[i][j][1]), st[0], st[1]), fmaf(bfhi(z2p[i][j][1]), st[0], st[1]));
        *(u32x2*)(reg + (16 * i + fr) * 304 + (16 * j + 4 * g) * 2) = v;
      }
    }
    WAITL0();
    const __amdgpu_buffer_rsrc_t so = mksrd((u16*)(ws + WS_Z2N) + (size_t)(tn * 144) * S_ + tok0, 144 * S_ * 2);
    const char* rp = reg + (8 * g + (fr >> 2)) * 304 + (lane & 3) * 8;
#pragma unroll
    for (int j = 0; j < 9; ++j)
      __builtin_amdgcn_raw_buffer_store_b128(__builtin_bit_cast(u32x4, cat8(trread(rp + j * 32), trread(rp + 4 * 304 + j * 32))), so, (j * 16 + fr) * (S_ * 2) + 8 * g * 2, 0, 16);
    asm volatile("s_waitcnt vmcnt(0)" ::: "memory");
    WAITL0(); BAR();
    if (w == 0 && lane == 0) __hip_atomic_fetch_add(cnt2, 1u, __ATOMIC_RELAXED, __HIP_MEMORY_SCOPE_AGENT);
  }
  u32x2 z1p[2][9];
  { f32x2 sum[2]; gelu(z1p, M_CSB, sum); }
  const unsigned bias0 = bsp[tok0 + fr], bias1 = bsp[tok0 + 16 + fr];
  zero_acc();
  STAMP(4, 0);
  BAR();
  STAMP(4, 1);
  rdNK(a0, b0, 0, 0);
  for (int kt = 0; kt < 31; ++kt) {
    SB(); rdNK(a1, b1, kt, 1); mm(a0, b0); SCHED(0, 11, 1) SB();
    WAITL0(); BAR();
    SB(); rdNK(a0, b0, kt + 1, 0); mm(a1, b1); SCHED(1, 11, 1)
  }
  SB(); rdNK(a1, b1, 31, 1); mm(a0, b0); mm(a1, b1); SB();
  WAITL0(); BAR();
  SB();
  STAMP(4, 2);
#undef SCHED
  char* reg = smem + w * (32 * 304);
#pragma unroll
  for (int i = 0; i < 2; ++i) {
    const float bi = bflo(i ? bias1 : bias0);
#pragma unroll
    for (int j = 0; j < 9; ++j) {
      u32x2 v;
      const unsigned e0 = cvtpk(acc[i][j][0], acc[i][j][1]), e1 = cvtpk(acc[i][j][2], acc[i][j][3]);
      const unsigned g0 = cvtpk(bflo(e0) + bi, bfhi(e0) + bi), g1 = cvtpk(bflo(e1) + bi, bfhi(e1) + bi);
      v[0] = cvtpk(bflo(z1p[i][j][0]) * bflo(g0), bfhi(z1p[i][j][0]) * bfhi(g0));
      v[1] = cvtpk(bflo(z1p[i][j][1]) * bflo(g1), bfhi(z1p[i][j][1]) * bfhi(g1));
      *(u32x2*)(reg + (16 * i + fr) * 304 + (16 * j + 4 * g) * 2) = v;
    }
  }
  WAITL0();
  const __amdgpu_buffer_rsrc_t so = mksrd((u16*)(ws + WS_U) + (size_t)tok0 * FH + tn * 144, 32 * FH * 2);
#pragma unroll
  for (int i = 0; i < 9; ++i) {
    const int c = i * 64 + lane, row = c / 18, ch = c - row * 18;
    __builtin_amdgcn_raw_buffer_store_b128(*(const u32x4*)(reg + row * 304 + ch * 16), so, row * (FH * 2) + ch * 16, 0, 16);
  }
  asm volatile("s_waitcnt vmcnt(0)" ::: "memory");
  WAITL0(); BAR();
  STAMP(4, 3);
  if (w == 0 && lane == 0) __hip_atomic_fetch_add(cnt3, 1u, __ATOMIC_RELAXED, __HIP_MEMORY_SCOPE_AGENT);
  {
    const int tok = drow0 + w * 16 + fr;
    const size_t ro = (size_t)tok * D_ + dtn * 72 + 4 * g;
    const u16* res = (const u16*)(ws + WS_H);
    u32x2 r[5];
#pragma unroll
    for (int j = 0; j < 5; ++j) r[j] = *(const u32x2*)(res + ro + ((j < 4 || g < 2) ? 16 * j : 0));
    f32x4 acd[1][5]; float rs[1], rq[1];
    consumer<CD, 1, 5, 0, false, false>(smem, w * 16, lane, acd, rs, rq, ws, 6, 0, NoHook());
#pragma unroll
    for (int j = 0; j < 5; ++j) {
      if (j < 4 || g < 2) {
        const unsigned p0 = cvtpk(acd[0][j][0], acd[0][j][1]), p1 = cvtpk(acd[0][j][2], acd[0][j][3]);
        u32x2 v;
        v[0] = cvtpk(bflo(r[j][0]) + bflo(p0), bfhi(r[j][0]) + bfhi(p0));
        v[1] = cvtpk(bflo(r[j][1]) + bflo(p1), bfhi(r[j][1]) + bfhi(p1));
        *(u32x2*)(outp + ro + 16 * j) = v;
      }
    }
  }
}

extern "C" void kernel_launch(void* const* d_in, const int* in_sizes, int n_in, void* d_out, int out_size, void* d_ws, size_t ws_size, hipStream_t stream) {
  const u16* x = (const u16*)d_in[0];
  const u16* wqkv = (const u16*)d_in[1];
  const u16* wo = (const u16*)d_in[2];
  const u16* win = (const u16*)d_in[4];
  const u16* wsp = (const u16*)d_in[5];
  const u16* bsp = (const u16*)d_in[6];
  const u16* wout = (const u16*)d_in[7];
  char* ws = (char*)d_ws;
  (void)hipFuncSetAttribute((const void*)k_qkv, hipFuncAttributeMaxDynamicSharedMemorySize, C1::LDS);
  (void)hipFuncSetAttribute((const void*)k_attn, hipFuncAttributeMaxDynamicSharedMemorySize, ATT_LDS);
  (void)hipFuncSetAttribute((const void*)k_mix, hipFuncAttributeMaxDynamicSharedMemorySize, M_LDS);
  hipLaunchKernelGGL(k_qkv, dim3(256), dim3(512), C1::LDS, stream, x, wqkv, win, wout, wo, ws);
  hipLaunchKernelGGL(k_attn, dim3(256), dim3(512), ATT_LDS, stream, ws, wsp, x);
  hipLaunchKernelGGL(k_mix, dim3(256), dim3(448), M_LDS, stream, wsp, bsp, ws, (u16*)d_out);
}
```

```cpp
#include <hip/hip_runtime.h>
#include <stdint.h>

typedef unsigned short u16;
typedef short bf16x8 __attribute__((ext_vector_type(8)));
typedef short s16x4 __attribute__((ext_vector_type(4)));
typedef float f32x2 __attribute__((ext_vector_type(2)));
typedef float f32x4 __attribute__((ext_vector_type(4)));
typedef float f32x16 __attribute__((ext_vector_type(16)));
typedef unsigned u32x2 __attribute__((ext_vector_type(2)));
typedef unsigned u32x4 __attribute__((ext_vector_type(4)));
typedef __bf16 bf16x2_t __attribute__((ext_vector_type(2)));
typedef __attribute__((address_space(3))) s16x4* lds_s16x4_ptr;
#define DI __device__ __forceinline__

constexpr int S_ = 2048, D_ = 576, H_ = 32, DH = 18, FH = 2304, F2 = 4608;
constexpr int QK_TILE = 1152;
constexpr int V_OFF = 1152;
constexpr int KV_TILE = 1152 + 4 * 304;
constexpr int KV_HEAD = 64 * KV_TILE;
constexpr int Q_HEAD = 64 * QK_TILE;
constexpr int WIN_TB = 64 * 288, WR_TB = 64 * 160;
constexpr size_t WS_Q = 0;
constexpr size_t WS_KV = WS_Q + (size_t)H_ * Q_HEAD;
constexpr size_t WS_AO = WS_KV + (size_t)H_ * KV_HEAD + 8192;
constexpr size_t WS_H = WS_AO + (size_t)S_ * D_ * 2;
constexpr size_t WS_Z2N = WS_H + (size_t)S_ * D_ * 2;
constexpr size_t WS_ST = WS_Z2N + (size_t)S_ * FH * 2;
constexpr size_t WS_CNT = WS_ST + (size_t)S_ * 16 * 2 * 4;
constexpr size_t WS_HST = WS_CNT + 8192;
constexpr size_t WS_U0 = WS_HST + (size_t)S_ * 8 * 2 * 4;
constexpr size_t WS_U = 0;
static_assert((size_t)S_ * FH * 2 <= WS_H, "u must fit below h");
constexpr size_t WS_WIN = WS_U0;
constexpr size_t WS_WOUT = WS_WIN + (size_t)32 * 9 * WIN_TB;
constexpr size_t WS_WO = WS_WOUT + (size_t)8 * 36 * WR_TB;
constexpr size_t WS_CS = WS_WO + (size_t)8 * 9 * WR_TB;
constexpr size_t WS_END = WS_CS + (size_t)F2 * 4;

DI unsigned cvtpk(float lo, float hi) { f32x2 v = {lo, hi}; bf16x2_t b = __builtin_convertvector(v, bf16x2_t); return __builtin_bit_cast(unsigned, b); }
DI float bflo(unsigned u) { return __uint_as_float(u << 16); }
DI float bfhi(unsigned u) { return __uint_as_float(u & 0xffff0000u); }
DI float bfr(float x) { return __uint_as_float(cvtpk(x, x) << 16); }
DI f32x2 ln_stats(float s1, float s2, float inv_n) {
  const float mu = s1 * inv_n, var = fmaxf(s2 * inv_n - mu * mu, 0.f);
  return f32x2{bfr(mu), 1.f / bfr(sqrtf(bfr(bfr(var) + 1e-5f)))};
}
DI unsigned rfl(unsigned x) { return __builtin_amdgcn_readfirstlane(x); }
DI float xor16(float v) { return __shfl_xor(v, 16); }
DI float xor32(float v) { return __shfl_xor(v, 32); }
#define DMAQ(sb, imm, voff, srd, soff) asm volatile("s_add_u32 m0, %0, %1\n\ts_nop 0\n\tbuffer_load_dwordx4 %2, %3, %4 offen lds" :: "s"(sb), "i"(imm), "v"(voff), "s"(srd), "s"(soff) : "scc")
#define DMAQ_SC1(sb, imm, voff, srd, soff) asm volatile("s_add_u32 m0, %0, %1\n\ts_nop 0\n\tbuffer_load_dwordx4 %2, %3, %4 offen sc1 lds" :: "s"(sb), "i"(imm), "v"(voff), "s"(srd), "s"(soff) : "scc")
#define WAITV(n) asm volatile("s_waitcnt vmcnt(%0)" :: "i"(n) : "memory")
#define WAITL0() asm volatile("s_waitcnt lgkmcnt(0)" ::: "memory")
#define BAR() asm volatile("s_barrier" ::: "memory")
#define SB() __builtin_amdgcn_sched_barrier(0)
#define MFMA16(a, b, c) __builtin_amdgcn_mfma_f32_16x16x32_bf16((a), (b), (c), 0, 0, 0)
#define MFMA32(a, b, c) __builtin_amdgcn_mfma_f32_32x32x16_bf16((a), (b), (c), 0, 0, 0)
#define VREG(x) asm volatile("" : "+v"(x))
DI __amdgpu_buffer_rsrc_t mksrd(const void* p, unsigned bytes) { return __builtin_amdgcn_make_buffer_rsrc((void*)p, (short)0, (int)bytes, 0x00020000); }
DI s16x4 trread(const char* p) { return __builtin_amdgcn_ds_read_tr16_b64_v4i16((lds_s16x4_ptr)(p)); }
DI bf16x8 cat8(s16x4 lo, s16x4 hi) { return __builtin_shufflevector(lo, hi, 0, 1, 2, 3, 4, 5, 6, 7); }
DI bf16x8 ones8() { const u32x4 o = {0x3F803F80u, 0x3F803F80u, 0x3F803F80u, 0x3F803F80u}; return __builtin_bit_cast(bf16x8, o); }
#define ROT1 (3 * (((idx >> 2) + (idx & 3)) % 3))
#define ST_AUX 16
DI void st16(__amdgpu_buffer_rsrc_t srd, unsigned off, u32x4 v) { __builtin_amdgcn_raw_buffer_store_b128(v, srd, off, 0, ST_AUX); }
#define TOUCH(dummy, ptr) asm volatile("global_load_dword %0, %1, off" : "+v"(dummy) : "v"(ptr))
#define STAMP(kid, slot)
#define STAMPW(kid, slot)
#define STAMP_END(kid)

enum { B_NK = 0, B_KN = 1, B_TILED = 2 };
template <int BM_, int NT_, int NP_, int NST_, int BMODE_, int KT_, bool COLSUM_>
struct Cfg {
  static constexpr int BM = BM_, NT = NT_, NP = NP_, NST = NST_, KT = KT_, BMODE = BMODE_;
  static constexpr bool BKN = BMODE_ != B_NK, COLSUM = COLSUM_;
  static constexpr int BN = NT * 16;
  static constexpr int NA = BM / 8, NB = BN / 8, NI = NA + NB;
  static constexpr int ABYTES = BM * 128, BBYTES = BN * 128, STAGE = ABYTES + BBYTES;
  static constexpr int RING = NST * STAGE;
  static constexpr int CS_OFF = RING;
  static constexpr int RS_OFF = RING + BN * 4;
  static constexpr int LDS = RS_OFF + BM * 8;
};

template <class C, int TAILN = 0, int TCPR = 1>
DI void run_producer(char* smem, const void* Ab, unsigned abytes, const void* Bb, unsigned bbytes, int lda, int ldb, int p, int lane, char* ws_dbg, int kid,
                     int rot = 0, const void* Tb = nullptr, unsigned tbytes = 0, int ldt = 0) {
  if (p == 0) STAMPW(kid, 4);
  constexpr int HA = C::NA / 2, HB = C::NB / 2, NQ = HA + HB;
  static_assert(C::NA % 4 == 0 && C::NB % 2 == 0, "piece split");
  const __amdgpu_buffer_rsrc_t srdA = mksrd(Ab, abytes), srdB = mksrd(Bb, bbytes);
  const unsigned ldsb = (unsigned)(size_t)smem;
  const unsigned mA = ldsb + p * (HA * 1024), mB = ldsb + C::ABYTES + p * (HB * 1024);
  const unsigned sw0 = ((lane & 7) ^ (lane >> 4)) << 4, sw1 = ((lane & 7) ^ (4 + (lane >> 4))) << 4;
  const unsigned va0 = (p * HA * 8 + (lane >> 3)) * (lda * 2) + sw0, va1 = (p * HA * 8 + (lane >> 3)) * (lda * 2) + sw1;
  unsigned vb[HB];
#pragma unroll
  for (int j = 0; j < HB; ++j) {
    if (C::BMODE == B_NK) {
      const int q = p * HB + j;
      vb[j] = (8 * q + (lane >> 3)) * (ldb * 2) + ((q & 1) ? sw1 : sw0);
    } else if (C::BMODE == B_KN) {
      constexpr int CPR = C::BN / 8;
      const int c = 64 * (p * HB + j) + lane, rho = c / CPR, ch = c - rho * CPR;
      const int k = (rho & ~12) | ((rho & 4) << 1) | ((rho & 8) >> 1);
      vb[j] = k * (ldb * 2) + ch * 16;
    } else {
      vb[j] = (p * HB + j) * 1024 + lane * 16;
    }
  }
  auto issue = [&](int kt) {
    const unsigned so = (kt % C::NST) * C::STAGE, sa = mA + so, sb = mB + so;
    int kr = kt + rot; if (kr >= C::KT) kr -= C::KT;
    const unsigned soA = kr * 128, soB = C::BMODE == B_NK ? kr * 128 : (C::BMODE == B_KN ? kr * 64 * ldb * 2 : kr * C::BBYTES);
#pragma unroll
    for (int j = 0; j < HA; ++j) { const unsigned v = ((j & 1) ? va1 : va0) + j * 8 * (lda * 2); DMAQ(sa, j * 1024, v, srdA, soA); }
#pragma unroll
    for (int j = 0; j < HB; ++j) DMAQ(sb, j * 1024, vb[j], srdB, soB);
  };
  constexpr int PF = C::NST - 1;
#pragma unroll
  for (int s = 0; s < PF; ++s) issue(s);
  if (p == 0) STAMPW(kid, 5);
  constexpr int NJ = C::COLSUM ? (C::NT + 1) / 2 : 0;
  f32x4 cs[NJ > 0 ? NJ : 1];
#pragma unroll
  for (int j = 0; j < NJ; ++j) cs[j] = f32x4{0.f, 0.f, 0.f, 0.f};
  const int g = lane >> 4;
  const int tb = C::ABYTES + (16 * (g >> 1) + 4 * (g & 1) + ((lane & 15) >> 2)) * (C::BN * 2) + (lane & 3) * 8 + p * 32;
  auto colsum = [&](int kt) {
    if constexpr (C::COLSUM) {
      const char* sb = smem + (kt % C::NST) * C::STAGE + tb;
#pragma unroll
      for (int jj = 0; jj < NJ; ++jj) {
#pragma unroll
        for (int ks = 0; ks < 2; ++ks) {
          const char* q = sb + ks * (32 * C::BN * 2) + jj * 64;
          cs[jj] = MFMA16(cat8(trread(q), trread(q + 8 * C::BN * 2)), ones8(), cs[jj]);
        }
      }
      WAITL0();
    }
  };
  for (int kt = 0; kt < C::KT - PF; ++kt) { WAITV((PF - 1) * NQ); BAR(); issue(kt + PF); colsum(kt); }
  constexpr int NTL = TAILN / 4;
  auto tail = [&](int half) {
    const unsigned sb = ldsb + ((C::KT + half) % C::NST) * C::STAGE + p * (NTL * 1024);
#pragma unroll
    for (int j = 0; j < NTL; ++j) {
      const int c = 64 * (half * 2 * NTL + p * NTL + j) + lane, row = c / TCPR, ch = c - row * TCPR;
      const unsigned vo2 = row * ldt + ch * 16;
      DMAQ(sb, j * 1024, vo2, mksrd(Tb, tbytes), 0u);
    }
  };
#pragma unroll
  for (int r = PF - 1; r >= 0; --r) {
    WAITV(r * NQ + (TAILN > 0 ? (r < PF - 1 ? NTL : 0) + (r < PF - 2 ? NTL : 0) : 0)); BAR();
    if (TAILN > 0 && r == PF - 1) tail(0);
    if (TAILN > 0 && r == PF - 2) tail(1);
    colsum(C::KT - 1 - r);
  }
  if (TAILN > 0) WAITV(0);
  if constexpr (C::COLSUM) {
    float* st = (float*)(smem + C::CS_OFF);
#pragma unroll
    for (int jj = 0; jj < NJ; ++jj) {
      if ((lane & 15) == 0 && p + 2 * jj < C::NT) *(f32x4*)(st + 16 * (p + 2 * jj) + 4 * g) = cs[jj];
    }
    WAITL0();
  }
  BAR();
}

struct NoHook { DI void operator()() const {} };
template <class C, int MI, int NTW, int J0, bool ROLL, bool ROWSTAT, class Hook>
DI void consumer(char* smem, int row0, int lane, f32x4 (&acc)[MI][NTW], float (&rs)[MI], float (&rq)[MI], char* ws_dbg, int kid, int hook_kt, Hook hook) {
  const int g = lane >> 4, fr = lane & 15, f = fr >> 1;
  const int la0 = row0 * 128 + fr * 128 + ((g ^ f) << 4), la1 = row0 * 128 + fr * 128 + (((4 + g) ^ f) << 4);
  const int lb0 = C::ABYTES + J0 * 2048 + fr * 128 + ((g ^ f) << 4), lb1 = C::ABYTES + J0 * 2048 + fr * 128 + (((4 + g) ^ f) << 4);
  const int tb = C::ABYTES + J0 * 32 + (16 * (g >> 1) + 4 * (g & 1) + (fr >> 2)) * (C::BN * 2) + (lane & 3) * 8;
  f32x4 ars[MI], arq[MI];
#pragma unroll
  for (int i = 0; i < MI; ++i) {
    ars[i] = f32x4{0.f, 0.f, 0.f, 0.f}; arq[i] = f32x4{0.f, 0.f, 0.f, 0.f};
#pragma unroll
    for (int j = 0; j < NTW; ++j) acc[i][j] = f32x4{0.f, 0.f, 0.f, 0.f};
  }
  bf16x8 a0[MI], b0[NTW], a1[MI], b1[ROLL ? 1 : NTW];
  auto rda = [&](bf16x8* a, int kt, int ks) {
    const char* sb = smem + (kt % C::NST) * C::STAGE + (ks ? la1 : la0);
#pragma unroll
    for (int i = 0; i < MI; ++i) a[i] = *(const bf16x8*)(sb + i * 2048);
  };
  auto rdb1 = [&](int j, int kt, int ks) -> bf16x8 {
    const char* sb = smem + (kt % C::NST) * C::STAGE;
    if constexpr (C::BKN) {
      const char* p = sb + tb + ks * (32 * C::BN * 2) + j * 32;
      return cat8(trread(p), trread(p + 8 * C::BN * 2));
    } else {
      return *(const bf16x8*)(sb + (ks ? lb1 : lb0) + j * 2048);
    }
  };
  auto stats = [&](bf16x8* a) {
    if constexpr (ROWSTAT) {
#pragma unroll
      for (int i = 0; i < MI; ++i) { ars[i] = MFMA16(ones8(), a[i], ars[i]); arq[i] = MFMA16(a[i], a[i], arq[i]); }
    }
  };
  STAMP(kid, 0);
  BAR();
  STAMP(kid, 1);
  if constexpr (!ROLL) {
    constexpr int NM = MI * NTW + (ROWSTAT ? 2 * MI : 0), ND = MI + NTW * (C::BKN ? 2 : 1), RPG = (ND + (NM * 2 / 3) - 1) / (NM * 2 / 3);
#define SCHED(id) _Pragma("unroll") for (int q = 0; q < NM; ++q) { __builtin_amdgcn_sched_group_barrier(0x008, 1, id); \
    if (q * RPG < ND) __builtin_amdgcn_sched_group_barrier(0x080, RPG, id); }
    auto rd = [&](bf16x8* a, bf16x8* bb, int kt, int ks) {
      rda(a, kt, ks);
#pragma unroll
      for (int j = 0; j < NTW; ++j) bb[j] = rdb1(j, kt, ks);
    };
    auto mm = [&](bf16x8* a, bf16x8* bb) {
#pragma unroll
      for (int j = 0; j < NTW; ++j)
#pragma unroll
        for (int i = 0; i < MI; ++i) acc[i][j] = MFMA16(bb[j], a[i], acc[i][j]);
      stats(a);
    };
    auto body = [&](int kt) {
      SB();
      rd(a1, b1, kt, 1);
      mm(a0, b0);
      SCHED(0)
      SB();
      WAITL0(); BAR();
      SB();
      rd(a0, b0, kt + 1, 0);
      mm(a1, b1);
      SCHED(1)
    };
    rd(a0, b0, 0, 0);
    if (hook_kt > 0) {
      for (int kt = 0; kt < hook_kt; ++kt) body(kt);
      SB(); hook(); SB();
      for (int kt = hook_kt; kt < C::KT - 1; ++kt) body(kt);
    } else {
      for (int kt = 0; kt < C::KT - 1; ++kt) body(kt);
    }
    SB();
    rd(a1, b1, C::KT - 1, 1);
    mm(a0, b0);
    mm(a1, b1);
#undef SCHED
  } else {
    auto sub = [&](bf16x8* a, int kt, int ks, bool more) {
#pragma unroll
      for (int j = 0; j < NTW; ++j) {
#pragma unroll
        for (int i = 0; i < MI; ++i) acc[i][j] = MFMA16(b0[j], a[i], acc[i][j]);
        if (more) b0[j] = rdb1(j, kt, ks);
        SB();
      }
      stats(a);
    };
    rda(a0, 0, 0);
#pragma unroll
    for (int j = 0; j < NTW; ++j) b0[j] = rdb1(j, 0, 0);
    for (int kt = 0; kt < C::KT - 1; ++kt) {
      rda(a1, kt, 1);
      sub(a0, kt, 1, true);
      WAITL0(); BAR();
      rda(a0, kt + 1, 0);
      sub(a1, kt + 1, 0, true);
    }
    rda(a1, C::KT - 1, 1);
    sub(a0, C::KT - 1, 1, true);
    sub(a1, 0, 0, false);
  }
  if constexpr (ROWSTAT) {
#pragma unroll
    for (int i = 0; i < MI; ++i) {
      rs[i] = ars[i][0];
      const float d = (fr & 2) ? ((fr & 1) ? arq[i][3] : arq[i][2]) : ((fr & 1) ? arq[i][1] : arq[i][0]);
      rq[i] = __shfl(d, fr + 16 * (fr >> 2));
    }
  }
  SB();
  WAITL0(); BAR();
  SB();
  STAMP(kid, 2);
}

using C1 = Cfg<128, 9, 4, 4, B_KN, 9, false>;
constexpr int RU1 = 32 * 9 * 18, RU2 = RU1 + 8 * 36 * 10, RU3 = RU2 + 8 * 9 * 10;
DI const u16* retile_src(const u16* __restrict__ win, const u16* __restrict__ wout, const u16* __restrict__ wo, int u, int lane) {
  if (u < RU1) {
    const int tile = u / 18, q = u - tile * 18, tn = tile / 9, kt = tile - tn * 9;
    const int c = 64 * q + lane, rho = c / 18, ch = c - rho * 18;
    const int k = (rho & ~12) | ((rho & 4) << 1) | ((rho & 8) >> 1);
    return win + (size_t)(kt * 64 + k) * F2 + tn * 144 + ch * 8;
  }
  const bool o = u < RU2; const int v = u - (o ? RU1 : RU2), KT = o ? 36 : 9;
  const int tile = v / 10, q = v - tile * 10, tn = tile / KT, kt = tile - tn * KT;
  const int c = 64 * q + lane, rho = c / 10, ch = c - rho * 10;
  const int k = (rho & ~12) | ((rho & 4) << 1) | ((rho & 8) >> 1);
  int col = tn * 72 + ch * 8; if (col > D_ - 8) col = D_ - 8;
  int kr = kt * 64 + k;
  if (!o) kr = kr < 512 ? 18 * (kr >> 4) + (kr & 15) : 18 * ((kr - 512) >> 1) + 16 + (kr & 1);
  return (o ? wout : wo) + (size_t)kr * D_ + col;
}
__global__ __launch_bounds__(512, 2) void k_qkv(const u16* __restrict__ x, const u16* __restrict__ wqkv, const u16* __restrict__ win, const u16* __restrict__ wout,
                                                const u16* __restrict__ wo, char* __restrict__ ws) {
  extern __shared__ __attribute__((aligned(16))) char smem[];
  const int tid = threadIdx.x, w = rfl(tid >> 6);
  const int b = blockIdx.x, xcd = b & 7, idx = b >> 3;
  char* ws_dbg = ws;
  if (b == 255 && tid < 56) *(unsigned*)(ws + WS_CNT + tid * 128) = 0u;
  if (idx >= 24) {
    STAMP(0, 0); STAMP(0, 1); STAMP(0, 2);
    const int pw = (idx - 24) * 8 + xcd, lane = tid & 63, gw = pw * 8 + w;
    constexpr int NU = (RU3 + 511) / 512;
    u32x4 v[NU];
#pragma unroll
    for (int i = 0; i < NU; ++i) { const int u = gw + 512 * i; v[i] = *(const u32x4*)retile_src(win, wout, wo, u < RU3 ? u : gw, lane); }
    const int cc = tid % 9, rg = tid / 9;
    u32x4 cv[11];
    if (rg < 56) {
#pragma unroll
      for (int i = 0; i < 11; ++i) { const int r = rg + 56 * i; cv[i] = *(const u32x4*)(win + (size_t)(r < D_ ? r : rg) * F2 + pw * 72 + cc * 8); }
    }
    const __amdgpu_buffer_rsrc_t so = mksrd(ws + WS_WIN, RU3 * 1024);
#pragma unroll
    for (int i = 0; i < NU; ++i) { const int u = gw + 512 * i; if (u < RU3) st16(so, u * 1024 + lane * 16, v[i]); }
    float* red = (float*)smem;
    if (rg < 56) {
      f32x4 sa = {0.f, 0.f, 0.f, 0.f}, sb = {0.f, 0.f, 0.f, 0.f};
#pragma unroll
      for (int i = 0; i < 11; ++i) if (rg + 56 * i < D_) {
        sa += f32x4{bflo(cv[i][0]), bfhi(cv[i][0]), bflo(cv[i][1]), bfhi(cv[i][1])};
        sb += f32x4{bflo(cv[i][2]), bfhi(cv[i][2]), bflo(cv[i][3]), bfhi(cv[i][3])};
      }
      *(f32x4*)(red + rg * 72 + cc * 8) = sa; *(f32x4*)(red + rg * 72 + cc * 8 + 4) = sb;
    }
    __syncthreads();
    if (tid < 288) {
      const int c = tid % 72, q = tid / 72;
      float t = 0.f;
#pragma unroll
      for (int r = 0; r < 14; ++r) t += red[(q * 14 + r) * 72 + c];
      red[56 * 72 + tid] = t;
    }
    __syncthreads();
    if (tid < 72) ((float*)(ws + WS_CS))[pw * 72 + tid] = (red[56 * 72 + tid] + red[56 * 72 + 72 + tid]) + (red[56 * 72 + 144 + tid] + red[56 * 72 + 216 + tid]);
    STAMP_END(0);
    return;
  }
  const int tm = (xcd & 3) * 4 + (idx & 3), tn = (xcd >> 2) * 6 + (idx >> 2);
  if (__builtin_expect(w == 4 || w == 5, 1)) {
    run_producer<C1>(smem, x + (size_t)tm * 128 * D_, 128 * D_ * 2, wqkv + tn * 144, (unsigned)((D_ * 3 * D_ - tn * 144) * 2), D_, 3 * D_, w - 4, tid & 63, ws, 0, ROT1);
    return;
  }
  if (w >= 6) {
    const int lane = tid & 63, p = w - 6, g = lane >> 4;
    f32x4 cs[5];
#pragma unroll
    for (int j = 0; j < 5; ++j) cs[j] = f32x4{0.f, 0.f, 0.f, 0.f};
    const int tb = C1::ABYTES + (16 * (g >> 1) + 4 * (g & 1) + ((lane & 15) >> 2)) * 288 + (lane & 3) * 8 + p * 32;
    for (int kt = 0; kt < 9; ++kt) {
      BAR();
      const char* sb = smem + (kt % C1::NST) * C1::STAGE + tb;
#pragma unroll
      for (int jj = 0; jj < 5; ++jj)
#pragma unroll
        for (int ks = 0; ks < 2; ++ks) {
          const char* q = sb + ks * (32 * 288) + jj * 64;
          cs[jj] = MFMA16(cat8(trread(q), trread(q + 8 * 288)), ones8(), cs[jj]);
        }
      WAITL0();
    }
    float* st = (float*)(smem + C1::CS_OFF);
#pragma unroll
    for (int jj = 0; jj < 5; ++jj) if ((lane & 15) == 0 && p + 2 * jj < 9) *(f32x4*)(st + 16 * (p + 2 * jj) + 4 * g) = cs[jj];
    WAITL0(); BAR();
    return;
  }
  f32x4 acc[2][9]; float rs[2], rq[2];
  consumer<C1, 2, 9, 0, false, true>(smem, w * 32, tid & 63, acc, rs, rq, ws, 0, 0, NoHook());
  int tz = threadIdx.x; VREG(tz);
  const int lane = tz & 63, g = lane >> 4, fr = lane & 15;
  const float* cs = (const float*)(smem + C1::CS_OFF);
  const int kind = tn >> 2, head0 = (tn & 3) * 8;
  const int tile32 = tm * 4 + w;
  const float sc = kind == 0 ? 0.23570226039551584f * 1.4426950408889634f : 1.f;
  float mu[2], rstd[2];
#pragma unroll
  for (int i = 0; i < 2; ++i) {
    const f32x2 st = ln_stats(rs[i], rq[i], 1.f / D_);
    mu[i] = st[0]; rstd[i] = st[1] * sc;
  }
  constexpr int RP = 336;
  char* reg = smem + w * (32 * RP);
  f32x4 c4a[9];
#pragma unroll
  for (int j = 0; j < 9; ++j) c4a[j] = *(const f32x4*)(cs + 16 * j + 4 * g);
#pragma unroll
  for (int j = 0; j < 9; ++j) {
    const f32x4 c4 = c4a[j];
#pragma unroll
    for (int rr = 0; rr < 2; ++rr) {
      const int c = 16 * j + 4 * g + 2 * rr, off = c * 2 + (c / 18) * 4;
#pragma unroll
      for (int i = 0; i < 2; ++i)
        *(unsigned*)(reg + (16 * i + fr) * RP + off) = cvtpk((acc[i][j][2 * rr] - mu[i] * c4[2 * rr]) * rstd[i], (acc[i][j][2 * rr + 1] - mu[i] * c4[2 * rr + 1]) * rstd[i]);
    }
  }
  WAITL0();
  if (kind < 2) {
    char* base = ws + (kind == 0 ? WS_Q + (size_t)tile32 * QK_TILE + (size_t)head0 * Q_HEAD : WS_KV + (size_t)tile32 * KV_TILE + (size_t)head0 * KV_HEAD);
    const int hstride = kind == 0 ? Q_HEAD : KV_HEAD;
    const char* rp = reg + (lane & 31) * RP + (lane >> 5) * 16;
#pragma unroll
    for (int hl = 0; hl < 8; ++hl) {
      const u32x2 lo = *(const u32x2*)(rp + hl * 40), hi = *(const u32x2*)(rp + hl * 40 + 8);
      *(u32x4*)(base + (size_t)hl * hstride + lane * 16) = u32x4{lo[0], lo[1], hi[0], hi[1]};
    }
#pragma unroll
    for (int q = 0; q < 4; ++q) {
      const int hl = 2 * q + (lane >> 5);
      *(unsigned*)(base + (size_t)hl * hstride + 1024 + (lane & 31) * 4) = *(const unsigned*)(reg + (lane & 31) * RP + hl * 40 + 32);
    }
  } else {
    char* base = ws + WS_KV + (size_t)tile32 * KV_TILE + V_OFF + (size_t)head0 * KV_HEAD;
    const int s = g >> 1, h = g & 1;
    const char* rp = reg + (16 * s + 4 * h + (fr >> 2)) * RP + (lane & 3) * 8;
#pragma unroll
    for (int j = 0; j < 10; ++j) {
      const bf16x8 v = cat8(trread(rp + j * 32), trread(rp + 8 * RP + j * 32));
      const int c = 16 * j + fr, hl = c / 20, d = c - hl * 20;
      if (d < 18) *(bf16x8*)(base + (size_t)hl * KV_HEAD + s * 608 + h * 304 + d * 16) = v;
    }
    if (lane < 32) {
      *(bf16x8*)(base + (size_t)(lane >> 2) * KV_HEAD + (lane & 3) * 304 + 18 * 16) = ones8();
    }
  }
  STAMP_END(0);
}

DI void spin_until(const unsigned* cnt, unsigned target) {
  for (unsigned spins = 0; spins < (1u << 24); ++spins) {
    if (__hip_atomic_load(cnt, __ATOMIC_RELAXED, __HIP_MEMORY_SCOPE_AGENT) >= target) break;
    __builtin_amdgcn_s_sleep(1);
  }
}
template <int KT> using CR = Cfg<64, 5, 4, 8, B_TILED, KT, false>;
constexpr int ATT_LDS = 152 * 1024;
__global__ __launch_bounds__(512, 2) void k_attn(char* __restrict__ ws, const u16* __restrict__ wsp, const u16* __restrict__ x) {
  extern __shared__ __attribute__((aligned(16))) char smem[];
  const int tid = threadIdx.x, lane = tid & 63, w = rfl(tid >> 6);
  const int b = blockIdx.x, xcd = b & 7, idx = b >> 3;
  const int head = xcd * 4 + (idx & 3), qb = idx >> 2;
  char* ws_dbg = ws; STAMP(1, 0);
  const int q31 = lane & 31, h = lane >> 5;
  const char* qp = ws + WS_Q + (size_t)head * Q_HEAD + (size_t)(qb * 8 + w) * QK_TILE;
  bf16x8 qf1; unsigned q2;
  asm volatile("global_load_dwordx4 %0, %1, off" : "=v"(qf1) : "v"(qp + lane * 16));
  asm volatile("global_load_dword %0, %1, off" : "=v"(q2) : "v"(qp + 1024 + q31 * 4));
  {
    __amdgpu_buffer_rsrc_t srd = mksrd(ws + WS_KV + (size_t)head * KV_HEAD, KV_HEAD);
    const unsigned ldsb = (unsigned)(size_t)smem + w * 1024, vo = lane * 16 + w * 1024;
#pragma unroll
    for (int j = 0; j < 19; ++j) DMAQ(ldsb, j * 8192, vo, srd, (unsigned)(j * 8192));
  }
  asm volatile("s_waitcnt vmcnt(16)" : "+v"(qf1), "+v"(q2) :: "memory");
  BAR();
  u32x4 q2v = {h == 0 ? q2 : 0u, 0u, 0u, 0u};
  const bf16x8 qf2 = __builtin_bit_cast(bf16x8, q2v);
  const int dcl = q31 < 19 ? q31 : (q31 >= 20 && q31 < 28 ? 12 : 16);
  const int koff = lane * 16, k2off = 1024 + q31 * 4, voff = V_OFF + h * 304 + dcl * 16;
  f32x16 o;
#pragma unroll
  for (int r = 0; r < 16; ++r) o[r] = 0.f;
  auto scores = [&](int t, const f32x16& c) {
    const char* tp = smem + t * KV_TILE;
    const bf16x8 k1 = *(const bf16x8*)(tp + koff);
    u32x4 k2v = {*(const unsigned*)(tp + k2off), 0u, 0u, 0u};
    f32x16 s = MFMA32(k1, qf1, c);
    return MFMA32(__builtin_bit_cast(bf16x8, k2v), qf2, s);
  };
  f32x16 zero;
#pragma unroll
  for (int r = 0; r < 16; ++r) zero[r] = 0.f;
  STAMP(1, 1);
  unsigned dummy_keep;
  f32x16 negm;
  {
    const f32x16 s = scores(0, zero);
    float m = s[0];
#pragma unroll
    for (int r = 1; r < 16; ++r) m = fmaxf(m, s[r]);
    m = fmaxf(m, xor32(m));
#pragma unroll
    for (int r = 0; r < 16; ++r) negm[r] = -m;
  }
  {
    struct Frag { bf16x8 k1, v0, v1; u32x4 k2; };
    Frag fA, fB;
    fA.k2 = u32x4{0u, 0u, 0u, 0u}; fB.k2 = u32x4{0u, 0u, 0u, 0u};
    f32x16 sA = scores(0, negm), sB;
    fA.k1 = *(const bf16x8*)(smem + KV_TILE + koff); fA.k2[0] = *(const unsigned*)(smem + KV_TILE + k2off);
    fA.v0 = *(const bf16x8*)(smem + voff); fA.v1 = *(const bf16x8*)(smem + voff + 608);
    sA[0] = __builtin_amdgcn_exp2f(sA[0]); sA[1] = __builtin_amdgcn_exp2f(sA[1]);
    auto step = [&](int t, f32x16& sc, f32x16& sn, Frag& fc, Frag& fn) {
      const char* tp = smem + t * KV_TILE;
      u32x4 p0, p1;
      fn.v1 = *(const bf16x8*)(tp + KV_TILE + voff + 608); SB();
      fn.v0 = *(const bf16x8*)(tp + KV_TILE + voff); SB();
      fn.k2[0] = *(const unsigned*)(tp + 2 * KV_TILE + k2off); SB();
      fn.k1 = *(const bf16x8*)(tp + 2 * KV_TILE + koff);
      SB();
      sn = MFMA32(fc.k1, qf1, negm);
#pragma unroll
      for (int e = 2; e < 5; ++e) sc[e] = __builtin_amdgcn_exp2f(sc[e]);
      SB();
      sn = MFMA32(__builtin_bit_cast(bf16x8, fc.k2), qf2, sn);
#pragma unroll
      for (int e = 5; e < 8; ++e) sc[e] = __builtin_amdgcn_exp2f(sc[e]);
#pragma unroll
      for (int e = 0; e < 4; ++e) p0[e] = cvtpk(sc[2 * e], sc[2 * e + 1]);
      SB();
      sc[8] = __builtin_amdgcn_exp2f(sc[8]); sc[9] = __builtin_amdgcn_exp2f(sc[9]);
      SB();
      o = MFMA32(fc.v0, __builtin_bit_cast(bf16x8, p0), o);
#pragma unroll
      for (int e = 10; e < 16; ++e) sc[e] = __builtin_amdgcn_exp2f(sc[e]);
#pragma unroll
      for (int e = 0; e < 4; ++e) p1[e] = cvtpk(sc[8 + 2 * e], sc[8 + 2 * e + 1]);
      SB();
      sn[0] = __builtin_amdgcn_exp2f(sn[0]); sn[1] = __builtin_amdgcn_exp2f(sn[1]);
      SB();
      o = MFMA32(fc.v1, __builtin_bit_cast(bf16x8, p1), o);
      SB();
    };
    unsigned dummy = 0;
    auto warm = [&]() { const unsigned T = b * 512 + tid; TOUCH(dummy, (const char*)wsp + (size_t)(T < 65536u ? T : 0u) * 128); };
#define CHUNK(c, nv) { if (c > 0) { WAITV(nv); BAR(); } if (c == 3) warm(); \
    for (int t = 8 * c; t < 8 * c + 8; t += 2) { step(t, sA, sB, fA, fB); step(t + 1, sB, sA, fB, fA); } }
    CHUNK(0, 16) CHUNK(1, 13) CHUNK(2, 11) CHUNK(3, 9) CHUNK(4, 7) CHUNK(5, 5) CHUNK(6, 3) CHUNK(7, 1)
#undef CHUNK
    VREG(dummy);
    dummy_keep = dummy;
  }
  STAMP(1, 2);
  float l = __shfl(o[10], q31);
  bool ok = l < 1.8446744e19f;
#pragma unroll
  for (int r = 0; r < 12; ++r) ok = ok && (fabsf(o[r]) <= 3.0e38f);
  if (!__all(ok)) {
    float m = -3.0e38f;
#pragma unroll
    for (int r = 0; r < 16; ++r) o[r] = 0.f;
    for (int t = 0; t < 64; ++t) {
      f32x16 s = scores(t, zero);
      float tmax = s[0];
#pragma unroll
      for (int r = 1; r < 16; ++r) tmax = fmaxf(tmax, s[r]);
      tmax = fmaxf(tmax, xor32(tmax));
      const float mn = fmaxf(m, tmax), alpha = __builtin_amdgcn_exp2f(m - mn);
      m = mn;
#pragma unroll
      for (int r = 0; r < 16; ++r) { o[r] *= alpha; s[r] = __builtin_amdgcn_exp2f(s[r] - mn); }
      const char* tp = smem + t * KV_TILE + voff;
      const bf16x8 v0 = *(const bf16x8*)(tp), v1 = *(const bf16x8*)(tp + 608);
      u32x4 p0, p1;
#pragma unroll
      for (int e = 0; e < 4; ++e) { p0[e] = cvtpk(s[2 * e], s[2 * e + 1]); p1[e] = cvtpk(s[8 + 2 * e], s[8 + 2 * e + 1]); }
      o = MFMA32(v0, __builtin_bit_cast(bf16x8, p0), o);
      o = MFMA32(v1, __builtin_bit_cast(bf16x8, p1), o);
    }
    l = __shfl(o[10], q31);
  }
  const float inv = 1.f / l;
  {
    unsigned a0 = cvtpk(o[0] * inv, o[1] * inv), a1 = cvtpk(o[2] * inv, o[3] * inv), b0 = cvtpk(o[4] * inv, o[5] * inv), b1 = cvtpk(o[6] * inv, o[7] * inv);
    { auto r = __builtin_amdgcn_permlane32_swap(a0, b0, false, false); a0 = r[0]; b0 = r[1]; }
    { auto r = __builtin_amdgcn_permlane32_swap(a1, b1, false, false); a1 = r[0]; b1 = r[1]; }
    const __amdgpu_buffer_rsrc_t so = mksrd(ws + WS_AO + (size_t)(qb * 32 + head) * 8192, 8192);
    __builtin_amdgcn_raw_buffer_store_b128(u32x4{a0, a1, b0, b1}, so, (w * 32 + q31) * 32 + h * 16, 0, 16);
    const __amdgpu_buffer_rsrc_t s2 = mksrd(ws + WS_AO + (size_t)S_ * 1024, S_ * 128);
    if (h == 0) __builtin_amdgcn_raw_buffer_store_b32(cvtpk(o[8] * inv, o[9] * inv), s2, (qb * 256 + w * 32 + q31) * 128 + head * 4, 0, 16);
  }
  asm volatile("s_waitcnt vmcnt(0)" : "+v"(dummy_keep) :: "memory");
  WAITL0(); BAR();
  STAMP(2, 0); STAMP(1, 3);
  using C = CR<9>;
  unsigned* cnt = (unsigned*)(ws + WS_CNT + (32 + qb) * 128);
  if (w == 0 && lane == 0) __hip_atomic_fetch_add(cnt, 1u, __ATOMIC_RELAXED, __HIP_MEMORY_SCOPE_AGENT);
  if (w >= 6) return;
  const int row0 = qb * 256 + (head >> 3) * 64, tn = head & 7;
  if (w >= 4) {
    const int p = w - 4;
    const __amdgpu_buffer_rsrc_t srdA = mksrd(ws + WS_AO, S_ * 1152), srdB = mksrd(ws + WS_WO + (size_t)tn * 9 * WR_TB, 9 * WR_TB);
    const unsigned ldsb = (unsigned)(size_t)smem, mA = ldsb + p * 4096, mB = ldsb + C::ABYTES + p * 5120;
    const int c0 = (lane & 7) ^ (lane >> 4), c1 = (lane & 7) ^ (4 + (lane >> 4));
    const int rl = (head >> 3) * 64 + p * 32 + (lane >> 3);
    const unsigned va0 = (unsigned)(qb * 32 + (c0 >> 1)) * 8192 + rl * 32 + (c0 & 1) * 16, va1 = (unsigned)(qb * 32 + (c1 >> 1)) * 8192 + rl * 32 + (c1 & 1) * 16;
    const unsigned vt0 = S_ * 1024 + (qb * 256 + rl) * 128 + c0 * 16, vt1 = S_ * 1024 + (qb * 256 + rl) * 128 + c1 * 16, vb = p * 5120 + lane * 16;
    auto issueA = [&](int kt) {
      const unsigned sa = mA + (kt & 7) * C::STAGE;
      if (kt < 8) {
#pragma unroll
        for (int j = 0; j < 4; ++j) { const unsigned v = ((j & 1) ? va1 : va0) + j * 8 * 32; DMAQ_SC1(sa, j * 1024, v, srdA, (unsigned)(kt * 4 * 8192)); }
      } else {
#pragma unroll
        for (int j = 0; j < 4; ++j) { const unsigned v = ((j & 1) ? vt1 : vt0) + j * 8 * 128; DMAQ_SC1(sa, j * 1024, v, srdA, 0u); }
      }
    };
    auto issueB = [&](int kt) {
      const unsigned sb = mB + (kt & 7) * C::STAGE, sk = kt * WR_TB;
#pragma unroll
      for (int j = 0; j < 5; ++j) { const unsigned v = vb + j * 1024; DMAQ(sb, j * 1024, v, srdB, sk); }
    };
#pragma unroll
    for (int s = 0; s < 7; ++s) issueB(s);
    if (lane == 0) spin_until(cnt, 32u);
    asm volatile("" ::: "memory");
#pragma unroll
    for (int s = 0; s < 7; ++s) issueA(s);
    WAITV(24); BAR(); issueA(7); issueB(7);
    WAITV(29); BAR(); issueA(8); issueB(8);
    WAITV(34); BAR(); WAITV(30); BAR(); WAITV(26); BAR(); WAITV(22); BAR(); WAITV(18); BAR(); WAITV(9); BAR(); WAITV(0); BAR();
    BAR();
    return;
  }
  {
    const int g = lane >> 4, fr = lane & 15;
    const int tok = row0 + w * 16 + fr;
    const size_t ro = (size_t)tok * D_ + tn * 72 + 4 * g;
    u32x2 r[5];
#pragma unroll
    for (int j = 0; j < 5; ++j) r[j] = *(const u32x2*)(x + ro + ((j < 4 || g < 2) ? 16 * j : 0));
    f32x4 acc[1][5]; float rs[1], rq[1];
    consumer<C, 1, 5, 0, false, false>(smem, w * 16, lane, acc, rs, rq, ws, 7, 0, NoHook());
    u16* out = (u16*)(ws + WS_H);
    float s1 = 0.f, s2 = 0.f;
#pragma unroll
    for (int j = 0; j < 5; ++j) {
      if (j < 4 || g < 2) {
        u32x2 v;
        const unsigned p0 = cvtpk(acc[0][j][0], acc[0][j][1]), p1 = cvtpk(acc[0][j][2], acc[0][j][3]);
        v[0] = cvtpk(bflo(r[j][0]) + bflo(p0), bfhi(r[j][0]) + bfhi(p0));
        v[1] = cvtpk(bflo(r[j][1]) + bflo(p1), bfhi(r[j][1]) + bfhi(p1));
        *(u32x2*)(out + ro + 16 * j) = v;
        const float a0 = bflo(v[0]), a1 = bfhi(v[0]), a2 = bflo(v[1]), a3 = bfhi(v[1]);
        s1 += (a0 + a1) + (a2 + a3);
        s2 = fmaf(a0, a0, fmaf(a1, a1, fmaf(a2, a2, fmaf(a3, a3, s2))));
      }
    }
    s1 += xor16(s1); s1 += xor32(s1); s2 += xor16(s2); s2 += xor32(s2);
    if (g == 0) *(f32x2*)(ws + WS_HST + ((size_t)tok * 8 + tn) * 8) = f32x2{s1, s2};
  }
  STAMP_END(2);
}

constexpr int MST = 36864, MNST = 4;
constexpr int M_CSA = MNST * MST, M_CSB = M_CSA + 576, M_RS = M_CSB + 576, M_S2 = M_RS + 1024, M_LDS = M_S2 + 1024;
__global__ __launch_bounds__(448, 2) void k_mix(const u16* __restrict__ wsp, const u16* __restrict__ bsp, char* __restrict__ ws, u16* __restrict__ outp) {
  extern __shared__ __attribute__((aligned(16))) char smem[];
  const int tid = threadIdx.x, w = rfl(tid >> 6);
  const int b = blockIdx.x, xcd = b & 7, idx = b >> 3;
  const int tm = (xcd & 1) * 8 + (idx & 7), tn = (xcd >> 1) * 4 + (idx >> 3);
  char* ws_dbg = ws;
  unsigned* cnt1 = (unsigned*)(ws + WS_CNT + tm * 128);
  unsigned* cnt2 = (unsigned*)(ws + WS_CNT + (16 + tn) * 128);
  unsigned* cnt3 = (unsigned*)(ws + WS_CNT + (40 + tm) * 128);
  using CD = CR<36>;
  const int drow0 = tm * 128 + (tn >> 3) * 64, dtn = tn & 7;
  if (__builtin_expect(w == 4 || w == 5, 1)) {
    const int lane = tid & 63, p = w - 4;
    if (p == 0) STAMPW(3, 4);
    const unsigned ldsb = (unsigned)(size_t)smem;
    const unsigned mA = ldsb + p * 8192, mB = ldsb + 16384 + p * 9216;
    const unsigned sw0 = ((lane & 7) ^ (lane >> 4)) << 4, sw1 = ((lane & 7) ^ (4 + (lane >> 4))) << 4;
    const int g = lane >> 4;
    constexpr int NQ = 17;
    {
      const __amdgpu_buffer_rsrc_t srdA = mksrd((const u16*)(ws + WS_H) + (size_t)tm * 128 * D_, 128 * D_ * 2), srdB = mksrd(ws + WS_WIN, 32 * 9 * WIN_TB);
      const unsigned va0 = (p * 64 + (lane >> 3)) * (D_ * 2) + sw0, va1 = (p * 64 + (lane >> 3)) * (D_ * 2) + sw1, vb = p * 9216 + lane * 16;
      auto issue = [&](int s) {
        const int ph = s >= 9, kt = s - 9 * ph;
        const unsigned so = (s & 3) * MST, sa = mA + so, sb = mB + so;
        const unsigned soA = kt * 128, soB = ((ph ? tn : tn + 16) * 9 + kt) * WIN_TB;
#pragma unroll
        for (int j = 0; j < 8; ++j) { const unsigned v = ((j & 1) ? va1 : va0) + j * 8 * (D_ * 2); DMAQ(sa, j * 1024, v, srdA, soA); }
#pragma unroll
        for (int j = 0; j < 9; ++j) { const unsigned v = vb + j * 1024; DMAQ(sb, j * 1024, v, srdB, soB); }
      };
      issue(0); issue(1); issue(2);
      if (p == 0) STAMPW(3, 5);
      for (int s = 0; s < 15; ++s) { WAITV(2 * NQ); BAR(); issue(s + 3); }
      WAITV(2 * NQ); BAR(); WAITV(NQ); BAR(); WAITV(0); BAR();
    }
    BAR();
    BAR();
    if (p == 0) STAMPW(5, 0);
    {
      const __amdgpu_buffer_rsrc_t srdA = mksrd(wsp + (size_t)tm * 128 * S_, 128 * S_ * 2), srdB = mksrd((const u16*)(ws + WS_Z2N) + (size_t)tn * 144 * S_, 144 * S_ * 2);
      const unsigned va0 = (p * 64 + (lane >> 3)) * (S_ * 2) + sw0, va1 = (p * 64 + (lane >> 3)) * (S_ * 2) + sw1;
      const unsigned vb0 = (p * 72 + (lane >> 3)) * (S_ * 2) + (p ? sw1 : sw0), vb1 = (p * 72 + (lane >> 3)) * (S_ * 2) + (p ? sw0 : sw1);
      auto issueA = [&](int kt) {
        const unsigned sa = mA + (kt & 3) * MST, sk = kt * 128;
#pragma unroll
        for (int j = 0; j < 8; ++j) { const unsigned v = ((j & 1) ? va1 : va0) + j * 8 * (S_ * 2); DMAQ(sa, j * 1024, v, srdA, sk); }
      };
      auto issueB = [&](int kt) {
        const unsigned sb = mB + (kt & 3) * MST, sk = kt * 128;
#pragma unroll
        for (int j = 0; j < 9; ++j) { const unsigned v = ((j & 1) ? vb1 : vb0) + j * 8 * (S_ * 2); DMAQ_SC1(sb, j * 1024, v, srdB, sk); }
      };
      auto issue = [&](int kt) { issueA(kt); issueB(kt); };
      issueA(0); issueA(1); issueA(2);
      if (lane == 0) spin_until(cnt2, 16u);
      asm volatile("" ::: "memory");
      if (p == 0) STAMPW(5, 1);
      if (p == 0) STAMPW(5, 2);
      issueB(0); issueB(1); issueB(2);
      if (p == 0) STAMPW(5, 3);
      WAITV(18); BAR(); issue(3);
      WAITV(9 + NQ); BAR(); issue(4);
      for (int kt = 2; kt < 29; ++kt) { WAITV(2 * NQ); BAR(); issue(kt + 3); }
      WAITV(2 * NQ); BAR(); WAITV(NQ); BAR(); WAITV(0); BAR();
    }
    BAR();
    BAR();
    {
      const __amdgpu_buffer_rsrc_t srdA = mksrd((const u16*)(ws + WS_U) + (size_t)drow0 * FH, 64 * FH * 2), srdB = mksrd(ws + WS_WOUT + (size_t)dtn * 36 * WR_TB, 36 * WR_TB);
      const unsigned mA2 = ldsb + p * 4096, mB2 = ldsb + CD::ABYTES + p * 5120;
      const unsigned va0 = (p * 32 + (lane >> 3)) * (FH * 2) + sw0, va1 = (p * 32 + (lane >> 3)) * (FH * 2) + sw1, vb = p * 5120 + lane * 16;
      auto issueA = [&](int kt) {
        const unsigned sa = mA2 + (kt & 7) * CD::STAGE, sk = kt * 128;
#pragma unroll
        for (int j = 0; j < 4; ++j) { const unsigned v = ((j & 1) ? va1 : va0) + j * 8 * (FH * 2); DMAQ_SC1(sa, j * 1024, v, srdA, sk); }
      };
      auto issueB = [&](int kt) {
        const unsigned sb = mB2 + (kt & 7) * CD::STAGE, sk = kt * WR_TB;
#pragma unroll
        for (int j = 0; j < 5; ++j) { const unsigned v = vb + j * 1024; DMAQ(sb, j * 1024, v, srdB, sk); }
      };
#pragma unroll
      for (int s = 0; s < 7; ++s) issueB(s);
      if (lane == 0) spin_until(cnt3, 16u);
      asm volatile("" ::: "memory");
#pragma unroll
      for (int s = 0; s < 7; ++s) issueA(s);
      WAITV(24); BAR(); issueA(7); issueB(7);
      WAITV(29); BAR(); issueA(8); issueB(8);
      WAITV(34); BAR(); issueA(9); issueB(9);
      WAITV(39); BAR(); issueA(10); issueB(10);
      WAITV(44); BAR(); issueA(11); issueB(11);
      WAITV(49); BAR(); issueA(12); issueB(12);
      for (int kt = 6; kt < 29; ++kt) { WAITV(54); BAR(); issueA(kt + 7); issueB(kt + 7); }
      WAITV(54); BAR(); WAITV(45); BAR(); WAITV(36); BAR(); WAITV(27); BAR(); WAITV(18); BAR(); WAITV(9); BAR(); WAITV(0); BAR();
      BAR();
    }
    return;
  }
  if (w == 6) {
    const int lane = tid & 63;
    f32x2* rst = (f32x2*)(smem + M_RS);
#pragma unroll
    for (int i = 0; i < 2; ++i) {
      const f32x4* q = (const f32x4*)(ws + WS_HST + (size_t)(tm * 128 + 64 * i + lane) * 64);
      const f32x4 a = q[0], bb = q[1], c = q[2], d = q[3];
      const float s1 = (a[0] + a[2]) + (bb[0] + bb[2]) + (c[0] + c[2]) + (d[0] + d[2]), s2 = (a[1] + a[3]) + (bb[1] + bb[3]) + (c[1] + c[3]) + (d[1] + d[3]);
      rst[64 * i + lane] = ln_stats(s1, s2, 1.f / D_);
    }
    if (lane < 36) {
      *(f32x4*)(smem + M_CSA + lane * 16) = *(const f32x4*)(ws + WS_CS + (size_t)((tn + 16) * 144 + lane * 4) * 4);
      *(f32x4*)(smem + M_CSB + lane * 16) = *(const f32x4*)(ws + WS_CS + (size_t)(tn * 144 + lane * 4) * 4);
    }
    WAITL0();
    for (int s = 0; s < 15; ++s) BAR();
    if (lane == 0) spin_until(cnt1, 16u);
    asm volatile("" ::: "memory");
    const __amdgpu_buffer_rsrc_t srdS = mksrd(ws + WS_ST + (size_t)tm * (16 * 128 * 8), 16 * 128 * 8);
    u32x4 pv[16];
#pragma unroll
    for (int q = 0; q < 16; ++q) pv[q] = __builtin_amdgcn_raw_buffer_load_b128(srdS, (q * 128 + 2 * lane) * 8, 0, 16);
    SB(); BAR(); BAR(); SB();
    f32x4 t4 = {0.f, 0.f, 0.f, 0.f};
#pragma unroll
    for (int q = 0; q < 16; ++q) t4 += __builtin_bit_cast(f32x4, pv[q]);
    {
      const f32x2 q0 = ln_stats(t4[0], t4[1], 1.f / FH), q1 = ln_stats(t4[2], t4[3], 1.f / FH);
      *(f32x4*)(smem + M_S2 + lane * 16) = f32x4{q0[1], -q0[0] * q0[1], q1[1], -q1[0] * q1[1]};
    }
    WAITL0();
    BAR();
    BAR();
    return;
  }
  const int lane = tid & 63, g = lane >> 4, fr = lane & 15, f = fr >> 1;
  const int l0 = fr * 128 + ((g ^ f) << 4), l1 = fr * 128 + (((4 + g) ^ f) << 4);
  const int tb = 16384 + (16 * (g >> 1) + 4 * (g & 1) + (fr >> 2)) * 288 + (lane & 3) * 8;
  const int tok0 = tm * 128 + w * 32;
  f32x4 acc[2][9];
  bf16x8 a0[2], b0[9], a1[2], b1[9];
  auto zero_acc = [&]() {
#pragma unroll
    for (int i = 0; i < 2; ++i)
#pragma unroll
      for (int j = 0; j < 9; ++j) acc[i][j] = f32x4{0.f, 0.f, 0.f, 0.f};
  };
  auto mm = [&](bf16x8* a, bf16x8* bb) {
#pragma unroll
    for (int j = 0; j < 9; ++j)
#pragma unroll
      for (int i = 0; i < 2; ++i) acc[i][j] = MFMA16(bb[j], a[i], acc[i][j]);
  };
  auto rdKN = [&](bf16x8* a, bf16x8* bb, int s, int ks) {
    const char* sb = smem + (s & 3) * MST;
#pragma unroll
    for (int i = 0; i < 2; ++i) a[i] = *(const bf16x8*)(sb + w * 4096 + i * 2048 + (ks ? l1 : l0));
#pragma unroll
    for (int j = 0; j < 9; ++j) { const char* q = sb + tb + ks * (32 * 288) + j * 32; bb[j] = cat8(trread(q), trread(q + 8 * 288)); }
  };
  auto rdNK = [&](bf16x8* a, bf16x8* bb, int s, int ks) {
    const char* sb = smem + (s & 3) * MST + (ks ? l1 : l0);
#pragma unroll
    for (int i = 0; i < 2; ++i) a[i] = *(const bf16x8*)(sb + w * 4096 + i * 2048);
#pragma unroll
    for (int j = 0; j < 9; ++j) bb[j] = *(const bf16x8*)(sb + 16384 + j * 2048);
  };
#define SCHED(id, ND, RPG) _Pragma("unroll") for (int q = 0; q < 18; ++q) { __builtin_amdgcn_sched_group_barrier(0x008, 1, id); \
    if (q * RPG < ND) __builtin_amdgcn_sched_group_barrier(0x080, RPG, id); }
  auto gelu = [&](u32x2 (&zp)[2][9], int csoff, f32x2 (&sum)[2]) {
    const float c1 = -2.f * 0.7978845608028654f * 1.4426950408889634f, c2 = c1 * 0.044715f;
    const float* cs = (const float*)(smem + csoff);
    const f32x2* rst = (const f32x2*)(smem + M_RS) + w * 32;
    float nmu[2], k1[2], k3[2], irs[2];
    f32x2 s1[2], s2[2];
#pragma unroll
    for (int i = 0; i < 2; ++i) {
      const f32x2 st = rst[16 * i + fr];
      nmu[i] = -st[0]; k1[i] = c1 * st[1]; k3[i] = c2 * st[1] * st[1] * st[1]; irs[i] = __builtin_amdgcn_rcpf(st[1]);
      s1[i] = f32x2{0.f, 0.f}; s2[i] = f32x2{0.f, 0.f};
    }
    f32x4 c4 = *(const f32x4*)(cs + 4 * g);
#pragma unroll
    for (int j = 0; j < 9; ++j) {
      const f32x4 c4n = *(const f32x4*)(cs + 16 * (j < 8 ? j + 1 : 8) + 4 * g);
      SB();
#pragma unroll
      for (int i = 0; i < 2; ++i)
#pragma unroll
        for (int hh = 0; hh < 2; ++hh) {
          const f32x2 a2 = {acc[i][j][2 * hh], acc[i][j][2 * hh + 1]}, c2v = {c4[2 * hh], c4[2 * hh + 1]};
          const f32x2 t = f32x2{nmu[i], nmu[i]} * c2v + a2;
          const f32x2 y = t * (t * t * f32x2{k3[i], k3[i]} + f32x2{k1[i], k1[i]});
          const f32x2 e = {__builtin_amdgcn_exp2f(y[0]), __builtin_amdgcn_exp2f(y[1])};
          const f32x2 d = e * f32x2{irs[i], irs[i]} + f32x2{irs[i], irs[i]};
          const f32x2 z = t * f32x2{__builtin_amdgcn_rcpf(d[0]), __builtin_amdgcn_rcpf(d[1])};
          s1[i] += z; s2[i] += z * z;
          zp[i][j][hh] = cvtpk(z[0], z[1]);
        }
      VREG(zp[0][j]); VREG(zp[1][j]);
      c4 = c4n;
      SB();
    }
#pragma unroll
    for (int i = 0; i < 2; ++i) sum[i] = f32x2{s1[i][0] + s1[i][1], s2[i][0] + s2[i][1]};
  };
  const __amdgpu_buffer_rsrc_t srdS = mksrd(ws + WS_ST + (size_t)tm * (16 * 128 * 8), 16 * 128 * 8);
  zero_acc();
  STAMP(3, 0);
  BAR();
  STAMP(3, 1);
  rdKN(a0, b0, 0, 0);
  u32x2 z2p[2][9];
  auto stageAB = [&](int s) {
    SB(); rdKN(a1, b1, s, 1); mm(a0, b0); SCHED(0, 20, 2) SB();
    WAITL0(); BAR();
    SB(); rdKN(a0, b0, s + 1, 0); mm(a1, b1); SCHED(1, 20, 2)
  };
  for (int s = 0; s < 9; ++s) stageAB(s);
  SB();
  STAMP(5, 6);
  {
    f32x2 sum[2];
    gelu(z2p, M_CSA, sum);
#pragma unroll
    for (int i = 0; i < 2; ++i) {
      f32x2 v = sum[i];
      v[0] += xor16(v[0]); v[0] += xor32(v[0]); v[1] += xor16(v[1]); v[1] += xor32(v[1]);
      if (g == 0) __builtin_amdgcn_raw_buffer_store_b64(__builtin_bit_cast(u32x2, v), srdS, (tn * 128 + w * 32 + 16 * i + fr) * 8, 0, 16);
    }
    asm volatile("s_waitcnt vmcnt(0)" ::: "memory");
    zero_acc();
  }
  SB();
  STAMP(5, 4);
  stageAB(9);
  if (w == 0 && lane == 0) __hip_atomic_fetch_add(cnt1, 1u, __ATOMIC_RELAXED, __HIP_MEMORY_SCOPE_AGENT);
  for (int s = 10; s < 17; ++s) stageAB(s);
  SB(); rdKN(a1, b1, 17, 1); mm(a0, b0); mm(a1, b1); SB();
  WAITL0(); BAR();
  STAMP(3, 2);
  {
    const f32x2* st2 = (const f32x2*)(smem + M_S2) + w * 32;
    char* reg = smem + w * (32 * 304);
#pragma unroll
    for (int i = 0; i < 2; ++i) {
      const f32x2 st = st2[16 * i + fr];
#pragma unroll
      for (int j = 0; j < 9; ++j) {
        u32x2 v;
        v[0] = cvtpk(fmaf(bflo(z2p[i][j][0]), st[0], st[1]), fmaf(bfhi(z2p[i][j][0]), st[0], st[1]));
        v[1] = cvtpk(fmaf(bflo(z2p[i][j][1]), st[0], st[1]), fmaf(bfhi(z2p[i][j][1]), st[0], st[1]));
        *(u32x2*)(reg + (16 * i + fr) * 304 + (16 * j + 4 * g) * 2) = v;
      }
    }
    WAITL0();
    const __amdgpu_buffer_rsrc_t so = mksrd((u16*)(ws + WS_Z2N) + (size_t)(tn * 144) * S_ + tok0, 144 * S_ * 2);
    const char* rp = reg + (8 * g + (fr >> 2)) * 304 + (lane & 3) * 8;
#pragma unroll
    for (int j = 0; j < 9; ++j)
      __builtin_amdgcn_raw_buffer_store_b128(__builtin_bit_cast(u32x4, cat8(trread(rp + j * 32), trread(rp + 4 * 304 + j * 32))), so, (j * 16 + fr) * (S_ * 2) + 8 * g * 2, 0, 16);
    asm volatile("s_waitcnt vmcnt(0)" ::: "memory");
    WAITL0(); BAR();
    if (w == 0 && lane == 0) __hip_atomic_fetch_add(cnt2, 1u, __ATOMIC_RELAXED, __HIP_MEMORY_SCOPE_AGENT);
  }
  u32x2 z1p[2][9];
  { f32x2 sum[2]; gelu(z1p, M_CSB, sum); }
  const unsigned bias0 = bsp[tok0 + fr], bias1 = bsp[tok0 + 16 + fr];
  zero_acc();
  STAMP(4, 0);
  BAR();
  STAMP(4, 1);
  rdNK(a0, b0, 0, 0);
  for (int kt = 0; kt < 31; ++kt) {
    SB(); rdNK(a1, b1, kt, 1); mm(a0, b0); SCHED(0, 11, 1) SB();
    WAITL0(); BAR();
    SB(); rdNK(a0, b0, kt + 1, 0); mm(a1, b1); SCHED(1, 11, 1)
  }
  SB(); rdNK(a1, b1, 31, 1); mm(a0, b0); mm(a1, b1); SB();
  WAITL0(); BAR();
  SB();
  STAMP(4, 2);
#undef SCHED
  char* reg = smem + w * (32 * 304);
#pragma unroll
  for (int i = 0; i < 2; ++i) {
    const float bi = bflo(i ? bias1 : bias0);
#pragma unroll
    for (int j = 0; j < 9; ++j) {
      u32x2 v;
      const unsigned e0 = cvtpk(acc[i][j][0], acc[i][j][1]), e1 = cvtpk(acc[i][j][2], acc[i][j][3]);
      const unsigned g0 = cvtpk(bflo(e0) + bi, bfhi(e0) + bi), g1 = cvtpk(bflo(e1) + bi, bfhi(e1) + bi);
      v[0] = cvtpk(bflo(z1p[i][j][0]) * bflo(g0), bfhi(z1p[i][j][0]) * bfhi(g0));
      v[1] = cvtpk(bflo(z1p[i][j][1]) * bflo(g1), bfhi(z1p[i][j][1]) * bfhi(g1));
      *(u32x2*)(reg + (16 * i + fr) * 304 + (16 * j + 4 * g) * 2) = v;
    }
  }
  WAITL0();
  const __amdgpu_buffer_rsrc_t so = mksrd((u16*)(ws + WS_U) + (size_t)tok0 * FH + tn * 144, 32 * FH * 2);
#pragma unroll
  for (int i = 0; i < 9; ++i) {
    const int c = i * 64 + lane, row = c / 18, ch = c - row * 18;
    __builtin_amdgcn_raw_buffer_store_b128(*(const u32x4*)(reg + row * 304 + ch * 16), so, row * (FH * 2) + ch * 16, 0, 16);
  }
  asm volatile("s_waitcnt vmcnt(0)" ::: "memory");
  WAITL0(); BAR();
  STAMP(4, 3);
  if (w == 0 && lane == 0) __hip_atomic_fetch_add(cnt3, 1u, __ATOMIC_RELAXED, __HIP_MEMORY_SCOPE_AGENT);
  {
    const int tok = drow0 + w * 16 + fr;
    const size_t ro = (size_t)tok * D_ + dtn * 72 + 4 * g;
    const u16* res = (const u16*)(ws + WS_H);
    u32x2 r[5];
#pragma unroll
    for (int j = 0; j < 5; ++j) r[j] = *(const u32x2*)(res + ro + ((j < 4 || g < 2) ? 16 * j : 0));
    f32x4 acd[1][5]; float rs[1], rq[1];
    consumer<CD, 1, 5, 0, false, false>(smem, w * 16, lane, acd, rs, rq, ws, 6, 0, NoHook());
#pragma unroll
    for (int j = 0; j < 5; ++j) {
      if (j < 4 || g < 2) {
        const unsigned p0 = cvtpk(acd[0][j][0], acd[0][j][1]), p1 = cvtpk(acd[0][j][2], acd[0][j][3]);
        u32x2 v;
        v[0] = cvtpk(bflo(r[j][0]) + bflo(p0), bfhi(r[j][0]) + bfhi(p0));
        v[1] = cvtpk(bflo(r[j][1]) + bflo(p1), bfhi(r[j][1]) + bfhi(p1));
        *(u32x2*)(outp + ro + 16 * j) = v;
      }
    }
  }
}

extern "C" void kernel_launch(void* const* d_in, const int* in_sizes, int n_in, void* d_out, int out_size, void* d_ws, size_t ws_size, hipStream_t stream) {
  const u16* x = (const u16*)d_in[0];
  const u16* wqkv = (const u16*)d_in[1];
  const u16* wo = (const u16*)d_in[2];
  const u16* win = (const u16*)d_in[4];
  const u16* wsp = (const u16*)d_in[5];
  const u16* bsp = (const u16*)d_in[6];
  const u16* wout = (const u16*)d_in[7];
  char* ws = (char*)d_ws;
  (void)hipFuncSetAttribute((const void*)k_qkv, hipFuncAttributeMaxDynamicSharedMemorySize, C1::LDS);
  (void)hipFuncSetAttribute((const void*)k_attn, hipFuncAttributeMaxDynamicSharedMemorySize, ATT_LDS);
  (void)hipFuncSetAttribute((const void*)k_mix, hipFuncAttributeMaxDynamicSharedMemorySize, M_LDS);
  hipLaunchKernelGGL(k_qkv, dim3(256), dim3(512), C1::LDS, stream, x, wqkv, win, wout, wo, ws);
  hipLaunchKernelGGL(k_attn, dim3(256), dim3(512), ATT_LDS, stream, ws, wsp, x);
  hipLaunchKernelGGL(k_mix, dim3(256), dim3(448), M_LDS, stream, wsp, bsp, ws, (u16*)d_out);
}
```

```cpp
#include <hip/hip_runtime.h>
#include <stdint.h>

typedef unsigned short u16;
typedef short bf16x8 __attribute__((ext_vector_type(8)));
typedef short s16x4 __attribute__((ext_vector_type(4)));
typedef float f32x2 __attribute__((ext_vector_type(2)));
typedef float f32x4 __attribute__((ext_vector_type(4)));
typedef float f32x16 __attribute__((ext_vector_type(16)));
typedef unsigned u32x2 __attribute__((ext_vector_type(2)));
typedef unsigned u32x4 __attribute__((ext_vector_type(4)));
typedef __bf16 bf16x2_t __attribute__((ext_vector_type(2)));
typedef __attribute__((address_space(3))) s16x4* lds_s16x4_ptr;
#define DI __device__ __forceinline__

constexpr int S_ = 2048, D_ = 576, H_ = 32, DH = 18, FH = 2304, F2 = 4608;
constexpr int QK_TILE = 1152;
constexpr int V_OFF = 1152;
constexpr int KV_TILE = 1152 + 4 * 304;
constexpr int KV_HEAD = 64 * KV_TILE;
constexpr int Q_HEAD = 64 * QK_TILE;
constexpr int WIN_TB = 64 * 288, WR_TB = 64 * 160;
constexpr size_t WS_Q = 0;
constexpr size_t WS_KV = WS_Q + (size_t)H_ * Q_HEAD;
constexpr size_t WS_AO = WS_KV + (size_t)H_ * KV_HEAD + 8192;
constexpr size_t WS_H = WS_AO + (size_t)S_ * D_ * 2;
constexpr size_t WS_Z2N = WS_H + (size_t)S_ * D_ * 2;
constexpr size_t WS_ST = WS_Z2N + (size_t)S_ * FH * 2;
constexpr size_t WS_CNT = WS_ST + (size_t)S_ * 16 * 2 * 4;
constexpr size_t WS_HST = WS_CNT + 8192;
constexpr size_t WS_U0 = WS_HST + (size_t)S_ * 8 * 2 * 4;
constexpr size_t WS_U = 0;
static_assert((size_t)S_ * FH * 2 <= WS_H, "u must fit below h");
constexpr size_t WS_WIN = WS_U0;
constexpr size_t WS_WOUT = WS_WIN + (size_t)32 * 9 * WIN_TB;
constexpr size_t WS_WO = WS_WOUT + (size_t)8 * 36 * WR_TB;
constexpr size_t WS_CS = WS_WO + (size_t)8 * 9 * WR_TB;
constexpr size_t WS_END = WS_CS + (size_t)F2 * 4;

DI unsigned cvtpk(float lo, float hi) { f32x2 v = {lo, hi}; bf16x2_t b = __builtin_convertvector(v, bf16x2_t); return __builtin_bit_cast(unsigned, b); }
DI float bflo(unsigned u) { return __uint_as_float(u << 16); }
DI float bfhi(unsigned u) { return __uint_as_float(u & 0xffff0000u); }
DI float bfr(float x) { return __uint_as_float(cvtpk(x, x) << 16); }
DI f32x2 ln_stats(float s1, float s2, float inv_n) {
  const float mu = s1 * inv_n, var = fmaxf(s2 * inv_n - mu * mu, 0.f);
  return f32x2{bfr(mu), 1.f / bfr(sqrtf(bfr(bfr(var) + 1e-5f)))};
}
DI unsigned rfl(unsigned x) { return __builtin_amdgcn_readfirstlane(x); }
DI float xor16(float v) { return __shfl_xor(v, 16); }
DI float xor32(float v) { return __shfl_xor(v, 32); }
#define DMAQ(sb, imm, voff, srd, soff) asm volatile("s_add_u32 m0, %0, %1\n\ts_nop 0\n\tbuffer_load_dwordx4 %2, %3, %4 offen lds" :: "s"(sb), "i"(imm), "v"(voff), "s"(srd), "s"(soff) : "scc")
#define DMAQ_SC1(sb, imm, voff, srd, soff) asm volatile("s_add_u32 m0, %0, %1\n\ts_nop 0\n\tbuffer_load_dwordx4 %2, %3, %4 offen sc1 lds" :: "s"(sb), "i"(imm), "v"(voff), "s"(srd), "s"(soff) : "scc")
#define WAITV(n) asm volatile("s_waitcnt vmcnt(%0)" :: "i"(n) : "memory")
#define WAITL0() asm volatile("s_waitcnt lgkmcnt(0)" ::: "memory")
#define BAR() asm volatile("s_barrier" ::: "memory")
#define SB() __builtin_amdgcn_sched_barrier(0)
#define MFMA16(a, b, c) __builtin_amdgcn_mfma_f32_16x16x32_bf16((a), (b), (c), 0, 0, 0)
#define MFMA32(a, b, c) __builtin_amdgcn_mfma_f32_32x32x16_bf16((a), (b), (c), 0, 0, 0)
#define MFMA_DRAIN() do { SB(); asm volatile("s_nop 7\n\ts_nop 7\n\ts_nop 7"); SB(); } while (0)
#define VREG(x) asm volatile("" : "+v"(x))
DI __amdgpu_buffer_rsrc_t mksrd(const void* p, unsigned bytes) { return __builtin_amdgcn_make_buffer_rsrc((void*)p, (short)0, (int)bytes, 0x00020000); }
DI s16x4 trread(const char* p) { return __builtin_amdgcn_ds_read_tr16_b64_v4i16((lds_s16x4_ptr)(p)); }
DI bf16x8 cat8(s16x4 lo, s16x4 hi) { return __builtin_shufflevector(lo, hi, 0, 1, 2, 3, 4, 5, 6, 7); }
DI bf16x8 ones8() { const u32x4 o = {0x3F803F80u, 0x3F803F80u, 0x3F803F80u, 0x3F803F80u}; return __builtin_bit_cast(bf16x8, o); }
#define ROT1 (3 * (((idx >> 2) + (idx & 3)) % 3))
#define ST_AUX 16
DI void st16(__amdgpu_buffer_rsrc_t srd, unsigned off, u32x4 v) { __builtin_amdgcn_raw_buffer_store_b128(v, srd, off, 0, ST_AUX); }
#define TOUCH(dummy, ptr) asm volatile("global_load_dword %0, %1, off" : "+v"(dummy) : "v"(ptr))
#define STAMP(kid, slot)
#define STAMPW(kid, slot)
#define STAMP_END(kid)

enum { B_NK = 0, B_KN = 1, B_TILED = 2 };
template <int BM_, int NT_, int NST_, int BMODE_, int KT_>
struct Cfg {
  static constexpr int BM = BM_, NT = NT_, NST = NST_, KT = KT_, BMODE = BMODE_;
  static constexpr bool BKN = BMODE_ != B_NK;
  static constexpr int BN = NT * 16;
  static constexpr int NA = BM / 8, NB = BN / 8;
  static constexpr int ABYTES = BM * 128, BBYTES = BN * 128, STAGE = ABYTES + BBYTES;
  static constexpr int RING = NST * STAGE;
  static constexpr int CS_OFF = RING;
  static constexpr int LDS = RING + BN * 4;
};

template <class C>
DI void run_producer(char* smem, const void* Ab, unsigned abytes, const void* Bb, unsigned bbytes, int lda, int ldb, int p, int lane, char* ws_dbg, int kid, int rot) {
  if (p == 0) STAMPW(kid, 4);
  constexpr int HA = C::NA / 2, HB = C::NB / 2, NQ = HA + HB;
  static_assert(C::NA % 4 == 0 && C::NB % 2 == 0 && C::BMODE != B_NK, "piece split");
  const __amdgpu_buffer_rsrc_t srdA = mksrd(Ab, abytes), srdB = mksrd(Bb, bbytes);
  const unsigned ldsb = (unsigned)(size_t)smem;
  const unsigned mA = ldsb + p * (HA * 1024), mB = ldsb + C::ABYTES + p * (HB * 1024);
  const unsigned sw0 = ((lane & 7) ^ (lane >> 4)) << 4, sw1 = ((lane & 7) ^ (4 + (lane >> 4))) << 4;
  const unsigned va0 = (p * HA * 8 + (lane >> 3)) * (lda * 2) + sw0, va1 = (p * HA * 8 + (lane >> 3)) * (lda * 2) + sw1;
  unsigned vb[HB];
#pragma unroll
  for (int j = 0; j < HB; ++j) {
    if (C::BMODE == B_KN) {
      constexpr int CPR = C::BN / 8;
      const int c = 64 * (p * HB + j) + lane, rho = c / CPR, ch = c - rho * CPR;
      const int k = (rho & ~12) | ((rho & 4) << 1) | ((rho & 8) >> 1);
      vb[j] = k * (ldb * 2) + ch * 16;
    } else {
      vb[j] = (p * HB + j) * 1024 + lane * 16;
    }
  }
  auto issue = [&](int kt) {
    const unsigned so = (kt % C::NST) * C::STAGE, sa = mA + so, sb = mB + so;
    int kr = kt + rot; if (kr >= C::KT) kr -= C::KT;
    const unsigned soA = kr * 128, soB = C::BMODE == B_KN ? kr * 64 * ldb * 2 : kr * C::BBYTES;
#pragma unroll
    for (int j = 0; j < HA; ++j) { const unsigned v = ((j & 1) ? va1 : va0) + j * 8 * (lda * 2); DMAQ(sa, j * 1024, v, srdA, soA); }
#pragma unroll
    for (int j = 0; j < HB; ++j) DMAQ(sb, j * 1024, vb[j], srdB, soB);
  };
  constexpr int PF = C::NST - 1;
#pragma unroll
  for (int s = 0; s < PF; ++s) issue(s);
  if (p == 0) STAMPW(kid, 5);
  for (int kt = 0; kt < C::KT - PF; ++kt) { WAITV((PF - 1) * NQ); BAR(); issue(kt + PF); }
#pragma unroll
  for (int r = PF - 1; r >= 0; --r) { WAITV(r * NQ); BAR(); }
  BAR();
}

template <class C, int MI, bool ROWSTAT>
DI void consumer(char* smem, int row0, int lane, f32x4 (&acc)[MI][C::NT], float (&rs)[MI], float (&rq)[MI], char* ws_dbg, int kid) {
  constexpr int NT = C::NT;
  const int g = lane >> 4, fr = lane & 15, f = fr >> 1;
  const int la0 = row0 * 128 + fr * 128 + ((g ^ f) << 4), la1 = row0 * 128 + fr * 128 + (((4 + g) ^ f) << 4);
  const int tb = C::ABYTES + (16 * (g >> 1) + 4 * (g & 1) + (fr >> 2)) * (C::BN * 2) + (lane & 3) * 8;
  f32x4 ars[MI], arq[MI];
#pragma unroll
  for (int i = 0; i < MI; ++i) {
    ars[i] = f32x4{0.f, 0.f, 0.f, 0.f}; arq[i] = f32x4{0.f, 0.f, 0.f, 0.f};
#pragma unroll
    for (int j = 0; j < NT; ++j) acc[i][j] = f32x4{0.f, 0.f, 0.f, 0.f};
  }
  bf16x8 a0[MI], b0[NT], a1[MI], b1[NT];
  auto rd = [&](bf16x8* a, bf16x8* bb, int kt, int ks) {
    const char* sb = smem + (kt % C::NST) * C::STAGE;
#pragma unroll
    for (int i = 0; i < MI; ++i) a[i] = *(const bf16x8*)(sb + (ks ? la1 : la0) + i * 2048);
#pragma unroll
    for (int j = 0; j < NT; ++j) { const char* p = sb + tb + ks * (32 * C::BN * 2) + j * 32; bb[j] = cat8(trread(p), trread(p + 8 * C::BN * 2)); }
  };
  auto mm = [&](bf16x8* a, bf16x8* bb) {
#pragma unroll
    for (int j = 0; j < NT; ++j)
#pragma unroll
      for (int i = 0; i < MI; ++i) acc[i][j] = MFMA16(bb[j], a[i], acc[i][j]);
    if constexpr (ROWSTAT) {
#pragma unroll
      for (int i = 0; i < MI; ++i) { ars[i] = MFMA16(ones8(), a[i], ars[i]); arq[i] = MFMA16(a[i], a[i], arq[i]); }
    }
  };
  constexpr int NM = MI * NT + (ROWSTAT ? 2 * MI : 0), ND = MI + NT * 2, RPG = (ND + (NM * 2 / 3) - 1) / (NM * 2 / 3);
#define SCHED(id) _Pragma("unroll") for (int q = 0; q < NM; ++q) { __builtin_amdgcn_sched_group_barrier(0x008, 1, id); \
    if (q * RPG < ND) __builtin_amdgcn_sched_group_barrier(0x080, RPG, id); }
  STAMP(kid, 0);
  BAR();
  STAMP(kid, 1);
  rd(a0, b0, 0, 0);
  for (int kt = 0; kt < C::KT - 1; ++kt) {
    SB();
    rd(a1, b1, kt, 1);
    mm(a0, b0);
    SCHED(0)
    SB();
    WAITL0(); BAR();
    SB();
    rd(a0, b0, kt + 1, 0);
    mm(a1, b1);
    SCHED(1)
  }
  SB();
  rd(a1, b1, C::KT - 1, 1);
  mm(a0, b0);
  mm(a1, b1);
#undef SCHED
  MFMA_DRAIN();
  if constexpr (ROWSTAT) {
#pragma unroll
    for (int i = 0; i < MI; ++i) {
      rs[i] = ars[i][0];
      const float d = (fr & 2) ? ((fr & 1) ? arq[i][3] : arq[i][2]) : ((fr & 1) ? arq[i][1] : arq[i][0]);
      rq[i] = __shfl(d, fr + 16 * (fr >> 2));
    }
  }
  WAITL0(); BAR();
  SB();
  STAMP(kid, 2);
}

using C1 = Cfg<128, 9, 4, B_KN, 9>;
constexpr int RU1 = 32 * 9 * 18, RU2 = RU1 + 8 * 36 * 10, RU3 = RU2 + 8 * 9 * 10;
DI const u16* retile_src(const u16* __restrict__ win, const u16* __restrict__ wout, const u16* __restrict__ wo, int u, int lane) {
  if (u < RU1) {
    const int tile = u / 18, q = u - tile * 18, tn = tile / 9, kt = tile - tn * 9;
    const int c = 64 * q + lane, rho = c / 18, ch = c - rho * 18;
    const int k = (rho & ~12) | ((rho & 4) << 1) | ((rho & 8) >> 1);
    return win + (size_t)(kt * 64 + k) * F2 + tn * 144 + ch * 8;
  }
  const bool o = u < RU2; const int v = u - (o ? RU1 : RU2), KT = o ? 36 : 9;
  const int tile = v / 10, q = v - tile * 10, tn = tile / KT, kt = tile - tn * KT;
  const int c = 64 * q + lane, rho = c / 10, ch = c - rho * 10;
  const int k = (rho & ~12) | ((rho & 4) << 1) | ((rho & 8) >> 1);
  int col = tn * 72 + ch * 8; if (col > D_ - 8) col = D_ - 8;
  int kr = kt * 64 + k;
  if (!o) kr = kr < 512 ? 18 * (kr >> 4) + (kr & 15) : 18 * ((kr - 512) >> 1) + 16 + (kr & 1);
  return (o ? wout : wo) + (size_t)kr * D_ + col;
}
__global__ __launch_bounds__(512, 2) void k_qkv(const u16* __restrict__ x, const u16* __restrict__ wqkv, const u16* __restrict__ win, const u16* __restrict__ wout,
                                                const u16* __restrict__ wo, char* __restrict__ ws) {
  extern __shared__ __attribute__((aligned(16))) char smem[];
  const int tid = threadIdx.x, w = rfl(tid >> 6);
  const int b = blockIdx.x, xcd = b & 7, idx = b >> 3;
  char* ws_dbg = ws;
  if (b == 255 && tid < 56) *(unsigned*)(ws + WS_CNT + tid * 128) = 0u;
  if (idx >= 24) {
    STAMP(0, 0); STAMP(0, 1); STAMP(0, 2);
    const int pw = (idx - 24) * 8 + xcd, lane = tid & 63, gw = pw * 8 + w;
    constexpr int NU = (RU3 + 511) / 512;
    u32x4 v[NU];
#pragma unroll
    for (int i = 0; i < NU; ++i) { const int u = gw + 512 * i; v[i] = *(const u32x4*)retile_src(win, wout, wo, u < RU3 ? u : gw, lane); }
    const int cc = tid % 9, rg = tid / 9;
    u32x4 cv[11];
    if (rg < 56) {
#pragma unroll
      for (int i = 0; i < 11; ++i) { const int r = rg + 56 * i; cv[i] = *(const u32x4*)(win + (size_t)(r < D_ ? r : rg) * F2 + pw * 72 + cc * 8); }
    }
    const __amdgpu_buffer_rsrc_t so = mksrd(ws + WS_WIN, RU3 * 1024);
#pragma unroll
    for (int i = 0; i < NU; ++i) { const int u = gw + 512 * i; if (u < RU3) st16(so, u * 1024 + lane * 16, v[i]); }
    float* red = (float*)smem;
    if (rg < 56) {
      f32x4 sa = {0.f, 0.f, 0.f, 0.f}, sb = {0.f, 0.f, 0.f, 0.f};
#pragma unroll
      for (int i = 0; i < 11; ++i) if (rg + 56 * i < D_) {
        sa += f32x4{bflo(cv[i][0]), bfhi(cv[i][0]), bflo(cv[i][1]), bfhi(cv[i][1])};
        sb += f32x4{bflo(cv[i][2]), bfhi(cv[i][2]), bflo(cv[i][3]), bfhi(cv[i][3])};
      }
      *(f32x4*)(red + rg * 72 + cc * 8) = sa; *(f32x4*)(red + rg * 72 + cc * 8 + 4) = sb;
    }
    __syncthreads();
    if (tid < 288) {
      const int c = tid % 72, q = tid / 72;
      float t = 0.f;
#pragma unroll
      for (int r = 0; r < 14; ++r) t += red[(q * 14 + r) * 72 + c];
      red[56 * 72 + tid] = t;
    }
    __syncthreads();
    if (tid < 72) ((float*)(ws + WS_CS))[pw * 72 + tid] = (red[56 * 72 + tid] + red[56 * 72 + 72 + tid]) + (red[56 * 72 + 144 + tid] + red[56 * 72 + 216 + tid]);
    STAMP_END(0);
    return;
  }
  const int tm = (xcd & 3) * 4 + (idx & 3), tn = (xcd >> 2) * 6 + (idx >> 2);
  if (__builtin_expect(w == 4 || w == 5, 1)) {
    run_producer<C1>(smem, x + (size_t)tm * 128 * D_, 128 * D_ * 2, wqkv + tn * 144, (unsigned)((D_ * 3 * D_ - tn * 144) * 2), D_, 3 * D_, w - 4, tid & 63, ws, 0, ROT1);
    return;
  }
  if (w >= 6) {
    const int lane = tid & 63, p = w - 6, g = lane >> 4;
    f32x4 cs[5];
#pragma unroll
    for (int j = 0; j < 5; ++j) cs[j] = f32x4{0.f, 0.f, 0.f, 0.f};
    const int tb = C1::ABYTES + (16 * (g >> 1) + 4 * (g & 1) + ((lane & 15) >> 2)) * 288 + (lane & 3) * 8 + p * 32;
    for (int kt = 0; kt < 9; ++kt) {
      BAR();
      const char* sb = smem + (kt % C1::NST) * C1::STAGE + tb;
#pragma unroll
      for (int jj = 0; jj < 5; ++jj)
#pragma unroll
        for (int ks = 0; ks < 2; ++ks) {
          const char* q = sb + ks * (32 * 288) + jj * 64;
          cs[jj] = MFMA16(cat8(trread(q), trread(q + 8 * 288)), ones8(), cs[jj]);
        }
      WAITL0();
    }
    MFMA_DRAIN();
    float* st = (float*)(smem + C1::CS_OFF);
#pragma unroll
    for (int jj = 0; jj < 5; ++jj) if ((lane & 15) == 0 && p + 2 * jj < 9) *(f32x4*)(st + 16 * (p + 2 * jj) + 4 * g) = cs[jj];
    WAITL0(); BAR();
    return;
  }
  f32x4 acc[2][9]; float rs[2], rq[2];
  consumer<C1, 2, true>(smem, w * 32, tid & 63, acc, rs, rq, ws, 0);
  int tz = threadIdx.x; VREG(tz);
  const int lane = tz & 63, g = lane >> 4, fr = lane & 15;
  const float* cs = (const float*)(smem + C1::CS_OFF);
  const int kind = tn >> 2, head0 = (tn & 3) * 8;
  const int tile32 = tm * 4 + w;
  const float sc = kind == 0 ? 0.23570226039551584f * 1.4426950408889634f : 1.f;
  float mu[2], rstd[2];
#pragma unroll
  for (int i = 0; i < 2; ++i) {
    const f32x2 st = ln_stats(rs[i], rq[i], 1.f / D_);
    mu[i] = st[0]; rstd[i] = st[1] * sc;
  }
  constexpr int RP = 336;
  char* reg = smem + w * (32 * RP);
  f32x4 c4a[9];
#pragma unroll
  for (int j = 0; j < 9; ++j) c4a[j] = *(const f32x4*)(cs + 16 * j + 4 * g);
#pragma unroll
  for (int j = 0; j < 9; ++j) {
    const f32x4 c4 = c4a[j];
#pragma unroll
    for (int rr = 0; rr < 2; ++rr) {
      const int c = 16 * j + 4 * g + 2 * rr, off = c * 2 + (c / 18) * 4;
#pragma unroll
      for (int i = 0; i < 2; ++i)
        *(unsigned*)(reg + (16 * i + fr) * RP + off) = cvtpk((acc[i][j][2 * rr] - mu[i] * c4[2 * rr]) * rstd[i], (acc[i][j][2 * rr + 1] - mu[i] * c4[2 * rr + 1]) * rstd[i]);
    }
  }
  WAITL0();
  if (kind < 2) {
    char* base = ws + (kind == 0 ? WS_Q + (size_t)tile32 * QK_TILE + (size_t)head0 * Q_HEAD : WS_KV + (size_t)tile32 * KV_TILE + (size_t)head0 * KV_HEAD);
    const int hstride = kind == 0 ? Q_HEAD : KV_HEAD;
    const char* rp = reg + (lane & 31) * RP + (lane >> 5) * 16;
#pragma unroll
    for (int hl = 0; hl < 8; ++hl) {
      const u32x2 lo = *(const u32x2*)(rp + hl * 40), hi = *(const u32x2*)(rp + hl * 40 + 8);
      *(u32x4*)(base + (size_t)hl * hstride + lane * 16) = u32x4{lo[0], lo[1], hi[0], hi[1]};
    }
#pragma unroll
    for (int q = 0; q < 4; ++q) {
      const int hl = 2 * q + (lane >> 5);
      *(unsigned*)(base + (size_t)hl * hstride + 1024 + (lane & 31) * 4) = *(const unsigned*)(reg + (lane & 31) * RP + hl * 40 + 32);
    }
  } else {
    char* base = ws + WS_KV + (size_t)tile32 * KV_TILE + V_OFF + (size_t)head0 * KV_HEAD;
    const int s = g >> 1, h = g & 1;
    const char* rp = reg + (16 * s + 4 * h + (fr >> 2)) * RP + (lane & 3) * 8;
#pragma unroll
    for (int j = 0; j < 10; ++j) {
      const bf16x8 v = cat8(trread(rp + j * 32), trread(rp + 8 * RP + j * 32));
      const int c = 16 * j + fr, hl = c / 20, d = c - hl * 20;
      if (d < 18) *(bf16x8*)(base + (size_t)hl * KV_HEAD + s * 608 + h * 304 + d * 16) = v;
    }
    if (lane < 32) {
      *(bf16x8*)(base + (size_t)(lane >> 2) * KV_HEAD + (lane & 3) * 304 + 18 * 16) = ones8();
    }
  }
  STAMP_END(0);
}

DI void spin_until(const unsigned* cnt, unsigned target) {
  for (unsigned spins = 0; spins < (1u << 24); ++spins) {
    if (__hip_atomic_load(cnt, __ATOMIC_RELAXED, __HIP_MEMORY_SCOPE_AGENT) >= target) break;
    __builtin_amdgcn_s_sleep(1);
  }
}
template <int KT> using CR = Cfg<64, 5, 8, B_TILED, KT>;
constexpr int ATT_LDS = 152 * 1024;
__global__ __launch_bounds__(512, 2) void k_attn(char* __restrict__ ws, const u16* __restrict__ wsp, const u16* __restrict__ x) {
  extern __shared__ __attribute__((aligned(16))) char smem[];
  const int tid = threadIdx.x, lane = tid & 63, w = rfl(tid >> 6);
  const int b = blockIdx.x, xcd = b & 7, idx = b >> 3;
  const int head = xcd * 4 + (idx & 3), qb = idx >> 2;
  char* ws_dbg = ws; STAMP(1, 0);
  const int q31 = lane & 31, h = lane >> 5;
  const char* qp = ws + WS_Q + (size_t)head * Q_HEAD + (size_t)(qb * 8 + w) * QK_TILE;
  bf16x8 qf1; unsigned q2;
  asm volatile("global_load_dwordx4 %0, %1, off" : "=v"(qf1) : "v"(qp + lane * 16));
  asm volatile("global_load_dword %0, %1, off" : "=v"(q2) : "v"(qp + 1024 + q31 * 4));
  {
    __amdgpu_buffer_rsrc_t srd = mksrd(ws + WS_KV + (size_t)head * KV_HEAD, KV_HEAD);
    const unsigned ldsb = (unsigned)(size_t)smem + w * 1024, vo = lane * 16 + w * 1024;
#pragma unroll
    for (int j = 0; j < 19; ++j) DMAQ(ldsb, j * 8192, vo, srd, (unsigned)(j * 8192));
  }
  asm volatile("s_waitcnt vmcnt(16)" : "+v"(qf1), "+v"(q2) :: "memory");
  BAR();
  u32x4 q2v = {h == 0 ? q2 : 0u, 0u, 0u, 0u};
  const bf16x8 qf2 = __builtin_bit_cast(bf16x8, q2v);
  const int dcl = q31 < 19 ? q31 : (q31 >= 20 && q31 < 28 ? 12 : 16);
  const int koff = lane * 16, k2off = 1024 + q31 * 4, voff = V_OFF + h * 304 + dcl * 16;
  f32x16 o;
#pragma unroll
  for (int r = 0; r < 16; ++r) o[r] = 0.f;
  auto scores = [&](int t, const f32x16& c) {
    const char* tp = smem + t * KV_TILE;
    const bf16x8 k1 = *(const bf16x8*)(tp + koff);
    u32x4 k2v = {*(const unsigned*)(tp + k2off), 0u, 0u, 0u};
    f32x16 s = MFMA32(k1, qf1, c);
    return MFMA32(__builtin_bit_cast(bf16x8, k2v), qf2, s);
  };
  f32x16 zero;
#pragma unroll
  for (int r = 0; r < 16; ++r) zero[r] = 0.f;
  STAMP(1, 1);
  unsigned dummy_keep;
  f32x16 negm;
  {
    const f32x16 s = scores(0, zero);
    float m = s[0];
#pragma unroll
    for (int r = 1; r < 16; ++r) m = fmaxf(m, s[r]);
    m = fmaxf(m, xor32(m));
#pragma unroll
    for (int r = 0; r < 16; ++r) negm[r] = -m;
  }
  {
    struct Frag { bf16x8 k1, v0, v1; u32x4 k2; };
    Frag fA, fB;
    fA.k2 = u32x4{0u, 0u, 0u, 0u}; fB.k2 = u32x4{0u, 0u, 0u, 0u};
    f32x16 sA = scores(0, negm), sB;
    fA.k1 = *(const bf16x8*)(smem + KV_TILE + koff); fA.k2[0] = *(const unsigned*)(smem + KV_TILE + k2off);
    fA.v0 = *(const bf16x8*)(smem + voff); fA.v1 = *(const bf16x8*)(smem + voff + 608);
    sA[0] = __builtin_amdgcn_exp2f(sA[0]); sA[1] = __builtin_amdgcn_exp2f(sA[1]);
    auto step = [&](int t, f32x16& sc, f32x16& sn, Frag& fc, Frag& fn) {
      const char* tp = smem + t * KV_TILE;
      u32x4 p0, p1;
      fn.v1 = *(const bf16x8*)(tp + KV_TILE + voff + 608); SB();
      fn.v0 = *(const bf16x8*)(tp + KV_TILE + voff); SB();
      fn.k2[0] = *(const unsigned*)(tp + 2 * KV_TILE + k2off); SB();
      fn.k1 = *(const bf16x8*)(tp + 2 * KV_TILE + koff);
      SB();
      sn = MFMA32(fc.k1, qf1, negm);
#pragma unroll
      for (int e = 2; e < 5; ++e) sc[e] = __builtin_amdgcn_exp2f(sc[e]);
      SB();
      sn = MFMA32(__builtin_bit_cast(bf16x8, fc.k2), qf2, sn);
#pragma unroll
      for (int e = 5; e < 8; ++e) sc[e] = __builtin_amdgcn_exp2f(sc[e]);
#pragma unroll
      for (int e = 0; e < 4; ++e) p0[e] = cvtpk(sc[2 * e], sc[2 * e + 1]);
      SB();
      sc[8] = __builtin_amdgcn_exp2f(sc[8]); sc[9] = __builtin_amdgcn_exp2f(sc[9]);
      SB();
      o = MFMA32(fc.v0, __builtin_bit_cast(bf16x8, p0), o);
#pragma unroll
      for (int e = 10; e < 16; ++e) sc[e] = __builtin_amdgcn_exp2f(sc[e]);
#pragma unroll
      for (int e = 0; e < 4; ++e) p1[e] = cvtpk(sc[8 + 2 * e], sc[8 + 2 * e + 1]);
      SB();
      sn[0] = __builtin_amdgcn_exp2f(sn[0]); sn[1] = __builtin_amdgcn_exp2f(sn[1]);
      SB();
      o = MFMA32(fc.v1, __builtin_bit_cast(bf16x8, p1), o);
      SB();
    };
    unsigned dummy = 0;
    auto warm = [&]() { const unsigned T = b * 512 + tid; TOUCH(dummy, (const char*)wsp + (size_t)(T < 65536u ? T : 0u) * 128); };
#define CHUNK(c, nv) { if (c > 0) { WAITV(nv); BAR(); } if (c == 3) warm(); \
    for (int t = 8 * c; t < 8 * c + 8; t += 2) { step(t, sA, sB, fA, fB); step(t + 1, sB, sA, fB, fA); } }
    CHUNK(0, 16) CHUNK(1, 13) CHUNK(2, 11) CHUNK(3, 9) CHUNK(4, 7) CHUNK(5, 5) CHUNK(6, 3) CHUNK(7, 1)
#undef CHUNK
    VREG(dummy);
    dummy_keep = dummy;
    MFMA_DRAIN();
  }
  STAMP(1, 2);
  float l = __shfl(o[10], q31);
  bool ok = l < 1.8446744e19f;
#pragma unroll
  for (int r = 0; r < 12; ++r) ok = ok && (fabsf(o[r]) <= 3.0e38f);
  if (!__all(ok)) {
    float m = -3.0e38f;
#pragma unroll
    for (int r = 0; r < 16; ++r) o[r] = 0.f;
    for (int t = 0; t < 64; ++t) {
      f32x16 s = scores(t, zero);
      float tmax = s[0];
#pragma unroll
      for (int r = 1; r < 16; ++r) tmax = fmaxf(tmax, s[r]);
      tmax = fmaxf(tmax, xor32(tmax));
      const float mn = fmaxf(m, tmax), alpha = __builtin_amdgcn_exp2f(m - mn);
      m = mn;
#pragma unroll
      for (int r = 0; r < 16; ++r) { o[r] *= alpha; s[r] = __builtin_amdgcn_exp2f(s[r] - mn); }
      const char* tp = smem + t * KV_TILE + voff;
      const bf16x8 v0 = *(const bf16x8*)(tp), v1 = *(const bf16x8*)(tp + 608);
      u32x4 p0, p1;
#pragma unroll
      for (int e = 0; e < 4; ++e) { p0[e] = cvtpk(s[2 * e], s[2 * e + 1]); p1[e] = cvtpk(s[8 + 2 * e], s[8 + 2 * e + 1]); }
      o = MFMA32(v0, __builtin_bit_cast(bf16x8, p0), o);
      o = MFMA32(v1, __builtin_bit_cast(bf16x8, p1), o);
    }
    l = __shfl(o[10], q31);
  }
  const float inv = 1.f / l;
  {
    unsigned a0 = cvtpk(o[0] * inv, o[1] * inv), a1 = cvtpk(o[2] * inv, o[3] * inv), b0 = cvtpk(o[4] * inv, o[5] * inv), b1 = cvtpk(o[6] * inv, o[7] * inv);
    { auto r = __builtin_amdgcn_permlane32_swap(a0, b0, false, false); a0 = r[0]; b0 = r[1]; }
    { auto r = __builtin_amdgcn_permlane32_swap(a1, b1, false, false); a1 = r[0]; b1 = r[1]; }
    const __amdgpu_buffer_rsrc_t so = mksrd(ws + WS_AO + (size_t)(qb * 32 + head) * 8192, 8192);
    __builtin_amdgcn_raw_buffer_store_b128(u32x4{a0, a1, b0, b1}, so, (w * 32 + q31) * 32 + h * 16, 0, 16);
    const __amdgpu_buffer_rsrc_t s2 = mksrd(ws + WS_AO + (size_t)S_ * 1024, S_ * 128);
    if (h == 0) __builtin_amdgcn_raw_buffer_store_b32(cvtpk(o[8] * inv, o[9] * inv), s2, (qb * 256 + w * 32 + q31) * 128 + head * 4, 0, 16);
  }
  asm volatile("s_waitcnt vmcnt(0)" : "+v"(dummy_keep) :: "memory");
  WAITL0(); BAR();
  STAMP(2, 0); STAMP(1, 3);
  using C = CR<9>;
  unsigned* cnt = (unsigned*)(ws + WS_CNT + (32 + qb) * 128);
  if (w == 0 && lane == 0) __hip_atomic_fetch_add(cnt, 1u, __ATOMIC_RELAXED, __HIP_MEMORY_SCOPE_AGENT);
  if (w >= 6) return;
  const int row0 = qb * 256 + (head >> 3) * 64, tn = head & 7;
  if (w >= 4) {
    const int p = w - 4;
    const __amdgpu_buffer_rsrc_t srdA = mksrd(ws + WS_AO, S_ * 1152), srdB = mksrd(ws + WS_WO + (size_t)tn * 9 * WR_TB, 9 * WR_TB);
    const unsigned ldsb = (unsigned)(size_t)smem, mA = ldsb + p * 4096, mB = ldsb + C::ABYTES + p * 5120;
    const int c0 = (lane & 7) ^ (lane >> 4), c1 = (lane & 7) ^ (4 + (lane >> 4));
    const int rl = (head >> 3) * 64 + p * 32 + (lane >> 3);
    const unsigned va0 = (unsigned)(qb * 32 + (c0 >> 1)) * 8192 + rl * 32 + (c0 & 1) * 16, va1 = (unsigned)(qb * 32 + (c1 >> 1)) * 8192 + rl * 32 + (c1 & 1) * 16;
    const unsigned vt0 = S_ * 1024 + (qb * 256 + rl) * 128 + c0 * 16, vt1 = S_ * 1024 + (qb * 256 + rl) * 128 + c1 * 16, vb = p * 5120 + lane * 16;
    auto issueA = [&](int kt) {
      const unsigned sa = mA + (kt & 7) * C::STAGE;
      if (kt < 8) {
#pragma unroll
        for (int j = 0; j < 4; ++j) { const unsigned v = ((j & 1) ? va1 : va0) + j * 8 * 32; DMAQ_SC1(sa, j * 1024, v, srdA, (unsigned)(kt * 4 * 8192)); }
      } else {
#pragma unroll
        for (int j = 0; j < 4; ++j) { const unsigned v = ((j & 1) ? vt1 : vt0) + j * 8 * 128; DMAQ_SC1(sa, j * 1024, v, srdA, 0u); }
      }
    };
    auto issueB = [&](int kt) {
      const unsigned sb = mB + (kt & 7) * C::STAGE, sk = kt * WR_TB;
#pragma unroll
      for (int j = 0; j < 5; ++j) { const unsigned v = vb + j * 1024; DMAQ(sb, j * 1024, v, srdB, sk); }
    };
#pragma unroll
    for (int s = 0; s < 7; ++s) issueB(s);
    if (lane == 0) spin_until(cnt, 32u);
    asm volatile("" ::: "memory");
#pragma unroll
    for (int s = 0; s < 7; ++s) issueA(s);
    WAITV(24); BAR(); issueA(7); issueB(7);
    WAITV(29); BAR(); issueA(8); issueB(8);
    WAITV(34); BAR(); WAITV(30); BAR(); WAITV(26); BAR(); WAITV(22); BAR(); WAITV(18); BAR(); WAITV(9); BAR(); WAITV(0); BAR();
    BAR();
    return;
  }
  {
    const int g = lane >> 4, fr = lane & 15;
    const int tok = row0 + w * 16 + fr;
    const size_t ro = (size_t)tok * D_ + tn * 72 + 4 * g;
    u32x2 r[5];
#pragma unroll
    for (int j = 0; j < 5; ++j) r[j] = *(const u32x2*)(x + ro + ((j < 4 || g < 2) ? 16 * j : 0));
    f32x4 acc[1][5]; float rs[1], rq[1];
    consumer<C, 1, false>(smem, w * 16, lane, acc, rs, rq, ws, 7);
    u16* out = (u16*)(ws + WS_H);
    float s1 = 0.f, s2 = 0.f;
#pragma unroll
    for (int j = 0; j < 5; ++j) {
      if (j < 4 || g < 2) {
        u32x2 v;
        const unsigned p0 = cvtpk(acc[0][j][0], acc[0][j][1]), p1 = cvtpk(acc[0][j][2], acc[0][j][3]);
        v[0] = cvtpk(bflo(r[j][0]) + bflo(p0), bfhi(r[j][0]) + bfhi(p0));
        v[1] = cvtpk(bflo(r[j][1]) + bflo(p1), bfhi(r[j][1]) + bfhi(p1));
        *(u32x2*)(out + ro + 16 * j) = v;
        const float a0 = bflo(v[0]), a1 = bfhi(v[0]), a2 = bflo(v[1]), a3 = bfhi(v[1]);
        s1 += (a0 + a1) + (a2 + a3);
        s2 = fmaf(a0, a0, fmaf(a1, a1, fmaf(a2, a2, fmaf(a3, a3, s2))));
      }
    }
    s1 += xor16(s1); s1 += xor32(s1); s2 += xor16(s2); s2 += xor32(s2);
    if (g == 0) *(f32x2*)(ws + WS_HST + ((size_t)tok * 8 + tn) * 8) = f32x2{s1, s2};
  }
  STAMP_END(2);
}

constexpr int MST = 36864, MNST = 4;
constexpr int M_CSA = MNST * MST, M_CSB = M_CSA + 576, M_RS = M_CSB + 576, M_S2 = M_RS + 1024, M_LDS = M_S2 + 1024;
__global__ __launch_bounds__(448, 2) void k_mix(const u16* __restrict__ wsp, const u16* __restrict__ bsp, char* __restrict__ ws, u16* __restrict__ outp) {
  extern __shared__ __attribute__((aligned(16))) char smem[];
  const int tid = threadIdx.x, w = rfl(tid >> 6);
  const int b = blockIdx.x, xcd = b & 7, idx = b >> 3;
  const int tm = (xcd & 1) * 8 + (idx & 7), tn = (xcd >> 1) * 4 + (idx >> 3);
  char* ws_dbg = ws;
  unsigned* cnt1 = (unsigned*)(ws + WS_CNT + tm * 128);
  unsigned* cnt2 = (unsigned*)(ws + WS_CNT + (16 + tn) * 128);
  unsigned* cnt3 = (unsigned*)(ws + WS_CNT + (40 + tm) * 128);
  using CD = CR<36>;
  const int drow0 = tm * 128 + (tn >> 3) * 64, dtn = tn & 7;
  if (__builtin_expect(w == 4 || w == 5, 1)) {
    const int lane = tid & 63, p = w - 4;
    if (p == 0) STAMPW(3, 4);
    const unsigned ldsb = (unsigned)(size_t)smem;
    const unsigned mA = ldsb + p * 8192, mB = ldsb + 16384 + p * 9216;
    const unsigned sw0 = ((lane & 7) ^ (lane >> 4)) << 4, sw1 = ((lane & 7) ^ (4 + (lane >> 4))) << 4;
    const int g = lane >> 4;
    constexpr int NQ = 17;
    {
      const __amdgpu_buffer_rsrc_t srdA = mksrd((const u16*)(ws + WS_H) + (size_t)tm * 128 * D_, 128 * D_ * 2), srdB = mksrd(ws + WS_WIN, 32 * 9 * WIN_TB);
      const unsigned va0 = (p * 64 + (lane >> 3)) * (D_ * 2) + sw0, va1 = (p * 64 + (lane >> 3)) * (D_ * 2) + sw1, vb = p * 9216 + lane * 16;
      auto issue = [&](int s) {
        const int ph = s >= 9, kt = s - 9 * ph;
        const unsigned so = (s & 3) * MST, sa = mA + so, sb = mB + so;
        const unsigned soA = kt * 128, soB = ((ph ? tn : tn + 16) * 9 + kt) * WIN_TB;
#pragma unroll
        for (int j = 0; j < 8; ++j) { const unsigned v = ((j & 1) ? va1 : va0) + j * 8 * (D_ * 2); DMAQ(sa, j * 1024, v, srdA, soA); }
#pragma unroll
        for (int j = 0; j < 9; ++j) { const unsigned v = vb + j * 1024; DMAQ(sb, j * 1024, v, srdB, soB); }
      };
      issue(0); issue(1); issue(2);
      if (p == 0) STAMPW(3, 5);
      for (int s = 0; s < 15; ++s) { WAITV(2 * NQ); BAR(); issue(s + 3); }
      WAITV(2 * NQ); BAR(); WAITV(NQ); BAR(); WAITV(0); BAR();
    }
    BAR();
    BAR();
    if (p == 0) STAMPW(5, 0);
    {
      const __amdgpu_buffer_rsrc_t srdA = mksrd(wsp + (size_t)tm * 128 * S_, 128 * S_ * 2), srdB = mksrd((const u16*)(ws + WS_Z2N) + (size_t)tn * 144 * S_, 144 * S_ * 2);
      const unsigned va0 = (p * 64 + (lane >> 3)) * (S_ * 2) + sw0, va1 = (p * 64 + (lane >> 3)) * (S_ * 2) + sw1;
      const unsigned vb0 = (p * 72 + (lane >> 3)) * (S_ * 2) + (p ? sw1 : sw0), vb1 = (p * 72 + (lane >> 3)) * (S_ * 2) + (p ? sw0 : sw1);
      auto issueA = [&](int kt) {
        const unsigned sa = mA + (kt & 3) * MST, sk = kt * 128;
#pragma unroll
        for (int j = 0; j < 8; ++j) { const unsigned v = ((j & 1) ? va1 : va0) + j * 8 * (S_ * 2); DMAQ(sa, j * 1024, v, srdA, sk); }
      };
      auto issueB = [&](int kt) {
        const unsigned sb = mB + (kt & 3) * MST, sk = kt * 128;
#pragma unroll
        for (int j = 0; j < 9; ++j) { const unsigned v = ((j & 1) ? vb1 : vb0) + j * 8 * (S_ * 2); DMAQ_SC1(sb, j * 1024, v, srdB, sk); }
      };
      auto issue = [&](int kt) { issueA(kt); issueB(kt); };
      issueA(0); issueA(1); issueA(2);
      if (lane == 0) spin_until(cnt2, 16u);
      asm volatile("" ::: "memory");
      if (p == 0) STAMPW(5, 1);
      if (p == 0) STAMPW(5, 2);
      issueB(0); issueB(1); issueB(2);
      if (p == 0) STAMPW(5, 3);
      WAITV(18); BAR(); issue(3);
      WAITV(9 + NQ); BAR(); issue(4);
      for (int kt = 2; kt < 29; ++kt) { WAITV(2 * NQ); BAR(); issue(kt + 3); }
      WAITV(2 * NQ); BAR(); WAITV(NQ); BAR(); WAITV(0); BAR();
    }
    BAR();
    BAR();
    {
      const __amdgpu_buffer_rsrc_t srdA = mksrd((const u16*)(ws + WS_U) + (size_t)drow0 * FH, 64 * FH * 2), srdB = mksrd(ws + WS_WOUT + (size_t)dtn * 36 * WR_TB, 36 * WR_TB);
      const unsigned mA2 = ldsb + p * 4096, mB2 = ldsb + CD::ABYTES + p * 5120;
      const unsigned va0 = (p * 32 + (lane >> 3)) * (FH * 2) + sw0, va1 = (p * 32 + (lane >> 3)) * (FH * 2) + sw1, vb = p * 5120 + lane * 16;
      auto issueA = [&](int kt) {
        const unsigned sa = mA2 + (kt & 7) * CD::STAGE, sk = kt * 128;
#pragma unroll
        for (int j = 0; j < 4; ++j) { const unsigned v = ((j & 1) ? va1 : va0) + j * 8 * (FH * 2); DMAQ_SC1(sa, j * 1024, v, srdA, sk); }
      };
      auto issueB = [&](int kt) {
        const unsigned sb = mB2 + (kt & 7) * CD::STAGE, sk = kt * WR_TB;
#pragma unroll
        for (int j = 0; j < 5; ++j) { const unsigned v = vb + j * 1024; DMAQ(sb, j * 1024, v, srdB, sk); }
      };
#pragma unroll
      for (int s = 0; s < 7; ++s) issueB(s);
      if (lane == 0) spin_until(cnt3, 16u);
      asm volatile("" ::: "memory");
#pragma unroll
      for (int s = 0; s < 7; ++s) issueA(s);
      WAITV(24); BAR(); issueA(7); issueB(7);
      WAITV(29); BAR(); issueA(8); issueB(8);
      WAITV(34); BAR(); issueA(9); issueB(9);
      WAITV(39); BAR(); issueA(10); issueB(10);
      WAITV(44); BAR(); issueA(11); issueB(11);
      WAITV(49); BAR(); issueA(12); issueB(12);
      for (int kt = 6; kt < 29; ++kt) { WAITV(54); BAR(); issueA(kt + 7); issueB(kt + 7); }
      WAITV(54); BAR(); WAITV(45); BAR(); WAITV(36); BAR(); WAITV(27); BAR(); WAITV(18); BAR(); WAITV(9); BAR(); WAITV(0); BAR();
      BAR();
    }
    return;
  }
  if (w == 6) {
    const int lane = tid & 63;
    f32x2* rst = (f32x2*)(smem + M_RS);
#pragma unroll
    for (int i = 0; i < 2; ++i) {
      const f32x4* q = (const f32x4*)(ws + WS_HST + (size_t)(tm * 128 + 64 * i + lane) * 64);
      const f32x4 a = q[0], bb = q[1], c = q[2], d = q[3];
      const float s1 = (a[0] + a[2]) + (bb[0] + bb[2]) + (c[0] + c[2]) + (d[0] + d[2]), s2 = (a[1] + a[3]) + (bb[1] + bb[3]) + (c[1] + c[3]) + (d[1] + d[3]);
      rst[64 * i + lane] = ln_stats(s1, s2, 1.f / D_);
    }
    if (lane < 36) {
      *(f32x4*)(smem + M_CSA + lane * 16) = *(const f32x4*)(ws + WS_CS + (size_t)((tn + 16) * 144 + lane * 4) * 4);
      *(f32x4*)(smem + M_CSB + lane * 16) = *(const f32x4*)(ws + WS_CS + (size_t)(tn * 144 + lane * 4) * 4);
    }
    WAITL0();
    for (int s = 0; s < 15; ++s) BAR();
    if (lane == 0) spin_until(cnt1, 16u);
    asm volatile("" ::: "memory");
    const __amdgpu_buffer_rsrc_t srdS = mksrd(ws + WS_ST + (size_t)tm * (16 * 128 * 8), 16 * 128 * 8);
    u32x4 pv[16];
#pragma unroll
    for (int q = 0; q < 16; ++q) pv[q] = __builtin_amdgcn_raw_buffer_load_b128(srdS, (q * 128 + 2 * lane) * 8, 0, 16);
    SB(); BAR(); BAR(); SB();
    f32x4 t4 = {0.f, 0.f, 0.f, 0.f};
#pragma unroll
    for (int q = 0; q < 16; ++q) t4 += __builtin_bit_cast(f32x4, pv[q]);
    {
      const f32x2 q0 = ln_stats(t4[0], t4[1], 1.f / FH), q1 = ln_stats(t4[2], t4[3], 1.f / FH);
      *(f32x4*)(smem + M_S2 + lane * 16) = f32x4{q0[1], -q0[0] * q0[1], q1[1], -q1[0] * q1[1]};
    }
    WAITL0();
    BAR();
    BAR();
    return;
  }
  const int lane = tid & 63, g = lane >> 4, fr = lane & 15, f = fr >> 1;
  const int l0 = fr * 128 + ((g ^ f) << 4), l1 = fr * 128 + (((4 + g) ^ f) << 4);
  const int tb = 16384 + (16 * (g >> 1) + 4 * (g & 1) + (fr >> 2)) * 288 + (lane & 3) * 8;
  const int tok0 = tm * 128 + w * 32;
  f32x4 acc[2][9];
  bf16x8 a0[2], b0[9], a1[2], b1[9];
  auto zero_acc = [&]() {
#pragma unroll
    for (int i = 0; i < 2; ++i)
#pragma unroll
      for (int j = 0; j < 9; ++j) acc[i][j] = f32x4{0.f, 0.f, 0.f, 0.f};
  };
  auto mm = [&](bf16x8* a, bf16x8* bb) {
#pragma unroll
    for (int j = 0; j < 9; ++j)
#pragma unroll
      for (int i = 0; i < 2; ++i) acc[i][j] = MFMA16(bb[j], a[i], acc[i][j]);
  };
  auto rdKN = [&](bf16x8* a, bf16x8* bb, int s, int ks) {
    const char* sb = smem + (s & 3) * MST;
#pragma unroll
    for (int i = 0; i < 2; ++i) a[i] = *(const bf16x8*)(sb + w * 4096 + i * 2048 + (ks ? l1 : l0));
#pragma unroll
    for (int j = 0; j < 9; ++j) { const char* q = sb + tb + ks * (32 * 288) + j * 32; bb[j] = cat8(trread(q), trread(q + 8 * 288)); }
  };
  auto rdNK = [&](bf16x8* a, bf16x8* bb, int s, int ks) {
    const char* sb = smem + (s & 3) * MST + (ks ? l1 : l0);
#pragma unroll
    for (int i = 0; i < 2; ++i) a[i] = *(const bf16x8*)(sb + w * 4096 + i * 2048);
#pragma unroll
    for (int j = 0; j < 9; ++j) bb[j] = *(const bf16x8*)(sb + 16384 + j * 2048);
  };
#define SCHED(id, ND, RPG) _Pragma("unroll") for (int q = 0; q < 18; ++q) { __builtin_amdgcn_sched_group_barrier(0x008, 1, id); \
    if (q * RPG < ND) __builtin_amdgcn_sched_group_barrier(0x080, RPG, id); }
  auto gelu = [&](u32x2 (&zp)[2][9], int csoff, f32x2 (&sum)[2]) {
    const float c1 = -2.f * 0.7978845608028654f * 1.4426950408889634f, c2 = c1 * 0.044715f;
    const float* cs = (const float*)(smem + csoff);
    const f32x2* rst = (const f32x2*)(smem + M_RS) + w * 32;
    float nmu[2], k1[2], k3[2], irs[2];
    f32x2 s1[2], s2[2];
#pragma unroll
    for (int i = 0; i < 2; ++i) {
      const f32x2 st = rst[16 * i + fr];
      nmu[i] = -st[0]; k1[i] = c1 * st[1]; k3[i] = c2 * st[1] * st[1] * st[1]; irs[i] = __builtin_amdgcn_rcpf(st[1]);
      s1[i] = f32x2{0.f, 0.f}; s2[i] = f32x2{0.f, 0.f};
    }
    f32x4 c4 = *(const f32x4*)(cs + 4 * g);
#pragma unroll
    for (int j = 0; j < 9; ++j) {
      const f32x4 c4n = *(const f32x4*)(cs + 16 * (j < 8 ? j + 1 : 8) + 4 * g);
      SB();
#pragma unroll
      for (int i = 0; i < 2; ++i)
#pragma unroll
        for (int hh = 0; hh < 2; ++hh) {
          const f32x2 a2 = {acc[i][j][2 * hh], acc[i][j][2 * hh + 1]}, c2v = {c4[2 * hh], c4[2 * hh + 1]};
          const f32x2 t = f32x2{nmu[i], nmu[i]} * c2v + a2;
          const f32x2 y = t * (t * t * f32x2{k3[i], k3[i]} + f32x2{k1[i], k1[i]});
          const f32x2 e = {__builtin_amdgcn_exp2f(y[0]), __builtin_amdgcn_exp2f(y[1])};
          const f32x2 d = e * f32x2{irs[i], irs[i]} + f32x2{irs[i], irs[i]};
          const f32x2 z = t * f32x2{__builtin_amdgcn_rcpf(d[0]), __builtin_amdgcn_rcpf(d[1])};
          s1[i] += z; s2[i] += z * z;
          zp[i][j][hh] = cvtpk(z[0], z[1]);
        }
      VREG(zp[0][j]); VREG(zp[1][j]);
      c4 = c4n;
      SB();
    }
#pragma unroll
    for (int i = 0; i < 2; ++i) sum[i] = f32x2{s1[i][0] + s1[i][1], s2[i][0] + s2[i][1]};
  };
  const __amdgpu_buffer_rsrc_t srdS = mksrd(ws + WS_ST + (size_t)tm * (16 * 128 * 8), 16 * 128 * 8);
  zero_acc();
  STAMP(3, 0);
  BAR();
  STAMP(3, 1);
  rdKN(a0, b0, 0, 0);
  u32x2 z2p[2][9];
  auto stageAB = [&](int s) {
    SB(); rdKN(a1, b1, s, 1); mm(a0, b0); SCHED(0, 20, 2) SB();
    WAITL0(); BAR();
    SB(); rdKN(a0, b0, s + 1, 0); mm(a1, b1); SCHED(1, 20, 2)
  };
  for (int s = 0; s < 9; ++s) stageAB(s);
  MFMA_DRAIN();
  STAMP(5, 6);
  {
    f32x2 sum[2];
    gelu(z2p, M_CSA, sum);
#pragma unroll
    for (int i = 0; i < 2; ++i) {
      f32x2 v = sum[i];
      v[0] += xor16(v[0]); v[0] += xor32(v[0]); v[1] += xor16(v[1]); v[1] += xor32(v[1]);
      if (g == 0) __builtin_amdgcn_raw_buffer_store_b64(__builtin_bit_cast(u32x2, v), srdS, (tn * 128 + w * 32 + 16 * i + fr) * 8, 0, 16);
    }
    asm volatile("s_waitcnt vmcnt(0)" ::: "memory");
    zero_acc();
  }
  SB();
  STAMP(5, 4);
  stageAB(9);
  if (w == 0 && lane == 0) __hip_atomic_fetch_add(cnt1, 1u, __ATOMIC_RELAXED, __HIP_MEMORY_SCOPE_AGENT);
  for (int s = 10; s < 17; ++s) stageAB(s);
  SB(); rdKN(a1, b1, 17, 1); mm(a0, b0); mm(a1, b1); MFMA_DRAIN();
  WAITL0(); BAR();
  STAMP(3, 2);
  {
    const f32x2* st2 = (const f32x2*)(smem + M_S2) + w * 32;
    char* reg = smem + w * (32 * 304);
#pragma unroll
    for (int i = 0; i < 2; ++i) {
      const f32x2 st = st2[16 * i + fr];
#pragma unroll
      for (int j = 0; j < 9; ++j) {
        u32x2 v;
        v[0] = cvtpk(fmaf(bflo(z2p[i][j][0]), st[0], st[1]), fmaf(bfhi(z2p[i][j][0]), st[0], st[1]));
        v[1] = cvtpk(fmaf(bflo(z2p[i][j][1]), st[0], st[1]), fmaf(bfhi(z2p[i][j][1]), st[0], st[1]));
        *(u32x2*)(reg + (16 * i + fr) * 304 + (16 * j + 4 * g) * 2) = v;
      }
    }
    WAITL0();
    const __amdgpu_buffer_rsrc_t so = mksrd((u16*)(ws + WS_Z2N) + (size_t)(tn * 144) * S_ + tok0, 144 * S_ * 2);
    const char* rp = reg + (8 * g + (fr >> 2)) * 304 + (lane & 3) * 8;
#pragma unroll
    for (int j = 0; j < 9; ++j)
      __builtin_amdgcn_raw_buffer_store_b128(__builtin_bit_cast(u32x4, cat8(trread(rp + j * 32), trread(rp + 4 * 304 + j * 32))), so, (j * 16 + fr) * (S_ * 2) + 8 * g * 2, 0, 16);
    asm volatile("s_waitcnt vmcnt(0)" ::: "memory");
    WAITL0(); BAR();
    if (w == 0 && lane == 0) __hip_atomic_fetch_add(cnt2, 1u, __ATOMIC_RELAXED, __HIP_MEMORY_SCOPE_AGENT);
  }
  u32x2 z1p[2][9];
  { f32x2 sum[2]; gelu(z1p, M_CSB, sum); }
  const unsigned bias0 = bsp[tok0 + fr], bias1 = bsp[tok0 + 16 + fr];
  zero_acc();
  STAMP(4, 0);
  BAR();
  STAMP(4, 1);
  rdNK(a0, b0, 0, 0);
  for (int kt = 0; kt < 31; ++kt) {
    SB(); rdNK(a1, b1, kt, 1); mm(a0, b0); SCHED(0, 11, 1) SB();
    WAITL0(); BAR();
    SB(); rdNK(a0, b0, kt + 1, 0); mm(a1, b1); SCHED(1, 11, 1)
  }
  SB(); rdNK(a1, b1, 31, 1); mm(a0, b0); mm(a1, b1); MFMA_DRAIN();
  WAITL0(); BAR();
  SB();
  STAMP(4, 2);
#undef SCHED
  char* reg = smem + w * (32 * 304);
#pragma unroll
  for (int i = 0; i < 2; ++i) {
    const float bi = bflo(i ? bias1 : bias0);
#pragma unroll
    for (int j = 0; j < 9; ++j) {
      u32x2 v;
      const unsigned e0 = cvtpk(acc[i][j][0], acc[i][j][1]), e1 = cvtpk(acc[i][j][2], acc[i][j][3]);
      const unsigned g0 = cvtpk(bflo(e0) + bi, bfhi(e0) + bi), g1 = cvtpk(bflo(e1) + bi, bfhi(e1) + bi);
      v[0] = cvtpk(bflo(z1p[i][j][0]) * bflo(g0), bfhi(z1p[i][j][0]) * bfhi(g0));
      v[1] = cvtpk(bflo(z1p[i][j][1]) * bflo(g1), bfhi(z1p[i][j][1]) * bfhi(g1));
      *(u32x2*)(reg + (16 * i + fr) * 304 + (16 * j + 4 * g) * 2) = v;
    }
  }
  WAITL0();
  const __amdgpu_buffer_rsrc_t so = mksrd((u16*)(ws + WS_U) + (size_t)tok0 * FH + tn * 144, 32 * FH * 2);
#pragma unroll
  for (int i = 0; i < 9; ++i) {
    const int c = i * 64 + lane, row = c / 18, ch = c - row * 18;
    __builtin_amdgcn_raw_buffer_store_b128(*(const u32x4*)(reg + row * 304 + ch * 16), so, row * (FH * 2) + ch * 16, 0, 16);
  }
  asm volatile("s_waitcnt vmcnt(0)" ::: "memory");
  WAITL0(); BAR();
  STAMP(4, 3);
  if (w == 0 && lane == 0) __hip_atomic_fetch_add(cnt3, 1u, __ATOMIC_RELAXED, __HIP_MEMORY_SCOPE_AGENT);
  {
    const int tok = drow0 + w * 16 + fr;
    const size_t ro = (size_t)tok * D_ + dtn * 72 + 4 * g;
    const u16* res = (const u16*)(ws + WS_H);
    u32x2 r[5];
#pragma unroll
    for (int j = 0; j < 5; ++j) r[j] = *(const u32x2*)(res + ro + ((j < 4 || g < 2) ? 16 * j : 0));
    f32x4 acd[1][5]; float rs[1], rq[1];
    consumer<CD, 1, false>(smem, w * 16, lane, acd, rs, rq, ws, 6);
#pragma unroll
    for (int j = 0; j < 5; ++j) {
      if (j < 4 || g < 2) {
        const unsigned p0 = cvtpk(acd[0][j][0], acd[0][j][1]), p1 = cvtpk(acd[0][j][2], acd[0][j][3]);
        u32x2 v;
        v[0] = cvtpk(bflo(r[j][0]) + bflo(p0), bfhi(r[j][0]) + bfhi(p0));
        v[1] = cvtpk(bflo(r[j][1]) + bflo(p1), bfhi(r[j][1]) + bfhi(p1));
        *(u32x2*)(outp + ro + 16 * j) = v;
      }
    }
  }
}

extern "C" void kernel_launch(void* const* d_in, const int* in_sizes, int n_in, void* d_out, int out_size, void* d_ws, size_t ws_size, hipStream_t stream) {
  const u16* x = (const u16*)d_in[0];
  const u16* wqkv = (const u16*)d_in[1];
  const u16* wo = (const u16*)d_in[2];
  const u16* win = (const u16*)d_in[4];
  const u16* wsp = (const u16*)d_in[5];
  const u16* bsp = (const u16*)d_in[6];
  const u16* wout = (const u16*)d_in[7];
  char* ws = (char*)d_ws;
  (void)hipFuncSetAttribute((const void*)k_qkv, hipFuncAttributeMaxDynamicSharedMemorySize, C1::LDS);
  (void)hipFuncSetAttribute((const void*)k_attn, hipFuncAttributeMaxDynamicSharedMemorySize, ATT_LDS);
  (void)hipFuncSetAttribute((const void*)k_mix, hipFuncAttributeMaxDynamicSharedMemorySize, M_LDS);
  hipLaunchKernelGGL(k_qkv, dim3(256), dim3(512), C1::LDS, stream, x, wqkv, win, wout, wo, ws);
  hipLaunchKernelGGL(k_attn, dim3(256), dim3(512), ATT_LDS, stream, ws, wsp, x);
  hipLaunchKernelGGL(k_mix, dim3(256), dim3(448), M_LDS, stream, wsp, bsp, ws, (u16*)d_out);
}
```

```cpp
#include <hip/hip_runtime.h>
#include <stdint.h>

typedef unsigned short u16;
typedef short bf16x8 __attribute__((ext_vector_type(8)));
typedef short s16x4 __attribute__((ext_vector_type(4)));
typedef float f32x2 __attribute__((ext_vector_type(2)));
typedef float f32x4 __attribute__((ext_vector_type(4)));
typedef float f32x16 __attribute__((ext_vector_type(16)));
typedef unsigned u32x2 __attribute__((ext_vector_type(2)));
typedef unsigned u32x4 __attribute__((ext_vector_type(4)));
typedef __bf16 bf16x2_t __attribute__((ext_vector_type(2)));
typedef __attribute__((address_space(3))) s16x4* lds_s16x4_ptr;
#define DI __device__ __forceinline__

constexpr int S_ = 2048, D_ = 576, H_ = 32, DH = 18, FH = 2304, F2 = 4608;
constexpr int QK_TILE = 1152;
constexpr int V_OFF = 1152;
constexpr int KV_TILE = 1152 + 4 * 304;
constexpr int KV_HEAD = 64 * KV_TILE;
constexpr int Q_HEAD = 64 * QK_TILE;
constexpr int WIN_TB = 64 * 288, WR_TB = 64 * 160;
constexpr size_t WS_Q = 0;
constexpr size_t WS_KV = WS_Q + (size_t)H_ * Q_HEAD;
constexpr size_t WS_AO = WS_KV + (size_t)H_ * KV_HEAD + 8192;
constexpr size_t WS_H = WS_AO + (size_t)S_ * D_ * 2;
constexpr size_t WS_Z2N = WS_H + (size_t)S_ * D_ * 2;
constexpr size_t WS_ST = WS_Z2N + (size_t)S_ * FH * 2;
constexpr size_t WS_CNT = WS_ST + (size_t)S_ * 16 * 2 * 4;
constexpr size_t WS_HST = WS_CNT + 8192;
constexpr size_t WS_U0 = WS_HST + (size_t)S_ * 8 * 2 * 4;
constexpr size_t WS_U = 0;
static_assert((size_t)S_ * FH * 2 <= WS_H, "u must fit below h");
constexpr size_t WS_WIN = WS_U0;
constexpr size_t WS_WOUT = WS_WIN + (size_t)32 * 9 * WIN_TB;
constexpr size_t WS_WO = WS_WOUT + (size_t)8 * 36 * WR_TB;
constexpr size_t WS_CS = WS_WO + (size_t)8 * 9 * WR_TB;
constexpr size_t WS_END = WS_CS + (size_t)F2 * 4;

DI unsigned cvtpk(float lo, float hi) { f32x2 v = {lo, hi}; bf16x2_t b = __builtin_convertvector(v, bf16x2_t); return __builtin_bit_cast(unsigned, b); }
DI float bflo(unsigned u) { return __uint_as_float(u << 16); }
DI float bfhi(unsigned u) { return __uint_as_float(u & 0xffff0000u); }
DI float bfr(float x) { return __uint_as_float(cvtpk(x, x) << 16); }
DI f32x2 ln_stats(float s1, float s2, float inv_n) {
  const float mu = s1 * inv_n, var = fmaxf(s2 * inv_n - mu * mu, 0.f);
  return f32x2{bfr(mu), 1.f / bfr(sqrtf(bfr(bfr(var) + 1e-5f)))};
}
DI unsigned rfl(unsigned x) { return __builtin_amdgcn_readfirstlane(x); }
DI float xor16(float v) { return __shfl_xor(v, 16); }
DI float xor32(float v) { return __shfl_xor(v, 32); }
#define DMAQ(sb, imm, voff, srd, soff) asm volatile("s_add_u32 m0, %0, %1\n\ts_nop 0\n\tbuffer_load_dwordx4 %2, %3, %4 offen lds" :: "s"(sb), "i"(imm), "v"(voff), "s"(srd), "s"(soff) : "scc")
#define DMAQ_SC1(sb, imm, voff, srd, soff) asm volatile("s_add_u32 m0, %0, %1\n\ts_nop 0\n\tbuffer_load_dwordx4 %2, %3, %4 offen sc1 lds" :: "s"(sb), "i"(imm), "v"(voff), "s"(srd), "s"(soff) : "scc")
#define WAITV(n) asm volatile("s_waitcnt vmcnt(%0)" :: "i"(n) : "memory")
#define WAITL0() asm volatile("s_waitcnt lgkmcnt(0)" ::: "memory")
#define BAR() asm volatile("s_barrier" ::: "memory")
#define SB() __builtin_amdgcn_sched_barrier(0)
#define MFMA16(a, b, c) __builtin_amdgcn_mfma_f32_16x16x32_bf16((a), (b), (c), 0, 0, 0)
#define MFMA32(a, b, c) __builtin_amdgcn_mfma_f32_32x32x16_bf16((a), (b), (c), 0, 0, 0)
#define MFMA_DRAIN() do { SB(); asm volatile("s_nop 7\n\ts_nop 7\n\ts_nop 7"); SB(); } while (0)
#define VREG(x) asm volatile("" : "+v"(x))
DI __amdgpu_buffer_rsrc_t mksrd(const void* p, unsigned bytes) { return __builtin_amdgcn_make_buffer_rsrc((void*)p, (short)0, (int)bytes, 0x00020000); }
DI s16x4 trread(const char* p) { return __builtin_amdgcn_ds_read_tr16_b64_v4i16((lds_s16x4_ptr)(p)); }
DI bf16x8 cat8(s16x4 lo, s16x4 hi) { return __builtin_shufflevector(lo, hi, 0, 1, 2, 3, 4, 5, 6, 7); }
DI bf16x8 ones8() { const u32x4 o = {0x3F803F80u, 0x3F803F80u, 0x3F803F80u, 0x3F803F80u}; return __builtin_bit_cast(bf16x8, o); }
#define ROT1 (3 * (((idx >> 2) + (idx & 3)) % 3))
#define ST_AUX 16
DI void st16(__amdgpu_buffer_rsrc_t srd, unsigned off, u32x4 v) { __builtin_amdgcn_raw_buffer_store_b128(v, srd, off, 0, ST_AUX); }
#define TOUCH(dummy, ptr) asm volatile("global_load_dword %0, %1, off" : "+v"(dummy) : "v"(ptr))
#define STAMP(kid, slot)
#define STAMPW(kid, slot)
#define STAMP_END(kid)

enum { B_NK = 0, B_KN = 1, B_TILED = 2 };
template <int BM_, int NT_, int NST_, int BMODE_, int KT_>
struct Cfg {
  static constexpr int BM = BM_, NT = NT_, NST = NST_, KT = KT_, BMODE = BMODE_;
  static constexpr bool BKN = BMODE_ != B_NK;
  static constexpr int BN = NT * 16;
  static constexpr int NA = BM / 8, NB = BN / 8;
  static constexpr int ABYTES = BM * 128, BBYTES = BN * 128, STAGE = ABYTES + BBYTES;
  static constexpr int RING = NST * STAGE;
  static constexpr int CS_OFF = RING;
  static constexpr int LDS = RING + BN * 4;
};

template <class C>
DI void run_producer(char* smem, const void* Ab, unsigned abytes, const void* Bb, unsigned bbytes, int lda, int ldb, int p, int lane, char* ws_dbg, int kid, int rot) {
  if (p == 0) STAMPW(kid, 4);
  constexpr int HA = C::NA / 2, HB = C::NB / 2, NQ = HA + HB;
  static_assert(C::NA % 4 == 0 && C::NB % 2 == 0 && C::BMODE != B_NK, "piece split");
  const __amdgpu_buffer_rsrc_t srdA = mksrd(Ab, abytes), srdB = mksrd(Bb, bbytes);
  const unsigned ldsb = (unsigned)(size_t)smem;
  const unsigned mA = ldsb + p * (HA * 1024), mB = ldsb + C::ABYTES + p * (HB * 1024);
  const unsigned sw0 = ((lane & 7) ^ (lane >> 4)) << 4, sw1 = ((lane & 7) ^ (4 + (lane >> 4))) << 4;
  const unsigned va0 = (p * HA * 8 + (lane >> 3)) * (lda * 2) + sw0, va1 = (p * HA * 8 + (lane >> 3)) * (lda * 2) + sw1;
  unsigned vb[HB];
#pragma unroll
  for (int j = 0; j < HB; ++j) {
    if (C::BMODE == B_KN) {
      constexpr int CPR = C::BN / 8;
      const int c = 64 * (p * HB + j) + lane, rho = c / CPR, ch = c - rho * CPR;
      const int k = (rho & ~12) | ((rho & 4) << 1) | ((rho & 8) >> 1);
      vb[j] = k * (ldb * 2) + ch * 16;
    } else {
      vb[j] = (p * HB + j) * 1024 + lane * 16;
    }
  }
  auto issue = [&](int kt) {
    const unsigned so = (kt % C::NST) * C::STAGE, sa = mA + so, sb = mB + so;
    int kr = kt + rot; if (kr >= C::KT) kr -= C::KT;
    const unsigned soA = kr * 128, soB = C::BMODE == B_KN ? kr * 64 * ldb * 2 : kr * C::BBYTES;
#pragma unroll
    for (int j = 0; j < HA; ++j) { const unsigned v = ((j & 1) ? va1 : va0) + j * 8 * (lda * 2); DMAQ(sa, j * 1024, v, srdA, soA); }
#pragma unroll
    for (int j = 0; j < HB; ++j) DMAQ(sb, j * 1024, vb[j], srdB, soB);
  };
  constexpr int PF = C::NST - 1;
#pragma unroll
  for (int s = 0; s < PF; ++s) issue(s);
  if (p == 0) STAMPW(kid, 5);
  for (int kt = 0; kt < C::KT - PF; ++kt) { WAITV((PF - 1) * NQ); BAR(); issue(kt + PF); }
#pragma unroll
  for (int r = PF - 1; r >= 0; --r) { WAITV(r * NQ); BAR(); }
  BAR();
}

template <class C, int MI, bool ROWSTAT>
DI void consumer(char* smem, int row0, int lane, f32x4 (&acc)[MI][C::NT], float (&rs)[MI], float (&rq)[MI], char* ws_dbg, int kid) {
  constexpr int NT = C::NT;
  const int g = lane >> 4, fr = lane & 15, f = fr >> 1;
  const int la0 = row0 * 128 + fr * 128 + ((g ^ f) << 4), la1 = row0 * 128 + fr * 128 + (((4 + g) ^ f) << 4);
  const int tb = C::ABYTES + (16 * (g >> 1) + 4 * (g & 1) + (fr >> 2)) * (C::BN * 2) + (lane & 3) * 8;
  f32x4 ars[MI], arq[MI];
#pragma unroll
  for (int i = 0; i < MI; ++i) {
    ars[i] = f32x4{0.f, 0.f, 0.f, 0.f}; arq[i] = f32x4{0.f, 0.f, 0.f, 0.f};
#pragma unroll
    for (int j = 0; j < NT; ++j) acc[i][j] = f32x4{0.f, 0.f, 0.f, 0.f};
  }
  bf16x8 a0[MI], b0[NT], a1[MI], b1[NT];
  auto rd = [&](bf16x8* a, bf16x8* bb, int kt, int ks) {
    const char* sb = smem + (kt % C::NST) * C::STAGE;
#pragma unroll
    for (int i = 0; i < MI; ++i) a[i] = *(const bf16x8*)(sb + (ks ? la1 : la0) + i * 2048);
#pragma unroll
    for (int j = 0; j < NT; ++j) { const char* p = sb + tb + ks * (32 * C::BN * 2) + j * 32; bb[j] = cat8(trread(p), trread(p + 8 * C::BN * 2)); }
  };
  auto mm = [&](bf16x8* a, bf16x8* bb) {
#pragma unroll
    for (int j = 0; j < NT; ++j)
#pragma unroll
      for (int i = 0; i < MI; ++i) acc[i][j] = MFMA16(bb[j], a[i], acc[i][j]);
    if constexpr (ROWSTAT) {
#pragma unroll
      for (int i = 0; i < MI; ++i) { ars[i] = MFMA16(ones8(), a[i], ars[i]); arq[i] = MFMA16(a[i], a[i], arq[i]); }
    }
  };
  constexpr int NM = MI * NT + (ROWSTAT ? 2 * MI : 0), ND = MI + NT * 2, RPG = (ND + (NM * 2 / 3) - 1) / (NM * 2 / 3);
#define SCHED(id) _Pragma("unroll") for (int q = 0; q < NM; ++q) { __builtin_amdgcn_sched_group_barrier(0x008, 1, id); \
    if (q * RPG < ND) __builtin_amdgcn_sched_group_barrier(0x080, RPG, id); }
  STAMP(kid, 0);
  BAR();
  STAMP(kid, 1);
  rd(a0, b0, 0, 0);
  for (int kt = 0; kt < C::KT - 1; ++kt) {
    SB();
    rd(a1, b1, kt, 1);
    mm(a0, b0);
    SCHED(0)
    SB();
    WAITL0(); BAR();
    SB();
    rd(a0, b0, kt + 1, 0);
    mm(a1, b1);
    SCHED(1)
  }
  SB();
  rd(a1, b1, C::KT - 1, 1);
  mm(a0, b0);
  mm(a1, b1);
#undef SCHED
  MFMA_DRAIN();
  if constexpr (ROWSTAT) {
#pragma unroll
    for (int i = 0; i < MI; ++i) {
      rs[i] = ars[i][0];
      const float d = (fr & 2) ? ((fr & 1) ? arq[i][3] : arq[i][2]) : ((fr & 1) ? arq[i][1] : arq[i][0]);
      rq[i] = __shfl(d, fr + 16 * (fr >> 2));
    }
  }
  WAITL0(); BAR();
  SB();
  STAMP(kid, 2);
}

using C1 = Cfg<128, 9, 4, B_KN, 9>;
constexpr int RU1 = 32 * 9 * 18, RU2 = RU1 + 8 * 36 * 10, RU3 = RU2 + 8 * 9 * 10;
DI const u16* retile_src(const u16* __restrict__ win, const u16* __restrict__ wout, const u16* __restrict__ wo, int u, int lane) {
  if (u < RU1) {
    const int tile = u / 18, q = u - tile * 18, tn = tile / 9, kt = tile - tn * 9;
    const int c = 64 * q + lane, rho = c / 18, ch = c - rho * 18;
    const int k = (rho & ~12) | ((rho & 4) << 1) | ((rho & 8) >> 1);
    return win + (size_t)(kt * 64 + k) * F2 + tn * 144 + ch * 8;
  }
  const bool o = u < RU2; const int v = u - (o ? RU1 : RU2), KT = o ? 36 : 9;
  const int tile = v / 10, q = v - tile * 10, tn = tile / KT, kt = tile - tn * KT;
  const int c = 64 * q + lane, rho = c / 10, ch = c - rho * 10;
  const int k = (rho & ~12) | ((rho & 4) << 1) | ((rho & 8) >> 1);
  int col = tn * 72 + ch * 8; if (col > D_ - 8) col = D_ - 8;
  int kr = kt * 64 + k;
  if (!o) kr = kr < 512 ? 18 * (kr >> 4) + (kr & 15) : 18 * ((kr - 512) >> 1) + 16 + (kr & 1);
  return (o ? wout : wo) + (size_t)kr * D_ + col;
}
__global__ __launch_bounds__(512, 2) void k_qkv(const u16* __restrict__ x, const u16* __restrict__ wqkv, const u16* __restrict__ win, const u16* __restrict__ wout,
                                                const u16* __restrict__ wo, char* __restrict__ ws) {
  extern __shared__ __attribute__((aligned(16))) char smem[];
  const int tid = threadIdx.x, w = rfl(tid >> 6);
  const int b = blockIdx.x, xcd = b & 7, idx = b >> 3;
  char* ws_dbg = ws;
  if (b == 255 && tid < 56) *(unsigned*)(ws + WS_CNT + tid * 128) = 0u;
  if (idx >= 24) {
    STAMP(0, 0); STAMP(0, 1); STAMP(0, 2);
    const int pw = (idx - 24) * 8 + xcd, lane = tid & 63, gw = pw * 8 + w;
    constexpr int NU = (RU3 + 511) / 512;
    u32x4 v[NU];
#pragma unroll
    for (int i = 0; i < NU; ++i) { const int u = gw + 512 * i; v[i] = *(const u32x4*)retile_src(win, wout, wo, u < RU3 ? u : gw, lane); }
    const int cc = tid % 9, rg = tid / 9;
    u32x4 cv[11];
    if (rg < 56) {
#pragma unroll
      for (int i = 0; i < 11; ++i) { const int r = rg + 56 * i; cv[i] = *(const u32x4*)(win + (size_t)(r < D_ ? r : rg) * F2 + pw * 72 + cc * 8); }
    }
    const __amdgpu_buffer_rsrc_t so = mksrd(ws + WS_WIN, RU3 * 1024);
#pragma unroll
    for (int i = 0; i < NU; ++i) { const int u = gw + 512 * i; if (u < RU3) st16(so, u * 1024 + lane * 16, v[i]); }
    float* red = (float*)smem;
    if (rg < 56) {
      f32x4 sa = {0.f, 0.f, 0.f, 0.f}, sb = {0.f, 0.f, 0.f, 0.f};
#pragma unroll
      for (int i = 0; i < 11; ++i) if (rg + 56 * i < D_) {
        sa += f32x4{bflo(cv[i][0]), bfhi(cv[i][0]), bflo(cv[i][1]), bfhi(cv[i][1])};
        sb += f32x4{bflo(cv[i][2]), bfhi(cv[i][2]), bflo(cv[i][3]), bfhi(cv[i][3])};
      }
      *(f32x4*)(red + rg * 72 + cc * 8) = sa; *(f32x4*)(red + rg * 72 + cc * 8 + 4) = sb;
    }
    __syncthreads();
    if (tid < 288) {
      const int c = tid % 72, q = tid / 72;
      float t = 0.f;
#pragma unroll
      for (int r = 0; r < 14; ++r) t += red[(q * 14 + r) * 72 + c];
      red[56 * 72 + tid] = t;
    }
    __syncthreads();
    if (tid < 72) ((float*)(ws + WS_CS))[pw * 72 + tid] = (red[56 * 72 + tid] + red[56 * 72 + 72 + tid]) + (red[56 * 72 + 144 + tid] + red[56 * 72 + 216 + tid]);
    STAMP_END(0);
    return;
  }
  const int tm = (xcd & 3) * 4 + (idx & 3), tn = (xcd >> 2) * 6 + (idx >> 2);
  if (__builtin_expect(w == 4 || w == 5, 1)) {
    run_producer<C1>(smem, x + (size_t)tm * 128 * D_, 128 * D_ * 2, wqkv + tn * 144, (unsigned)((D_ * 3 * D_ - tn * 144) * 2), D_, 3 * D_, w - 4, tid & 63, ws, 0, ROT1);
    return;
  }
  if (w >= 6) {
    const int lane = tid & 63, p = w - 6, g = lane >> 4;
    f32x4 cs[5];
#pragma unroll
    for (int j = 0; j < 5; ++j) cs[j] = f32x4{0.f, 0.f, 0.f, 0.f};
    const int tb = C1::ABYTES + (16 * (g >> 1) + 4 * (g & 1) + ((lane & 15) >> 2)) * 288 + (lane & 3) * 8 + p * 32;
    for (int kt = 0; kt < 9; ++kt) {
      BAR();
      const char* sb = smem + (kt % C1::NST) * C1::STAGE + tb;
#pragma unroll
      for (int jj = 0; jj < 5; ++jj)
#pragma unroll
        for (int ks = 0; ks < 2; ++ks) {
          const char* q = sb + ks * (32 * 288) + jj * 64;
          cs[jj] = MFMA16(cat8(trread(q), trread(q + 8 * 288)), ones8(), cs[jj]);
        }
      WAITL0();
    }
    MFMA_DRAIN();
    float* st = (float*)(smem + C1::CS_OFF);
#pragma unroll
    for (int jj = 0; jj < 5; ++jj) if ((lane & 15) == 0 && p + 2 * jj < 9) *(f32x4*)(st + 16 * (p + 2 * jj) + 4 * g) = cs[jj];
    WAITL0(); BAR();
    return;
  }
  f32x4 acc[2][9]; float rs[2], rq[2];
  consumer<C1, 2, true>(smem, w * 32, tid & 63, acc, rs, rq, ws, 0);
  int tz = threadIdx.x; VREG(tz);
  const int lane = tz & 63, g = lane >> 4, fr = lane & 15;
  const float* cs = (const float*)(smem + C1::CS_OFF);
  const int kind = tn >> 2, head0 = (tn & 3) * 8;
  const int tile32 = tm * 4 + w;
  const float sc = kind == 0 ? 0.23570226039551584f * 1.4426950408889634f : 1.f;
  float mu[2], rstd[2];
#pragma unroll
  for (int i = 0; i < 2; ++i) {
    const f32x2 st = ln_stats(rs[i], rq[i], 1.f / D_);
    mu[i] = st[0]; rstd[i] = st[1] * sc;
  }
  constexpr int RP = 336;
  char* reg = smem + w * (32 * RP);
  f32x4 c4a[9];
#pragma unroll
  for (int j = 0; j < 9; ++j) c4a[j] = *(const f32x4*)(cs + 16 * j + 4 * g);
#pragma unroll
  for (int j = 0; j < 9; ++j) {
    const f32x4 c4 = c4a[j];
#pragma unroll
    for (int rr = 0; rr < 2; ++rr) {
      const int c = 16 * j + 4 * g + 2 * rr, off = c * 2 + (c / 18) * 4;
#pragma unroll
      for (int i = 0; i < 2; ++i)
        *(unsigned*)(reg + (16 * i + fr) * RP + off) = cvtpk((acc[i][j][2 * rr] - mu[i] * c4[2 * rr]) * rstd[i], (acc[i][j][2 * rr + 1] - mu[i] * c4[2 * rr + 1]) * rstd[i]);
    }
  }
  WAITL0();
  if (kind < 2) {
    char* base = ws + (kind == 0 ? WS_Q + (size_t)tile32 * QK_TILE + (size_t)head0 * Q_HEAD : WS_KV + (size_t)tile32 * KV_TILE + (size_t)head0 * KV_HEAD);
    const int hstride = kind == 0 ? Q_HEAD : KV_HEAD;
    const char* rp = reg + (lane & 31) * RP + (lane >> 5) * 16;
#pragma unroll
    for (int hl = 0; hl < 8; ++hl) {
      const u32x2 lo = *(const u32x2*)(rp + hl * 40), hi = *(const u32x2*)(rp + hl * 40 + 8);
      *(u32x4*)(base + (size_t)hl * hstride + lane * 16) = u32x4{lo[0], lo[1], hi[0], hi[1]};
    }
#pragma unroll
    for (int q = 0; q < 4; ++q) {
      const int hl = 2 * q + (lane >> 5);
      *(unsigned*)(base + (size_t)hl * hstride + 1024 + (lane & 31) * 4) = *(const unsigned*)(reg + (lane & 31) * RP + hl * 40 + 32);
    }
  } else {
    char* base = ws + WS_KV + (size_t)tile32 * KV_TILE + V_OFF + (size_t)head0 * KV_HEAD;
    const int s = g >> 1, h = g & 1;
    const char* rp = reg + (16 * s + 4 * h + (fr >> 2)) * RP + (lane & 3) * 8;
#pragma unroll
    for (int j = 0; j < 10; ++j) {
      const bf16x8 v = cat8(trread(rp + j * 32), trread(rp + 8 * RP + j * 32));
      const int c = 16 * j + fr, hl = c / 20, d = c - hl * 20;
      if (d < 18) *(bf16x8*)(base + (size_t)hl * KV_HEAD + s * 608 + h * 304 + d * 16) = v;
    }
    if (lane < 32) {
      *(bf16x8*)(base + (size_t)(lane >> 2) * KV_HEAD + (lane & 3) * 304 + 18 * 16) = ones8();
    }
  }
  STAMP_END(0);
}

DI void spin_until(const unsigned* cnt, unsigned target) {
  for (unsigned spins = 0; spins < (1u << 24); ++spins) {
    if (__hip_atomic_load(cnt, __ATOMIC_RELAXED, __HIP_MEMORY_SCOPE_AGENT) >= target) break;
    __builtin_amdgcn_s_sleep(1);
  }
}
template <int KT> using CR = Cfg<64, 5, 8, B_TILED, KT>;
constexpr int ATT_LDS = 152 * 1024;
__global__ __launch_bounds__(512, 2) void k_attn(char* __restrict__ ws, const u16* __restrict__ wsp, const u16* __restrict__ x, const char* __restrict__ outp) {
  extern __shared__ __attribute__((aligned(16))) char smem[];
  const int tid = threadIdx.x, lane = tid & 63, w = rfl(tid >> 6);
  const int b = blockIdx.x, xcd = b & 7, idx = b >> 3;
  const int head = xcd * 4 + (idx & 3), qb = idx >> 2;
  char* ws_dbg = ws; STAMP(1, 0);
  const int q31 = lane & 31, h = lane >> 5;
  const char* qp = ws + WS_Q + (size_t)head * Q_HEAD + (size_t)(qb * 8 + w) * QK_TILE;
  bf16x8 qf1; unsigned q2;
  asm volatile("global_load_dwordx4 %0, %1, off" : "=v"(qf1) : "v"(qp + lane * 16));
  asm volatile("global_load_dword %0, %1, off" : "=v"(q2) : "v"(qp + 1024 + q31 * 4));
  {
    __amdgpu_buffer_rsrc_t srd = mksrd(ws + WS_KV + (size_t)head * KV_HEAD, KV_HEAD);
    const unsigned ldsb = (unsigned)(size_t)smem + w * 1024, vo = lane * 16 + w * 1024;
#pragma unroll
    for (int j = 0; j < 19; ++j) DMAQ(ldsb, j * 8192, vo, srd, (unsigned)(j * 8192));
  }
  asm volatile("s_waitcnt vmcnt(16)" : "+v"(qf1), "+v"(q2) :: "memory");
  BAR();
  u32x4 q2v = {h == 0 ? q2 : 0u, 0u, 0u, 0u};
  const bf16x8 qf2 = __builtin_bit_cast(bf16x8, q2v);
  const int dcl = q31 < 19 ? q31 : (q31 >= 20 && q31 < 28 ? 12 : 16);
  const int koff = lane * 16, k2off = 1024 + q31 * 4, voff = V_OFF + h * 304 + dcl * 16;
  f32x16 o;
#pragma unroll
  for (int r = 0; r < 16; ++r) o[r] = 0.f;
  auto scores = [&](int t, const f32x16& c) {
    const char* tp = smem + t * KV_TILE;
    const bf16x8 k1 = *(const bf16x8*)(tp + koff);
    u32x4 k2v = {*(const unsigned*)(tp + k2off), 0u, 0u, 0u};
    f32x16 s = MFMA32(k1, qf1, c);
    return MFMA32(__builtin_bit_cast(bf16x8, k2v), qf2, s);
  };
  f32x16 zero;
#pragma unroll
  for (int r = 0; r < 16; ++r) zero[r] = 0.f;
  STAMP(1, 1);
  unsigned dummy_keep;
  f32x16 negm;
  {
    const f32x16 s = scores(0, zero);
    float m = s[0];
#pragma unroll
    for (int r = 1; r < 16; ++r) m = fmaxf(m, s[r]);
    m = fmaxf(m, xor32(m));
#pragma unroll
    for (int r = 0; r < 16; ++r) negm[r] = -m;
  }
  {
    struct Frag { bf16x8 k1, v0, v1; u32x4 k2; };
    Frag fA, fB;
    fA.k2 = u32x4{0u, 0u, 0u, 0u}; fB.k2 = u32x4{0u, 0u, 0u, 0u};
    f32x16 sA = scores(0, negm), sB;
    fA.k1 = *(const bf16x8*)(smem + KV_TILE + koff); fA.k2[0] = *(const unsigned*)(smem + KV_TILE + k2off);
    fA.v0 = *(const bf16x8*)(smem + voff); fA.v1 = *(const bf16x8*)(smem + voff + 608);
    sA[0] = __builtin_amdgcn_exp2f(sA[0]); sA[1] = __builtin_amdgcn_exp2f(sA[1]);
    auto step = [&](int t, f32x16& sc, f32x16& sn, Frag& fc, Frag& fn) {
      const char* tp = smem + t * KV_TILE;
      u32x4 p0, p1;
      fn.v1 = *(const bf16x8*)(tp + KV_TILE + voff + 608); SB();
      fn.v0 = *(const bf16x8*)(tp + KV_TILE + voff); SB();
      fn.k2[0] = *(const unsigned*)(tp + 2 * KV_TILE + k2off); SB();
      fn.k1 = *(const bf16x8*)(tp + 2 * KV_TILE + koff);
      SB();
      sn = MFMA32(fc.k1, qf1, negm);
#pragma unroll
      for (int e = 2; e < 5; ++e) sc[e] = __builtin_amdgcn_exp2f(sc[e]);
      SB();
      sn = MFMA32(__builtin_bit_cast(bf16x8, fc.k2), qf2, sn);
#pragma unroll
      for (int e = 5; e < 8; ++e) sc[e] = __builtin_amdgcn_exp2f(sc[e]);
#pragma unroll
      for (int e = 0; e < 4; ++e) p0[e] = cvtpk(sc[2 * e], sc[2 * e + 1]);
      SB();
      sc[8] = __builtin_amdgcn_exp2f(sc[8]); sc[9] = __builtin_amdgcn_exp2f(sc[9]);
      SB();
      o = MFMA32(fc.v0, __builtin_bit_cast(bf16x8, p0), o);
#pragma unroll
      for (int e = 10; e < 16; ++e) sc[e] = __builtin_amdgcn_exp2f(sc[e]);
#pragma unroll
      for (int e = 0; e < 4; ++e) p1[e] = cvtpk(sc[8 + 2 * e], sc[8 + 2 * e + 1]);
      SB();
      sn[0] = __builtin_amdgcn_exp2f(sn[0]); sn[1] = __builtin_amdgcn_exp2f(sn[1]);
      SB();
      o = MFMA32(fc.v1, __builtin_bit_cast(bf16x8, p1), o);
      SB();
    };
    unsigned dummy = 0;
    auto warm = [&]() { const unsigned T = b * 512 + tid;
      TOUCH(dummy, T < 65536u ? (const char*)wsp + (size_t)T * 128 : ws + WS_Z2N + (size_t)(T - 65536u) * 128);
      TOUCH(dummy, T < 8192u ? ws + WS_Z2N + (size_t)(65536u + T) * 128 : outp + (size_t)(T < 26624u ? T - 8192u : 0u) * 128); };
#define CHUNK(c, nv) { if (c > 0) { WAITV(nv); BAR(); } if (c == 3) warm(); \
    for (int t = 8 * c; t < 8 * c + 8; t += 2) { step(t, sA, sB, fA, fB); step(t + 1, sB, sA, fB, fA); } }
    CHUNK(0, 16) CHUNK(1, 13) CHUNK(2, 11) CHUNK(3, 9) CHUNK(4, 8) CHUNK(5, 6) CHUNK(6, 4) CHUNK(7, 2)
#undef CHUNK
    VREG(dummy);
    dummy_keep = dummy;
    MFMA_DRAIN();
  }
  STAMP(1, 2);
  float l = __shfl(o[10], q31);
  bool ok = l < 1.8446744e19f;
#pragma unroll
  for (int r = 0; r < 12; ++r) ok = ok && (fabsf(o[r]) <= 3.0e38f);
  if (!__all(ok)) {
    float m = -3.0e38f;
#pragma unroll
    for (int r = 0; r < 16; ++r) o[r] = 0.f;
    for (int t = 0; t < 64; ++t) {
      f32x16 s = scores(t, zero);
      float tmax = s[0];
#pragma unroll
      for (int r = 1; r < 16; ++r) tmax = fmaxf(tmax, s[r]);
      tmax = fmaxf(tmax, xor32(tmax));
      const float mn = fmaxf(m, tmax), alpha = __builtin_amdgcn_exp2f(m - mn);
      m = mn;
#pragma unroll
      for (int r = 0; r < 16; ++r) { o[r] *= alpha; s[r] = __builtin_amdgcn_exp2f(s[r] - mn); }
      const char* tp = smem + t * KV_TILE + voff;
      const bf16x8 v0 = *(const bf16x8*)(tp), v1 = *(const bf16x8*)(tp + 608);
      u32x4 p0, p1;
#pragma unroll
      for (int e = 0; e < 4; ++e) { p0[e] = cvtpk(s[2 * e], s[2 * e + 1]); p1[e] = cvtpk(s[8 + 2 * e], s[8 + 2 * e + 1]); }
      o = MFMA32(v0, __builtin_bit_cast(bf16x8, p0), o);
      o = MFMA32(v1, __builtin_bit_cast(bf16x8, p1), o);
    }
    l = __shfl(o[10], q31);
  }
  const float inv = 1.f / l;
  {
    unsigned a0 = cvtpk(o[0] * inv, o[1] * inv), a1 = cvtpk(o[2] * inv, o[3] * inv), b0 = cvtpk(o[4] * inv, o[5] * inv), b1 = cvtpk(o[6] * inv, o[7] * inv);
    { auto r = __builtin_amdgcn_permlane32_swap(a0, b0, false, false); a0 = r[0]; b0 = r[1]; }
    { auto r = __builtin_amdgcn_permlane32_swap(a1, b1, false, false); a1 = r[0]; b1 = r[1]; }
    const __amdgpu_buffer_rsrc_t so = mksrd(ws + WS_AO + (size_t)(qb * 32 + head) * 8192, 8192);
    __builtin_amdgcn_raw_buffer_store_b128(u32x4{a0, a1, b0, b1}, so, (w * 32 + q31) * 32 + h * 16, 0, 16);
    const __amdgpu_buffer_rsrc_t s2 = mksrd(ws + WS_AO + (size_t)S_ * 1024, S_ * 128);
    if (h == 0) __builtin_amdgcn_raw_buffer_store_b32(cvtpk(o[8] * inv, o[9] * inv), s2, (qb * 256 + w * 32 + q31) * 128 + head * 4, 0, 16);
  }
  asm volatile("s_waitcnt vmcnt(0)" : "+v"(dummy_keep) :: "memory");
  WAITL0(); BAR();
  STAMP(2, 0); STAMP(1, 3);
  using C = CR<9>;
  unsigned* cnt = (unsigned*)(ws + WS_CNT + (32 + qb) * 128);
  if (w == 0 && lane == 0) __hip_atomic_fetch_add(cnt, 1u, __ATOMIC_RELAXED, __HIP_MEMORY_SCOPE_AGENT);
  if (w >= 6) return;
  const int row0 = qb * 256 + (head >> 3) * 64, tn = head & 7;
  if (w >= 4) {
    const int p = w - 4;
    const __amdgpu_buffer_rsrc_t srdA = mksrd(ws + WS_AO, S_ * 1152), srdB = mksrd(ws + WS_WO + (size_t)tn * 9 * WR_TB, 9 * WR_TB);
    const unsigned ldsb = (unsigned)(size_t)smem, mA = ldsb + p * 4096, mB = ldsb + C::ABYTES + p * 5120;
    const int c0 = (lane & 7) ^ (lane >> 4), c1 = (lane & 7) ^ (4 + (lane >> 4));
    const int rl = (head >> 3) * 64 + p * 32 + (lane >> 3);
    const unsigned va0 = (unsigned)(qb * 32 + (c0 >> 1)) * 8192 + rl * 32 + (c0 & 1) * 16, va1 = (unsigned)(qb * 32 + (c1 >> 1)) * 8192 + rl * 32 + (c1 & 1) * 16;
    const unsigned vt0 = S_ * 1024 + (qb * 256 + rl) * 128 + c0 * 16, vt1 = S_ * 1024 + (qb * 256 + rl) * 128 + c1 * 16, vb = p * 5120 + lane * 16;
    auto issueA = [&](int kt) {
      const unsigned sa = mA + (kt & 7) * C::STAGE;
      if (kt < 8) {
#pragma unroll
        for (int j = 0; j < 4; ++j) { const unsigned v = ((j & 1) ? va1 : va0) + j * 8 * 32; DMAQ_SC1(sa, j * 1024, v, srdA, (unsigned)(kt * 4 * 8192)); }
      } else {
#pragma unroll
        for (int j = 0; j < 4; ++j) { const unsigned v = ((j & 1) ? vt1 : vt0) + j * 8 * 128; DMAQ_SC1(sa, j * 1024, v, srdA, 0u); }
      }
    };
    auto issueB = [&](int kt) {
      const unsigned sb = mB + (kt & 7) * C::STAGE, sk = kt * WR_TB;
#pragma unroll
      for (int j = 0; j < 5; ++j) { const unsigned v = vb + j * 1024; DMAQ(sb, j * 1024, v, srdB, sk); }
    };
#pragma unroll
    for (int s = 0; s < 7; ++s) issueB(s);
    if (lane == 0) spin_until(cnt, 32u);
    asm volatile("" ::: "memory");
#pragma unroll
    for (int s = 0; s < 7; ++s) issueA(s);
    WAITV(24); BAR(); issueA(7); issueB(7);
    WAITV(29); BAR(); issueA(8); issueB(8);
    WAITV(34); BAR(); WAITV(30); BAR(); WAITV(26); BAR(); WAITV(22); BAR(); WAITV(18); BAR(); WAITV(9); BAR(); WAITV(0); BAR();
    BAR();
    return;
  }
  {
    const int g = lane >> 4, fr = lane & 15;
    const int tok = row0 + w * 16 + fr;
    const size_t ro = (size_t)tok * D_ + tn * 72 + 4 * g;
    u32x2 r[5];
#pragma unroll
    for (int j = 0; j < 5; ++j) r[j] = *(const u32x2*)(x + ro + ((j < 4 || g < 2) ? 16 * j : 0));
    f32x4 acc[1][5]; float rs[1], rq[1];
    consumer<C, 1, false>(smem, w * 16, lane, acc, rs, rq, ws, 7);
    u16* out = (u16*)(ws + WS_H);
    float s1 = 0.f, s2 = 0.f;
#pragma unroll
    for (int j = 0; j < 5; ++j) {
      if (j < 4 || g < 2) {
        u32x2 v;
        const unsigned p0 = cvtpk(acc[0][j][0], acc[0][j][1]), p1 = cvtpk(acc[0][j][2], acc[0][j][3]);
        v[0] = cvtpk(bflo(r[j][0]) + bflo(p0), bfhi(r[j][0]) + bfhi(p0));
        v[1] = cvtpk(bflo(r[j][1]) + bflo(p1), bfhi(r[j][1]) + bfhi(p1));
        *(u32x2*)(out + ro + 16 * j) = v;
        const float a0 = bflo(v[0]), a1 = bfhi(v[0]), a2 = bflo(v[1]), a3 = bfhi(v[1]);
        s1 += (a0 + a1) + (a2 + a3);
        s2 = fmaf(a0, a0, fmaf(a1, a1, fmaf(a2, a2, fmaf(a3, a3, s2))));
      }
    }
    s1 += xor16(s1); s1 += xor32(s1); s2 += xor16(s2); s2 += xor32(s2);
    if (g == 0) *(f32x2*)(ws + WS_HST + ((size_t)tok * 8 + tn) * 8) = f32x2{s1, s2};
  }
  STAMP_END(2);
}

constexpr int MST = 36864, MNST = 4;
constexpr int M_CSA = MNST * MST, M_CSB = M_CSA + 576, M_RS = M_CSB + 576, M_S2 = M_RS + 1024, M_LDS = M_S2 + 1024;
__global__ __launch_bounds__(448, 2) void k_mix(const u16* __restrict__ wsp, const u16* __restrict__ bsp, char* __restrict__ ws, u16* __restrict__ outp) {
  extern __shared__ __attribute__((aligned(16))) char smem[];
  const int tid = threadIdx.x, w = rfl(tid >> 6);
  const int b = blockIdx.x, xcd = b & 7, idx = b >> 3;
  const int tm = (xcd & 1) * 8 + (idx & 7), tn = (xcd >> 1) * 4 + (idx >> 3);
  char* ws_dbg = ws;
  unsigned* cnt1 = (unsigned*)(ws + WS_CNT + tm * 128);
  unsigned* cnt2 = (unsigned*)(ws + WS_CNT + (16 + tn) * 128);
  unsigned* cnt3 = (unsigned*)(ws + WS_CNT + (40 + tm) * 128);
  using CD = CR<36>;
  const int drow0 = tm * 128 + (tn >> 3) * 64, dtn = tn & 7;
  if (__builtin_expect(w == 4 || w == 5, 1)) {
    const int lane = tid & 63, p = w - 4;
    if (p == 0) STAMPW(3, 4);
    const unsigned ldsb = (unsigned)(size_t)smem;
    const unsigned mA = ldsb + p * 8192, mB = ldsb + 16384 + p * 9216;
    const unsigned sw0 = ((lane & 7) ^ (lane >> 4)) << 4, sw1 = ((lane & 7) ^ (4 + (lane >> 4))) << 4;
    const int g = lane >> 4;
    constexpr int NQ = 17;
    {
      const __amdgpu_buffer_rsrc_t srdA = mksrd((const u16*)(ws + WS_H) + (size_t)tm * 128 * D_, 128 * D_ * 2), srdB = mksrd(ws + WS_WIN, 32 * 9 * WIN_TB);
      const unsigned va0 = (p * 64 + (lane >> 3)) * (D_ * 2) + sw0, va1 = (p * 64 + (lane >> 3)) * (D_ * 2) + sw1, vb = p * 9216 + lane * 16;
      auto issue = [&](int s) {
        const int ph = s >= 9, kt = s - 9 * ph;
        const unsigned so = (s & 3) * MST, sa = mA + so, sb = mB + so;
        const unsigned soA = kt * 128, soB = ((ph ? tn : tn + 16) * 9 + kt) * WIN_TB;
#pragma unroll
        for (int j = 0; j < 8; ++j) { const unsigned v = ((j & 1) ? va1 : va0) + j * 8 * (D_ * 2); DMAQ(sa, j * 1024, v, srdA, soA); }
#pragma unroll
        for (int j = 0; j < 9; ++j) { const unsigned v = vb + j * 1024; DMAQ(sb, j * 1024, v, srdB, soB); }
      };
      issue(0); issue(1); issue(2);
      if (p == 0) STAMPW(3, 5);
      for (int s = 0; s < 15; ++s) { WAITV(2 * NQ); BAR(); issue(s + 3); }
      WAITV(2 * NQ); BAR(); WAITV(NQ); BAR(); WAITV(0); BAR();
    }
    BAR();
    BAR();
    if (p == 0) STAMPW(5, 0);
    {
      const __amdgpu_buffer_rsrc_t srdA = mksrd(wsp + (size_t)tm * 128 * S_, 128 * S_ * 2), srdB = mksrd((const u16*)(ws + WS_Z2N) + (size_t)tn * 144 * S_, 144 * S_ * 2);
      const unsigned va0 = (p * 64 + (lane >> 3)) * (S_ * 2) + sw0, va1 = (p * 64 + (lane >> 3)) * (S_ * 2) + sw1;
      const unsigned vb0 = (p * 72 + (lane >> 3)) * (S_ * 2) + (p ? sw1 : sw0), vb1 = (p * 72 + (lane >> 3)) * (S_ * 2) + (p ? sw0 : sw1);
      auto issueA = [&](int kt) {
        const unsigned sa = mA + (kt & 3) * MST, sk = kt * 128;
#pragma unroll
        for (int j = 0; j < 8; ++j) { const unsigned v = ((j & 1) ? va1 : va0) + j * 8 * (S_ * 2); DMAQ(sa, j * 1024, v, srdA, sk); }
      };
      auto issueB = [&](int kt) {
        const unsigned sb = mB + (kt & 3) * MST, sk = kt * 128;
#pragma unroll
        for (int j = 0; j < 9; ++j) { const unsigned v = ((j & 1) ? vb1 : vb0) + j * 8 * (S_ * 2); DMAQ_SC1(sb, j * 1024, v, srdB, sk); }
      };
      auto issue = [&](int kt) { issueA(kt); issueB(kt); };
      issueA(0); issueA(1); issueA(2);
      if (lane == 0) spin_until(cnt2, 16u);
      asm volatile("" ::: "memory");
      if (p == 0) STAMPW(5, 1);
      if (p == 0) STAMPW(5, 2);
      issueB(0); issueB(1); issueB(2);
      if (p == 0) STAMPW(5, 3);
      WAITV(18); BAR(); issue(3);
      WAITV(9 + NQ); BAR(); issue(4);
      for (int kt = 2; kt < 29; ++kt) { WAITV(2 * NQ); BAR(); issue(kt + 3); }
      WAITV(2 * NQ); BAR(); WAITV(NQ); BAR(); WAITV(0); BAR();
    }
    BAR();
    BAR();
    {
      const __amdgpu_buffer_rsrc_t srdA = mksrd((const u16*)(ws + WS_U) + (size_t)drow0 * FH, 64 * FH * 2), srdB = mksrd(ws + WS_WOUT + (size_t)dtn * 36 * WR_TB, 36 * WR_TB);
      const unsigned mA2 = ldsb + p * 4096, mB2 = ldsb + CD::ABYTES + p * 5120;
      const unsigned va0 = (p * 32 + (lane >> 3)) * (FH * 2) + sw0, va1 = (p * 32 + (lane >> 3)) * (FH * 2) + sw1, vb = p * 5120 + lane * 16;
      auto issueA = [&](int kt) {
        const unsigned sa = mA2 + (kt & 7) * CD::STAGE, sk = kt * 128;
#pragma unroll
        for (int j = 0; j < 4; ++j) { const unsigned v = ((j & 1) ? va1 : va0) + j * 8 * (FH * 2); DMAQ_SC1(sa, j * 1024, v, srdA, sk); }
      };
      auto issueB = [&](int kt) {
        const unsigned sb = mB2 + (kt & 7) * CD::STAGE, sk = kt * WR_TB;
#pragma unroll
        for (int j = 0; j < 5; ++j) { const unsigned v = vb + j * 1024; DMAQ(sb, j * 1024, v, srdB, sk); }
      };
#pragma unroll
      for (int s = 0; s < 7; ++s) issueB(s);
      if (lane == 0) spin_until(cnt3, 16u);
      asm volatile("" ::: "memory");
#pragma unroll
      for (int s = 0; s < 7; ++s) issueA(s);
      WAITV(24); BAR(); issueA(7); issueB(7);
      WAITV(29); BAR(); issueA(8); issueB(8);
      WAITV(34); BAR(); issueA(9); issueB(9);
      WAITV(39); BAR(); issueA(10); issueB(10);
      WAITV(44); BAR(); issueA(11); issueB(11);
      WAITV(49); BAR(); issueA(12); issueB(12);
      for (int kt = 6; kt < 29; ++kt) { WAITV(54); BAR(); issueA(kt + 7); issueB(kt + 7); }
      WAITV(54); BAR(); WAITV(45); BAR(); WAITV(36); BAR(); WAITV(27); BAR(); WAITV(18); BAR(); WAITV(9); BAR(); WAITV(0); BAR();
      BAR();
    }
    return;
  }
  if (w == 6) {
    const int lane = tid & 63;
    f32x2* rst = (f32x2*)(smem + M_RS);
#pragma unroll
    for (int i = 0; i < 2; ++i) {
      const f32x4* q = (const f32x4*)(ws + WS_HST + (size_t)(tm * 128 + 64 * i + lane) * 64);
      const f32x4 a = q[0], bb = q[1], c = q[2], d = q[3];
      const float s1 = (a[0] + a[2]) + (bb[0] + bb[2]) + (c[0] + c[2]) + (d[0] + d[2]), s2 = (a[1] + a[3]) + (bb[1] + bb[3]) + (c[1] + c[3]) + (d[1] + d[3]);
      rst[64 * i + lane] = ln_stats(s1, s2, 1.f / D_);
    }
    if (lane < 36) {
      *(f32x4*)(smem + M_CSA + lane * 16) = *(const f32x4*)(ws + WS_CS + (size_t)((tn + 16) * 144 + lane * 4) * 4);
      *(f32x4*)(smem + M_CSB + lane * 16) = *(const f32x4*)(ws + WS_CS + (size_t)(tn * 144 + lane * 4) * 4);
    }
    WAITL0();
    for (int s = 0; s < 15; ++s) BAR();
    if (lane == 0) spin_until(cnt1, 16u);
    asm volatile("" ::: "memory");
    const __amdgpu_buffer_rsrc_t srdS = mksrd(ws + WS_ST + (size_t)tm * (16 * 128 * 8), 16 * 128 * 8);
    u32x4 pv[16];
#pragma unroll
    for (int q = 0; q < 16; ++q) pv[q] = __builtin_amdgcn_raw_buffer_load_b128(srdS, (q * 128 + 2 * lane) * 8, 0, 16);
    SB(); BAR(); BAR(); SB();
    f32x4 t4 = {0.f, 0.f, 0.f, 0.f};
#pragma unroll
    for (int q = 0; q < 16; ++q) t4 += __builtin_bit_cast(f32x4, pv[q]);
    {
      const f32x2 q0 = ln_stats(t4[0], t4[1], 1.f / FH), q1 = ln_stats(t4[2], t4[3], 1.f / FH);
      *(f32x4*)(smem + M_S2 + lane * 16) = f32x4{q0[1], -q0[0] * q0[1], q1[1], -q1[0] * q1[1]};
    }
    WAITL0();
    BAR();
    BAR();
    return;
  }
  const int lane = tid & 63, g = lane >> 4, fr = lane & 15, f = fr >> 1;
  const int l0 = fr * 128 + ((g ^ f) << 4), l1 = fr * 128 + (((4 + g) ^ f) << 4);
  const int tb = 16384 + (16 * (g >> 1) + 4 * (g & 1) + (fr >> 2)) * 288 + (lane & 3) * 8;
  const int tok0 = tm * 128 + w * 32;
  f32x4 acc[2][9];
  bf16x8 a0[2], b0[9], a1[2], b1[9];
  auto zero_acc = [&]() {
#pragma unroll
    for (int i = 0; i < 2; ++i)
#pragma unroll
      for (int j = 0; j < 9; ++j) acc[i][j] = f32x4{0.f, 0.f, 0.f, 0.f};
  };
  auto mm = [&](bf16x8* a, bf16x8* bb) {
#pragma unroll
    for (int j = 0; j < 9; ++j)
#pragma unroll
      for (int i = 0; i < 2; ++i) acc[i][j] = MFMA16(bb[j], a[i], acc[i][j]);
  };
  auto rdKN = [&](bf16x8* a, bf16x8* bb, int s, int ks) {
    const char* sb = smem + (s & 3) * MST;
#pragma unroll
    for (int i = 0; i < 2; ++i) a[i] = *(const bf16x8*)(sb + w * 4096 + i * 2048 + (ks ? l1 : l0));
#pragma unroll
    for (int j = 0; j < 9; ++j) { const char* q = sb + tb + ks * (32 * 288) + j * 32; bb[j] = cat8(trread(q), trread(q + 8 * 288)); }
  };
  auto rdNK = [&](bf16x8* a, bf16x8* bb, int s, int ks) {
    const char* sb = smem + (s & 3) * MST + (ks ? l1 : l0);
#pragma unroll
    for (int i = 0; i < 2; ++i) a[i] = *(const bf16x8*)(sb + w * 4096 + i * 2048);
#pragma unroll
    for (int j = 0; j < 9; ++j) bb[j] = *(const bf16x8*)(sb + 16384 + j * 2048);
  };
#define SCHED(id, ND, RPG) _Pragma("unroll") for (int q = 0; q < 18; ++q) { __builtin_amdgcn_sched_group_barrier(0x008, 1, id); \
    if (q * RPG < ND) __builtin_amdgcn_sched_group_barrier(0x080, RPG, id); }
  auto gelu = [&](u32x2 (&zp)[2][9], int csoff, f32x2 (&sum)[2]) {
    const float c1 = -2.f * 0.7978845608028654f * 1.4426950408889634f, c2 = c1 * 0.044715f;
    const float* cs = (const float*)(smem + csoff);
    const f32x2* rst = (const f32x2*)(smem + M_RS) + w * 32;
    float nmu[2], k1[2], k3[2], irs[2];
    f32x2 s1[2], s2[2];
#pragma unroll
    for (int i = 0; i < 2; ++i) {
      const f32x2 st = rst[16 * i + fr];
      nmu[i] = -st[0]; k1[i] = c1 * st[1]; k3[i] = c2 * st[1] * st[1] * st[1]; irs[i] = __builtin_amdgcn_rcpf(st[1]);
      s1[i] = f32x2{0.f, 0.f}; s2[i] = f32x2{0.f, 0.f};
    }
    f32x4 c4 = *(const f32x4*)(cs + 4 * g);
#pragma unroll
    for (int j = 0; j < 9; ++j) {
      const f32x4 c4n = *(const f32x4*)(cs + 16 * (j < 8 ? j + 1 : 8) + 4 * g);
      SB();
#pragma unroll
      for (int i = 0; i < 2; ++i)
#pragma unroll
        for (int hh = 0; hh < 2; ++hh) {
          const f32x2 a2 = {acc[i][j][2 * hh], acc[i][j][2 * hh + 1]}, c2v = {c4[2 * hh], c4[2 * hh + 1]};
          const f32x2 t = f32x2{nmu[i], nmu[i]} * c2v + a2;
          const f32x2 y = t * (t * t * f32x2{k3[i], k3[i]} + f32x2{k1[i], k1[i]});
          const f32x2 e = {__builtin_amdgcn_exp2f(y[0]), __builtin_amdgcn_exp2f(y[1])};
          const f32x2 d = e * f32x2{irs[i], irs[i]} + f32x2{irs[i], irs[i]};
          const f32x2 z = t * f32x2{__builtin_amdgcn_rcpf(d[0]), __builtin_amdgcn_rcpf(d[1])};
          s1[i] += z; s2[i] += z * z;
          zp[i][j][hh] = cvtpk(z[0], z[1]);
        }
      VREG(zp[0][j]); VREG(zp[1][j]);
      c4 = c4n;
      SB();
    }
#pragma unroll
    for (int i = 0; i < 2; ++i) sum[i] = f32x2{s1[i][0] + s1[i][1], s2[i][0] + s2[i][1]};
  };
  const __amdgpu_buffer_rsrc_t srdS = mksrd(ws + WS_ST + (size_t)tm * (16 * 128 * 8), 16 * 128 * 8);
  zero_acc();
  STAMP(3, 0);
  BAR();
  STAMP(3, 1);
  rdKN(a0, b0, 0, 0);
  u32x2 z2p[2][9];
  auto stageAB = [&](int s) {
    SB(); rdKN(a1, b1, s, 1); mm(a0, b0); SCHED(0, 20, 2) SB();
    WAITL0(); BAR();
    SB(); rdKN(a0, b0, s + 1, 0); mm(a1, b1); SCHED(1, 20, 2)
  };
  for (int s = 0; s < 9; ++s) stageAB(s);
  MFMA_DRAIN();
  STAMP(5, 6);
  {
    f32x2 sum[2];
    gelu(z2p, M_CSA, sum);
#pragma unroll
    for (int i = 0; i < 2; ++i) {
      f32x2 v = sum[i];
      v[0] += xor16(v[0]); v[0] += xor32(v[0]); v[1] += xor16(v[1]); v[1] += xor32(v[1]);
      if (g == 0) __builtin_amdgcn_raw_buffer_store_b64(__builtin_bit_cast(u32x2, v), srdS, (tn * 128 + w * 32 + 16 * i + fr) * 8, 0, 16);
    }
    asm volatile("s_waitcnt vmcnt(0)" ::: "memory");
    zero_acc();
  }
  SB();
  STAMP(5, 4);
  stageAB(9);
  if (w == 0 && lane == 0) __hip_atomic_fetch_add(cnt1, 1u, __ATOMIC_RELAXED, __HIP_MEMORY_SCOPE_AGENT);
  for (int s = 10; s < 17; ++s) stageAB(s);
  SB(); rdKN(a1, b1, 17, 1); mm(a0, b0); mm(a1, b1); MFMA_DRAIN();
  WAITL0(); BAR();
  STAMP(3, 2);
  {
    const f32x2* st2 = (const f32x2*)(smem + M_S2) + w * 32;
    char* reg = smem + w * (32 * 304);
#pragma unroll
    for (int i = 0; i < 2; ++i) {
      const f32x2 st = st2[16 * i + fr];
#pragma unroll
      for (int j = 0; j < 9; ++j) {
        u32x2 v;
        v[0] = cvtpk(fmaf(bflo(z2p[i][j][0]), st[0], st[1]), fmaf(bfhi(z2p[i][j][0]), st[0], st[1]));
        v[1] = cvtpk(fmaf(bflo(z2p[i][j][1]), st[0], st[1]), fmaf(bfhi(z2p[i][j][1]), st[0], st[1]));
        *(u32x2*)(reg + (16 * i + fr) * 304 + (16 * j + 4 * g) * 2) = v;
      }
    }
    WAITL0();
    const __amdgpu_buffer_rsrc_t so = mksrd((u16*)(ws + WS_Z2N) + (size_t)(tn * 144) * S_ + tok0, 144 * S_ * 2);
    const char* rp = reg + (8 * g + (fr >> 2)) * 304 + (lane & 3) * 8;
#pragma unroll
    for (int j = 0; j < 9; ++j)
      __builtin_amdgcn_raw_buffer_store_b128(__builtin_bit_cast(u32x4, cat8(trread(rp + j * 32), trread(rp + 4 * 304 + j * 32))), so, (j * 16 + fr) * (S_ * 2) + 8 * g * 2, 0, 16);
    asm volatile("s_waitcnt vmcnt(0)" ::: "memory");
    WAITL0(); BAR();
    if (w == 0 && lane == 0) __hip_atomic_fetch_add(cnt2, 1u, __ATOMIC_RELAXED, __HIP_MEMORY_SCOPE_AGENT);
  }
  u32x2 z1p[2][9];
  { f32x2 sum[2]; gelu(z1p, M_CSB, sum); }
  const unsigned bias0 = bsp[tok0 + fr], bias1 = bsp[tok0 + 16 + fr];
  zero_acc();
  STAMP(4, 0);
  BAR();
  STAMP(4, 1);
  rdNK(a0, b0, 0, 0);
  for (int kt = 0; kt < 31; ++kt) {
    SB(); rdNK(a1, b1, kt, 1); mm(a0, b0); SCHED(0, 11, 1) SB();
    WAITL0(); BAR();
    SB(); rdNK(a0, b0, kt + 1, 0); mm(a1, b1); SCHED(1, 11, 1)
  }
  SB(); rdNK(a1, b1, 31, 1); mm(a0, b0); mm(a1, b1); MFMA_DRAIN();
  WAITL0(); BAR();
  SB();
  STAMP(4, 2);
#undef SCHED
  char* reg = smem + w * (32 * 304);
#pragma unroll
  for (int i = 0; i < 2; ++i) {
    const float bi = bflo(i ? bias1 : bias0);
#pragma unroll
    for (int j = 0; j < 9; ++j) {
      u32x2 v;
      const unsigned e0 = cvtpk(acc[i][j][0], acc[i][j][1]), e1 = cvtpk(acc[i][j][2], acc[i][j][3]);
      const unsigned g0 = cvtpk(bflo(e0) + bi, bfhi(e0) + bi), g1 = cvtpk(bflo(e1) + bi, bfhi(e1) + bi);
      v[0] = cvtpk(bflo(z1p[i][j][0]) * bflo(g0), bfhi(z1p[i][j][0]) * bfhi(g0));
      v[1] = cvtpk(bflo(z1p[i][j][1]) * bflo(g1), bfhi(z1p[i][j][1]) * bfhi(g1));
      *(u32x2*)(reg + (16 * i + fr) * 304 + (16 * j + 4 * g) * 2) = v;
    }
  }
  WAITL0();
  const __amdgpu_buffer_rsrc_t so = mksrd((u16*)(ws + WS_U) + (size_t)tok0 * FH + tn * 144, 32 * FH * 2);
#pragma unroll
  for (int i = 0; i < 9; ++i) {
    const int c = i * 64 + lane, row = c / 18, ch = c - row * 18;
    __builtin_amdgcn_raw_buffer_store_b128(*(const u32x4*)(reg + row * 304 + ch * 16), so, row * (FH * 2) + ch * 16, 0, 16);
  }
  asm volatile("s_waitcnt vmcnt(0)" ::: "memory");
  WAITL0(); BAR();
  STAMP(4, 3);
  if (w == 0 && lane == 0) __hip_atomic_fetch_add(cnt3, 1u, __ATOMIC_RELAXED, __HIP_MEMORY_SCOPE_AGENT);
  {
    const int tok = drow0 + w * 16 + fr;
    const size_t ro = (size_t)tok * D_ + dtn * 72 + 4 * g;
    const u16* res = (const u16*)(ws + WS_H);
    u32x2 r[5];
#pragma unroll
    for (int j = 0; j < 5; ++j) r[j] = *(const u32x2*)(res + ro + ((j < 4 || g < 2) ? 16 * j : 0));
    f32x4 acd[1][5]; float rs[1], rq[1];
    consumer<CD, 1, false>(smem, w * 16, lane, acd, rs, rq, ws, 6);
#pragma unroll
    for (int j = 0; j < 5; ++j) {
      if (j < 4 || g < 2) {
        const unsigned p0 = cvtpk(acd[0][j][0], acd[0][j][1]), p1 = cvtpk(acd[0][j][2], acd[0][j][3]);
        u32x2 v;
        v[0] = cvtpk(bflo(r[j][0]) + bflo(p0), bfhi(r[j][0]) + bfhi(p0));
        v[1] = cvtpk(bflo(r[j][1]) + bflo(p1), bfhi(r[j][1]) + bfhi(p1));
        *(u32x2*)(outp + ro + 16 * j) = v;
      }
    }
  }
}

extern "C" void kernel_launch(void* const* d_in, const int* in_sizes, int n_in, void* d_out, int out_size, void* d_ws, size_t ws_size, hipStream_t stream) {
  const u16* x = (const u16*)d_in[0];
  const u16* wqkv = (const u16*)d_in[1];
  const u16* wo = (const u16*)d_in[2];
  const u16* win = (const u16*)d_in[4];
  const u16* wsp = (const u16*)d_in[5];
  const u16* bsp = (const u16*)d_in[6];
  const u16* wout = (const u16*)d_in[7];
  char* ws = (char*)d_ws;
  (void)hipFuncSetAttribute((const void*)k_qkv, hipFuncAttributeMaxDynamicSharedMemorySize, C1::LDS);
  (void)hipFuncSetAttribute((const void*)k_attn, hipFuncAttributeMaxDynamicSharedMemorySize, ATT_LDS);
  (void)hipFuncSetAttribute((const void*)k_mix, hipFuncAttributeMaxDynamicSharedMemorySize, M_LDS);
  hipLaunchKernelGGL(k_qkv, dim3(256), dim3(512), C1::LDS, stream, x, wqkv, win, wout, wo, ws);
  hipLaunchKernelGGL(k_attn, dim3(256), dim3(512), ATT_LDS, stream, ws, wsp, x, (const char*)d_out);
  hipLaunchKernelGGL(k_mix, dim3(256), dim3(448), M_LDS, stream, wsp, bsp, ws, (u16*)d_out);
}
```

```cpp
#include <hip/hip_runtime.h>
#include <stdint.h>

typedef unsigned short u16;
typedef short bf16x8 __attribute__((ext_vector_type(8)));
typedef short s16x4 __attribute__((ext_vector_type(4)));
typedef float f32x2 __attribute__((ext_vector_type(2)));
typedef float f32x4 __attribute__((ext_vector_type(4)));
typedef float f32x16 __attribute__((ext_vector_type(16)));
typedef unsigned u32x2 __attribute__((ext_vector_type(2)));
typedef unsigned u32x4 __attribute__((ext_vector_type(4)));
typedef __bf16 bf16x2_t __attribute__((ext_vector_type(2)));
typedef __attribute__((address_space(3))) s16x4* lds_s16x4_ptr;
#define DI __device__ __forceinline__

constexpr int S_ = 2048, D_ = 576, H_ = 32, DH = 18, FH = 2304, F2 = 4608;
constexpr int QK_TILE = 1152;
constexpr int V_OFF = 1152;
constexpr int KV_TILE = 1152 + 4 * 304;
constexpr int KV_HEAD = 64 * KV_TILE;
constexpr int Q_HEAD = 64 * QK_TILE;
constexpr int WIN_TB = 64 * 288, WR_TB = 64 * 160;
constexpr size_t WS_Q = 0;
constexpr size_t WS_KV = WS_Q + (size_t)H_ * Q_HEAD;
constexpr size_t WS_AO = WS_KV + (size_t)H_ * KV_HEAD + 8192;
constexpr size_t WS_H = WS_AO + (size_t)S_ * D_ * 2;
constexpr size_t WS_Z2N = WS_H + (size_t)S_ * D_ * 2;
constexpr size_t WS_ST = WS_Z2N + (size_t)S_ * FH * 2;
constexpr size_t WS_CNT = WS_ST + (size_t)S_ * 16 * 2 * 4;
constexpr size_t WS_HST = WS_CNT + 8192;
constexpr size_t WS_U0 = WS_HST + (size_t)S_ * 8 * 2 * 4;
constexpr size_t WS_U = 0;
static_assert((size_t)S_ * FH * 2 <= WS_H, "u must fit below h");
constexpr size_t WS_WIN = WS_U0;
constexpr size_t WS_WOUT = WS_WIN + (size_t)32 * 9 * WIN_TB;
constexpr size_t WS_WO = WS_WOUT + (size_t)8 * 36 * WR_TB;
constexpr size_t WS_CS = WS_WO + (size_t)8 * 9 * WR_TB;
constexpr size_t WS_END = WS_CS + (size_t)F2 * 4;

DI unsigned cvtpk(float lo, float hi) { f32x2 v = {lo, hi}; bf16x2_t b = __builtin_convertvector(v, bf16x2_t); return __builtin_bit_cast(unsigned, b); }
DI float bflo(unsigned u) { return __uint_as_float(u << 16); }
DI float bfhi(unsigned u) { return __uint_as_float(u & 0xffff0000u); }
DI float bfr(float x) { return __uint_as_float(cvtpk(x, x) << 16); }
DI f32x2 ln_stats(float s1, float s2, float inv_n) {
  const float mu = s1 * inv_n, var = fmaxf(s2 * inv_n - mu * mu, 0.f);
  return f32x2{bfr(mu), 1.f / bfr(sqrtf(bfr(bfr(var) + 1e-5f)))};
}
DI unsigned rfl(unsigned x) { return __builtin_amdgcn_readfirstlane(x); }
DI float xor16(float v) { return __shfl_xor(v, 16); }
DI float xor32(float v) { return __shfl_xor(v, 32); }
#define DMAQ(sb, imm, voff, srd, soff) asm volatile("s_add_u32 m0, %0, %1\n\ts_nop 0\n\tbuffer_load_dwordx4 %2, %3, %4 offen lds" :: "s"(sb), "i"(imm), "v"(voff), "s"(srd), "s"(soff) : "scc")
#define DMAQ_SC1(sb, imm, voff, srd, soff) asm volatile("s_add_u32 m0, %0, %1\n\ts_nop 0\n\tbuffer_load_dwordx4 %2, %3, %4 offen sc1 lds" :: "s"(sb), "i"(imm), "v"(voff), "s"(srd), "s"(soff) : "scc")
#define WAITV(n) asm volatile("s_waitcnt vmcnt(%0)" :: "i"(n) : "memory")
#define WAITL0() asm volatile("s_waitcnt lgkmcnt(0)" ::: "memory")
#define BAR() asm volatile("s_barrier" ::: "memory")
#define SB() __builtin_amdgcn_sched_barrier(0)
#define MFMA16(a, b, c) __builtin_amdgcn_mfma_f32_16x16x32_bf16((a), (b), (c), 0, 0, 0)
#define MFMA32(a, b, c) __builtin_amdgcn_mfma_f32_32x32x16_bf16((a), (b), (c), 0, 0, 0)
#define MFMA_DRAIN() do { SB(); asm volatile("s_nop 7\n\ts_nop 7\n\ts_nop 7"); SB(); } while (0)
#define VREG(x) asm volatile("" : "+v"(x))
DI __amdgpu_buffer_rsrc_t mksrd(const void* p, unsigned bytes) { return __builtin_amdgcn_make_buffer_rsrc((void*)p, (short)0, (int)bytes, 0x00020000); }
DI s16x4 trread(const char* p) { return __builtin_amdgcn_ds_read_tr16_b64_v4i16((lds_s16x4_ptr)(p)); }
DI bf16x8 cat8(s16x4 lo, s16x4 hi) { return __builtin_shufflevector(lo, hi, 0, 1, 2, 3, 4, 5, 6, 7); }
DI bf16x8 ones8() { const u32x4 o = {0x3F803F80u, 0x3F803F80u, 0x3F803F80u, 0x3F803F80u}; return __builtin_bit_cast(bf16x8, o); }
#define ROT1 (3 * (((idx >> 2) + (idx & 3)) % 3))
#define ST_AUX 16
DI void st16(__amdgpu_buffer_rsrc_t srd, unsigned off, u32x4 v) { __builtin_amdgcn_raw_buffer_store_b128(v, srd, off, 0, ST_AUX); }
#define TOUCH(dummy, ptr) asm volatile("global_load_dword %0, %1, off" : "+v"(dummy) : "v"(ptr))
#define STAMP(kid, slot)
#define STAMPW(kid, slot)
#define STAMP_END(kid)

enum { B_NK = 0, B_KN = 1, B_TILED = 2 };
template <int BM_, int NT_, int NST_, int BMODE_, int KT_>
struct Cfg {
  static constexpr int BM = BM_, NT = NT_, NST = NST_, KT = KT_, BMODE = BMODE_;
  static constexpr bool BKN = BMODE_ != B_NK;
  static constexpr int BN = NT * 16;
  static constexpr int NA = BM / 8, NB = BN / 8;
  static constexpr int ABYTES = BM * 128, BBYTES = BN * 128, STAGE = ABYTES + BBYTES;
  static constexpr int RING = NST * STAGE;
  static constexpr int CS_OFF = RING;
  static constexpr int LDS = RING + BN * 4;
};

template <class C>
DI void run_producer(char* smem, const void* Ab, unsigned abytes, const void* Bb, unsigned bbytes, int lda, int ldb, int p, int lane, char* ws_dbg, int kid, int rot) {
  if (p == 0) STAMPW(kid, 4);
  constexpr int HA = C::NA / 2, HB = C::NB / 2, NQ = HA + HB;
  static_assert(C::NA % 4 == 0 && C::NB % 2 == 0 && C::BMODE != B_NK, "piece split");
  const __amdgpu_buffer_rsrc_t srdA = mksrd(Ab, abytes), srdB = mksrd(Bb, bbytes);
  const unsigned ldsb = (unsigned)(size_t)smem;
  const unsigned mA = ldsb + p * (HA * 1024), mB = ldsb + C::ABYTES + p * (HB * 1024);
  const unsigned sw0 = ((lane & 7) ^ (lane >> 4)) << 4, sw1 = ((lane & 7) ^ (4 + (lane >> 4))) << 4;
  const unsigned va0 = (p * HA * 8 + (lane >> 3)) * (lda * 2) + sw0, va1 = (p * HA * 8 + (lane >> 3)) * (lda * 2) + sw1;
  unsigned vb[HB];
#pragma unroll
  for (int j = 0; j < HB; ++j) {
    if (C::BMODE == B_KN) {
      constexpr int CPR = C::BN / 8;
      const int c = 64 * (p * HB + j) + lane, rho = c / CPR, ch = c - rho * CPR;
      const int k = (rho & ~12) | ((rho & 4) << 1) | ((rho & 8) >> 1);
      vb[j] = k * (ldb * 2) + ch * 16;
    } else {
      vb[j] = (p * HB + j) * 1024 + lane * 16;
    }
  }
  auto issue = [&](int kt) {
    const unsigned so = (kt % C::NST) * C::STAGE, sa = mA + so, sb = mB + so;
    int kr = kt + rot; if (kr >= C::KT) kr -= C::KT;
    const unsigned soA = kr * 128, soB = C::BMODE == B_KN ? kr * 64 * ldb * 2 : kr * C::BBYTES;
#pragma unroll
    for (int j = 0; j < HA; ++j) { const unsigned v = ((j & 1) ? va1 : va0) + j * 8 * (lda * 2); DMAQ(sa, j * 1024, v, srdA, soA); }
#pragma unroll
    for (int j = 0; j < HB; ++j) DMAQ(sb, j * 1024, vb[j], srdB, soB);
  };
  constexpr int PF = C::NST - 1;
#pragma unroll
  for (int s = 0; s < PF; ++s) issue(s);
  if (p == 0) STAMPW(kid, 5);
  for (int kt = 0; kt < C::KT - PF; ++kt) { WAITV((PF - 1) * NQ); BAR(); issue(kt + PF); }
#pragma unroll
  for (int r = PF - 1; r >= 0; --r) { WAITV(r * NQ); BAR(); }
  BAR();
}

template <class C, int MI, bool ROWSTAT>
DI void consumer(char* smem, int row0, int lane, f32x4 (&acc)[MI][C::NT], float (&rs)[MI], float (&rq)[MI], char* ws_dbg, int kid) {
  constexpr int NT = C::NT;
  const int g = lane >> 4, fr = lane & 15, f = fr >> 1;
  const int la0 = row0 * 128 + fr * 128 + ((g ^ f) << 4), la1 = row0 * 128 + fr * 128 + (((4 + g) ^ f) << 4);
  const int tb = C::ABYTES + (16 * (g >> 1) + 4 * (g & 1) + (fr >> 2)) * (C::BN * 2) + (lane & 3) * 8;
  f32x4 ars[MI], arq[MI];
#pragma unroll
  for (int i = 0; i < MI; ++i) {
    ars[i] = f32x4{0.f, 0.f, 0.f, 0.f}; arq[i] = f32x4{0.f, 0.f, 0.f, 0.f};
#pragma unroll
    for (int j = 0; j < NT; ++j) acc[i][j] = f32x4{0.f, 0.f, 0.f, 0.f};
  }
  bf16x8 a0[MI], b0[NT], a1[MI], b1[NT];
  auto rd = [&](bf16x8* a, bf16x8* bb, int kt, int ks) {
    const char* sb = smem + (kt % C::NST) * C::STAGE;
#pragma unroll
    for (int i = 0; i < MI; ++i) a[i] = *(const bf16x8*)(sb + (ks ? la1 : la0) + i * 2048);
#pragma unroll
    for (int j = 0; j < NT; ++j) { const char* p = sb + tb + ks * (32 * C::BN * 2) + j * 32; bb[j] = cat8(trread(p), trread(p + 8 * C::BN * 2)); }
  };
  auto mm = [&](bf16x8* a, bf16x8* bb) {
#pragma unroll
    for (int j = 0; j < NT; ++j)
#pragma unroll
      for (int i = 0; i < MI; ++i) acc[i][j] = MFMA16(bb[j], a[i], acc[i][j]);
    if constexpr (ROWSTAT) {
#pragma unroll
      for (int i = 0; i < MI; ++i) { ars[i] = MFMA16(ones8(), a[i], ars[i]); arq[i] = MFMA16(a[i], a[i], arq[i]); }
    }
  };
  constexpr int NM = MI * NT + (ROWSTAT ? 2 * MI : 0), ND = MI + NT * 2, RPG = (ND + (NM * 2 / 3) - 1) / (NM * 2 / 3);
#define SCHED(id) _Pragma("unroll") for (int q = 0; q < NM; ++q) { __builtin_amdgcn_sched_group_barrier(0x008, 1, id); \
    if (q * RPG < ND) __builtin_amdgcn_sched_group_barrier(0x080, RPG, id); }
  STAMP(kid, 0);
  BAR();
  STAMP(kid, 1);
  rd(a0, b0, 0, 0);
  for (int kt = 0; kt < C::KT - 1; ++kt) {
    SB();
    rd(a1, b1, kt, 1);
    mm(a0, b0);
    SCHED(0)
    SB();
    WAITL0(); BAR();
    SB();
    rd(a0, b0, kt + 1, 0);
    mm(a1, b1);
    SCHED(1)
  }
  SB();
  rd(a1, b1, C::KT - 1, 1);
  mm(a0, b0);
  mm(a1, b1);
#undef SCHED
  MFMA_DRAIN();
  if constexpr (ROWSTAT) {
#pragma unroll
    for (int i = 0; i < MI; ++i) {
      rs[i] = ars[i][0];
      const float d = (fr & 2) ? ((fr & 1) ? arq[i][3] : arq[i][2]) : ((fr & 1) ? arq[i][1] : arq[i][0]);
      rq[i] = __shfl(d, fr + 16 * (fr >> 2));
    }
  }
  WAITL0(); BAR();
  SB();
  STAMP(kid, 2);
}

using C1 = Cfg<128, 9, 4, B_KN, 9>;
constexpr int RU1 = 32 * 9 * 18, RU2 = RU1 + 8 * 36 * 10, RU3 = RU2 + 8 * 9 * 10;
DI const u16* retile_src(const u16* __restrict__ win, const u16* __restrict__ wout, const u16* __restrict__ wo, int u, int lane) {
  if (u < RU1) {
    const int tile = u / 18, q = u - tile * 18, tn = tile / 9, kt = tile - tn * 9;
    const int c = 64 * q + lane, rho = c / 18, ch = c - rho * 18;
    const int k = (rho & ~12) | ((rho & 4) << 1) | ((rho & 8) >> 1);
    return win + (size_t)(kt * 64 + k) * F2 + tn * 144 + ch * 8;
  }
  const bool o = u < RU2; const int v = u - (o ? RU1 : RU2), KT = o ? 36 : 9;
  const int tile = v / 10, q = v - tile * 10, tn = tile / KT, kt = tile - tn * KT;
  const int c = 64 * q + lane, rho = c / 10, ch = c - rho * 10;
  const int k = (rho & ~12) | ((rho & 4) << 1) | ((rho & 8) >> 1);
  int col = tn * 72 + ch * 8; if (col > D_ - 8) col = D_ - 8;
  int kr = kt * 64 + k;
  if (!o) kr = kr < 512 ? 18 * (kr >> 4) + (kr & 15) : 18 * ((kr - 512) >> 1) + 16 + (kr & 1);
  return (o ? wout : wo) + (size_t)kr * D_ + col;
}
__global__ __launch_bounds__(512, 2) void k_qkv(const u16* __restrict__ x, const u16* __restrict__ wqkv, const u16* __restrict__ win, const u16* __restrict__ wout,
                                                const u16* __restrict__ wo, char* __restrict__ ws) {
  extern __shared__ __attribute__((aligned(16))) char smem[];
  const int tid = threadIdx.x, w = rfl(tid >> 6);
  const int b = blockIdx.x, xcd = b & 7, idx = b >> 3;
  char* ws_dbg = ws;
  if (b == 255 && tid < 56) *(unsigned*)(ws + WS_CNT + tid * 128) = 0u;
  if (idx >= 24) {
    STAMP(0, 0); STAMP(0, 1); STAMP(0, 2);
    const int pw = (idx - 24) * 8 + xcd, lane = tid & 63, gw = pw * 8 + w;
    constexpr int NU = (RU3 + 511) / 512;
    u32x4 v[NU];
#pragma unroll
    for (int i = 0; i < NU; ++i) { const int u = gw + 512 * i; v[i] = *(const u32x4*)retile_src(win, wout, wo, u < RU3 ? u : gw, lane); }
    const int cc = tid % 9, rg = tid / 9;
    u32x4 cv[11];
    if (rg < 56) {
#pragma unroll
      for (int i = 0; i < 11; ++i) { const int r = rg + 56 * i; cv[i] = *(const u32x4*)(win + (size_t)(r < D_ ? r : rg) * F2 + pw * 72 + cc * 8); }
    }
    const __amdgpu_buffer_rsrc_t so = mksrd(ws + WS_WIN, RU3 * 1024);
#pragma unroll
    for (int i = 0; i < NU; ++i) { const int u = gw + 512 * i; if (u < RU3) st16(so, u * 1024 + lane * 16, v[i]); }
    float* red = (float*)smem;
    if (rg < 56) {
      f32x4 sa = {0.f, 0.f, 0.f, 0.f}, sb = {0.f, 0.f, 0.f, 0.f};
#pragma unroll
      for (int i = 0; i < 11; ++i) if (rg + 56 * i < D_) {
        sa += f32x4{bflo(cv[i][0]), bfhi(cv[i][0]), bflo(cv[i][1]), bfhi(cv[i][1])};
        sb += f32x4{bflo(cv[i][2]), bfhi(cv[i][2]), bflo(cv[i][3]), bfhi(cv[i][3])};
      }
      *(f32x4*)(red + rg * 72 + cc * 8) = sa; *(f32x4*)(red + rg * 72 + cc * 8 + 4) = sb;
    }
    __syncthreads();
    if (tid < 288) {
      const int c = tid % 72, q = tid / 72;
      float t = 0.f;
#pragma unroll
      for (int r = 0; r < 14; ++r) t += red[(q * 14 + r) * 72 + c];
      red[56 * 72 + tid] = t;
    }
    __syncthreads();
    if (tid < 72) ((float*)(ws + WS_CS))[pw * 72 + tid] = (red[56 * 72 + tid] + red[56 * 72 + 72 + tid]) + (red[56 * 72 + 144 + tid] + red[56 * 72 + 216 + tid]);
    STAMP_END(0);
    return;
  }
  const int tm = (xcd & 3) * 4 + (idx & 3), tn = (xcd >> 2) * 6 + (idx >> 2);
  if (__builtin_expect(w == 4 || w == 5, 1)) {
    run_producer<C1>(smem, x + (size_t)tm * 128 * D_, 128 * D_ * 2, wqkv + tn * 144, (unsigned)((D_ * 3 * D_ - tn * 144) * 2), D_, 3 * D_, w - 4, tid & 63, ws, 0, ROT1);
    return;
  }
  if (w >= 6) {
    const int lane = tid & 63, p = w - 6, g = lane >> 4;
    f32x4 cs[5];
#pragma unroll
    for (int j = 0; j < 5; ++j) cs[j] = f32x4{0.f, 0.f, 0.f, 0.f};
    const int tb = C1::ABYTES + (16 * (g >> 1) + 4 * (g & 1) + ((lane & 15) >> 2)) * 288 + (lane & 3) * 8 + p * 32;
    for (int kt = 0; kt < 9; ++kt) {
      BAR();
      const char* sb = smem + (kt % C1::NST) * C1::STAGE + tb;
#pragma unroll
      for (int jj = 0; jj < 5; ++jj)
#pragma unroll
        for (int ks = 0; ks < 2; ++ks) {
          const char* q = sb + ks * (32 * 288) + jj * 64;
          cs[jj] = MFMA16(cat8(trread(q), trread(q + 8 * 288)), ones8(), cs[jj]);
        }
      WAITL0();
    }
    MFMA_DRAIN();
    float* st = (float*)(smem + C1::CS_OFF);
#pragma unroll
    for (int jj = 0; jj < 5; ++jj) if ((lane & 15) == 0 && p + 2 * jj < 9) *(f32x4*)(st + 16 * (p + 2 * jj) + 4 * g) = cs[jj];
    WAITL0(); BAR();
    return;
  }
  f32x4 acc[2][9]; float rs[2], rq[2];
  consumer<C1, 2, true>(smem, w * 32, tid & 63, acc, rs, rq, ws, 0);
  int tz = threadIdx.x; VREG(tz);
  const int lane = tz & 63, g = lane >> 4, fr = lane & 15;
  const float* cs = (const float*)(smem + C1::CS_OFF);
  const int kind = tn >> 2, head0 = (tn & 3) * 8;
  const int tile32 = tm * 4 + w;
  const float sc = kind == 0 ? 0.23570226039551584f * 1.4426950408889634f : 1.f;
  float mu[2], rstd[2];
#pragma unroll
  for (int i = 0; i < 2; ++i) {
    const f32x2 st = ln_stats(rs[i], rq[i], 1.f / D_);
    mu[i] = st[0]; rstd[i] = st[1] * sc;
  }
  constexpr int RP = 336;
  char* reg = smem + w * (32 * RP);
  f32x4 c4a[9];
#pragma unroll
  for (int j = 0; j < 9; ++j) c4a[j] = *(const f32x4*)(cs + 16 * j + 4 * g);
#pragma unroll
  for (int j = 0; j < 9; ++j) {
    const f32x4 c4 = c4a[j];
#pragma unroll
    for (int rr = 0; rr < 2; ++rr) {
      const int c = 16 * j + 4 * g + 2 * rr, off = c * 2 + (c / 18) * 4;
#pragma unroll
      for (int i = 0; i < 2; ++i)
        *(unsigned*)(reg + (16 * i + fr) * RP + off) = cvtpk((acc[i][j][2 * rr] - mu[i] * c4[2 * rr]) * rstd[i], (acc[i][j][2 * rr + 1] - mu[i] * c4[2 * rr + 1]) * rstd[i]);
    }
  }
  WAITL0();
  if (kind < 2) {
    char* base = ws + (kind == 0 ? WS_Q + (size_t)tile32 * QK_TILE + (size_t)head0 * Q_HEAD : WS_KV + (size_t)tile32 * KV_TILE + (size_t)head0 * KV_HEAD);
    const int hstride = kind == 0 ? Q_HEAD : KV_HEAD;
    const char* rp = reg + (lane & 31) * RP + (lane >> 5) * 16;
#pragma unroll
    for (int hl = 0; hl < 8; ++hl) {
      const u32x2 lo = *(const u32x2*)(rp + hl * 40), hi = *(const u32x2*)(rp + hl * 40 + 8);
      *(u32x4*)(base + (size_t)hl * hstride + lane * 16) = u32x4{lo[0], lo[1], hi[0], hi[1]};
    }
#pragma unroll
    for (int q = 0; q < 4; ++q) {
      const int hl = 2 * q + (lane >> 5);
      *(unsigned*)(base + (size_t)hl * hstride + 1024 + (lane & 31) * 4) = *(const unsigned*)(reg + (lane & 31) * RP + hl * 40 + 32);
    }
  } else {
    char* base = ws + WS_KV + (size_t)tile32 * KV_TILE + V_OFF + (size_t)head0 * KV_HEAD;
    const int s = g >> 1, h = g & 1;
    const char* rp = reg + (16 * s + 4 * h + (fr >> 2)) * RP + (lane & 3) * 8;
#pragma unroll
    for (int j = 0; j < 10; ++j) {
      const bf16x8 v = cat8(trread(rp + j * 32), trread(rp + 8 * RP + j * 32));
      const int c = 16 * j + fr, hl = c / 20, d = c - hl * 20;
      if (d < 18) *(bf16x8*)(base + (size_t)hl * KV_HEAD + s * 608 + h * 304 + d * 16) = v;
    }
    if (lane < 32) {
      *(bf16x8*)(base + (size_t)(lane >> 2) * KV_HEAD + (lane & 3) * 304 + 18 * 16) = ones8();
    }
  }
  STAMP_END(0);
}

DI void spin_until(const unsigned* cnt, unsigned target) {
  for (unsigned spins = 0; spins < (1u << 20); ++spins) {
    if (__hip_atomic_load(cnt, __ATOMIC_RELAXED, __HIP_MEMORY_SCOPE_AGENT) >= target) break;
    __builtin_amdgcn_s_sleep(1);
  }
}
template <int KT> using CR = Cfg<64, 5, 8, B_TILED, KT>;
constexpr int ATT_LDS = 152 * 1024;
__global__ __launch_bounds__(512, 2) void k_attn(char* __restrict__ ws, const u16* __restrict__ wsp, const u16* __restrict__ x) {
  extern __shared__ __attribute__((aligned(16))) char smem[];
  const int tid = threadIdx.x, lane = tid & 63, w = rfl(tid >> 6);
  const int b = blockIdx.x, xcd = b & 7, idx = b >> 3;
  const int head = xcd * 4 + (idx & 3), qb = idx >> 2;
  char* ws_dbg = ws; STAMP(1, 0);
  const int q31 = lane & 31, h = lane >> 5;
  const char* qp = ws + WS_Q + (size_t)head * Q_HEAD + (size_t)(qb * 8 + w) * QK_TILE;
  bf16x8 qf1; unsigned q2;
  asm volatile("global_load_dwordx4 %0, %1, off" : "=v"(qf1) : "v"(qp + lane * 16));
  asm volatile("global_load_dword %0, %1, off" : "=v"(q2) : "v"(qp + 1024 + q31 * 4));
  {
    __amdgpu_buffer_rsrc_t srd = mksrd(ws + WS_KV + (size_t)head * KV_HEAD, KV_HEAD);
    const unsigned ldsb = (unsigned)(size_t)smem + w * 1024, vo = lane * 16 + w * 1024;
#pragma unroll
    for (int j = 0; j < 19; ++j) DMAQ(ldsb, j * 8192, vo, srd, (unsigned)(j * 8192));
  }
  asm volatile("s_waitcnt vmcnt(16)" : "+v"(qf1), "+v"(q2) :: "memory");
  BAR();
  u32x4 q2v = {h == 0 ? q2 : 0u, 0u, 0u, 0u};
  const bf16x8 qf2 = __builtin_bit_cast(bf16x8, q2v);
  const int dcl = q31 < 19 ? q31 : (q31 >= 20 && q31 < 28 ? 12 : 16);
  const int koff = lane * 16, k2off = 1024 + q31 * 4, voff = V_OFF + h * 304 + dcl * 16;
  f32x16 o;
#pragma unroll
  for (int r = 0; r < 16; ++r) o[r] = 0.f;
  auto scores = [&](int t, const f32x16& c) {
    const char* tp = smem + t * KV_TILE;
    const bf16x8 k1 = *(const bf16x8*)(tp + koff);
    u32x4 k2v = {*(const unsigned*)(tp + k2off), 0u, 0u, 0u};
    f32x16 s = MFMA32(k1, qf1, c);
    return MFMA32(__builtin_bit_cast(bf16x8, k2v), qf2, s);
  };
  f32x16 zero;
#pragma unroll
  for (int r = 0; r < 16; ++r) zero[r] = 0.f;
  STAMP(1, 1);
  unsigned dummy_keep;
  f32x16 negm;
  {
    const f32x16 s = scores(0, zero);
    float m = s[0];
#pragma unroll
    for (int r = 1; r < 16; ++r) m = fmaxf(m, s[r]);
    m = fmaxf(m, xor32(m));
#pragma unroll
    for (int r = 0; r < 16; ++r) negm[r] = -m;
  }
  {
    struct Frag { bf16x8 k1, v0, v1; u32x4 k2; };
    Frag fA, fB;
    fA.k2 = u32x4{0u, 0u, 0u, 0u}; fB.k2 = u32x4{0u, 0u, 0u, 0u};
    f32x16 sA = scores(0, negm), sB;
    fA.k1 = *(const bf16x8*)(smem + KV_TILE + koff); fA.k2[0] = *(const unsigned*)(smem + KV_TILE + k2off);
    fA.v0 = *(const bf16x8*)(smem + voff); fA.v1 = *(const bf16x8*)(smem + voff + 608);
    sA[0] = __builtin_amdgcn_exp2f(sA[0]); sA[1] = __builtin_amdgcn_exp2f(sA[1]);
    auto step = [&](int t, f32x16& sc, f32x16& sn, Frag& fc, Frag& fn) {
      const char* tp = smem + t * KV_TILE;
      u32x4 p0, p1;
      fn.v1 = *(const bf16x8*)(tp + KV_TILE + voff + 608); SB();
      fn.v0 = *(const bf16x8*)(tp + KV_TILE + voff); SB();
      fn.k2[0] = *(const unsigned*)(tp + 2 * KV_TILE + k2off); SB();
      fn.k1 = *(const bf16x8*)(tp + 2 * KV_TILE + koff);
      SB();
      sn = MFMA32(fc.k1, qf1, negm);
#pragma unroll
      for (int e = 2; e < 5; ++e) sc[e] = __builtin_amdgcn_exp2f(sc[e]);
      SB();
      sn = MFMA32(__builtin_bit_cast(bf16x8, fc.k2), qf2, sn);
#pragma unroll
      for (int e = 5; e < 8; ++e) sc[e] = __builtin_amdgcn_exp2f(sc[e]);
#pragma unroll
      for (int e = 0; e < 4; ++e) p0[e] = cvtpk(sc[2 * e], sc[2 * e + 1]);
      SB();
      sc[8] = __builtin_amdgcn_exp2f(sc[8]); sc[9] = __builtin_amdgcn_exp2f(sc[9]);
      SB();
      o = MFMA32(fc.v0, __builtin_bit_cast(bf16x8, p0), o);
#pragma unroll
      for (int e = 10; e < 16; ++e) sc[e] = __builtin_amdgcn_exp2f(sc[e]);
#pragma unroll
      for (int e = 0; e < 4; ++e) p1[e] = cvtpk(sc[8 + 2 * e], sc[8 + 2 * e + 1]);
      SB();
      sn[0] = __builtin_amdgcn_exp2f(sn[0]); sn[1] = __builtin_amdgcn_exp2f(sn[1]);
      SB();
      o = MFMA32(fc.v1, __builtin_bit_cast(bf16x8, p1), o);
      SB();
    };
    unsigned dummy = 0;
    auto warm = [&]() { const unsigned T = b * 512 + tid; TOUCH(dummy, (const char*)wsp + (size_t)(T < 65536u ? T : 0u) * 128); };
#define CHUNK(c, nv) { if (c > 0) { WAITV(nv); BAR(); } if (c == 3) warm(); \
    for (int t = 8 * c; t < 8 * c + 8; t += 2) { step(t, sA, sB, fA, fB); step(t + 1, sB, sA, fB, fA); } }
    CHUNK(0, 16) CHUNK(1, 13) CHUNK(2, 11) CHUNK(3, 9) CHUNK(4, 7) CHUNK(5, 5) CHUNK(6, 3) CHUNK(7, 1)
#undef CHUNK
    VREG(dummy);
    dummy_keep = dummy;
    MFMA_DRAIN();
  }
  STAMP(1, 2);
  float l = __shfl(o[10], q31);
  bool ok = l < 1.8446744e19f;
#pragma unroll
  for (int r = 0; r < 12; ++r) ok = ok && (fabsf(o[r]) <= 3.0e38f);
  if (!__all(ok)) {
    float m = -3.0e38f;
#pragma unroll
    for (int r = 0; r < 16; ++r) o[r] = 0.f;
    for (int t = 0; t < 64; ++t) {
      f32x16 s = scores(t, zero);
      float tmax = s[0];
#pragma unroll
      for (int r = 1; r < 16; ++r) tmax = fmaxf(tmax, s[r]);
      tmax = fmaxf(tmax, xor32(tmax));
      const float mn = fmaxf(m, tmax), alpha = __builtin_amdgcn_exp2f(m - mn);
      m = mn;
#pragma unroll
      for (int r = 0; r < 16; ++r) { o[r] *= alpha; s[r] = __builtin_amdgcn_exp2f(s[r] - mn); }
      const char* tp = smem + t * KV_TILE + voff;
      const bf16x8 v0 = *(const bf16x8*)(tp), v1 = *(const bf16x8*)(tp + 608);
      u32x4 p0, p1;
#pragma unroll
      for (int e = 0; e < 4; ++e) { p0[e] = cvtpk(s[2 * e], s[2 * e + 1]); p1[e] = cvtpk(s[8 + 2 * e], s[8 + 2 * e + 1]); }
      o = MFMA32(v0, __builtin_bit_cast(bf16x8, p0), o);
      o = MFMA32(v1, __builtin_bit_cast(bf16x8, p1), o);
    }
    l = __shfl(o[10], q31);
  }
  const float inv = 1.f / l;
  {
    unsigned a0 = cvtpk(o[0] * inv, o[1] * inv), a1 = cvtpk(o[2] * inv, o[3] * inv), b0 = cvtpk(o[4] * inv, o[5] * inv), b1 = cvtpk(o[6] * inv, o[7] * inv);
    { auto r = __builtin_amdgcn_permlane32_swap(a0, b0, false, false); a0 = r[0]; b0 = r[1]; }
    { auto r = __builtin_amdgcn_permlane32_swap(a1, b1, false, false); a1 = r[0]; b1 = r[1]; }
    const __amdgpu_buffer_rsrc_t so = mksrd(ws + WS_AO + (size_t)(qb * 32 + head) * 8192, 8192);
    __builtin_amdgcn_raw_buffer_store_b128(u32x4{a0, a1, b0, b1}, so, (w * 32 + q31) * 32 + h * 16, 0, 16);
    const __amdgpu_buffer_rsrc_t s2 = mksrd(ws + WS_AO + (size_t)S_ * 1024, S_ * 128);
    if (h == 0) __builtin_amdgcn_raw_buffer_store_b32(cvtpk(o[8] * inv, o[9] * inv), s2, (qb * 256 + w * 32 + q31) * 128 + head * 4, 0, 16);
  }
  asm volatile("s_waitcnt vmcnt(0)" : "+v"(dummy_keep) :: "memory");
  WAITL0(); BAR();
  STAMP(2, 0); STAMP(1, 3);
  using C = CR<9>;
  unsigned* cnt = (unsigned*)(ws + WS_CNT + (32 + qb) * 128);
  if (w == 0 && lane == 0) __hip_atomic_fetch_add(cnt, 1u, __ATOMIC_RELAXED, __HIP_MEMORY_SCOPE_AGENT);
  if (w >= 6) return;
  const int row0 = qb * 256 + (head >> 3) * 64, tn = head & 7;
  if (w >= 4) {
    const int p = w - 4;
    const __amdgpu_buffer_rsrc_t srdA = mksrd(ws + WS_AO, S_ * 1152), srdB = mksrd(ws + WS_WO + (size_t)tn * 9 * WR_TB, 9 * WR_TB);
    const unsigned ldsb = (unsigned)(size_t)smem, mA = ldsb + p * 4096, mB = ldsb + C::ABYTES + p * 5120;
    const int c0 = (lane & 7) ^ (lane >> 4), c1 = (lane & 7) ^ (4 + (lane >> 4));
    const int rl = (head >> 3) * 64 + p * 32 + (lane >> 3);
    const unsigned va0 = (unsigned)(qb * 32 + (c0 >> 1)) * 8192 + rl * 32 + (c0 & 1) * 16, va1 = (unsigned)(qb * 32 + (c1 >> 1)) * 8192 + rl * 32 + (c1 & 1) * 16;
    const unsigned vt0 = S_ * 1024 + (qb * 256 + rl) * 128 + c0 * 16, vt1 = S_ * 1024 + (qb * 256 + rl) * 128 + c1 * 16, vb = p * 5120 + lane * 16;
    auto issueA = [&](int kt) {
      const unsigned sa = mA + (kt & 7) * C::STAGE;
      if (kt < 8) {
#pragma unroll
        for (int j = 0; j < 4; ++j) { const unsigned v = ((j & 1) ? va1 : va0) + j * 8 * 32; DMAQ_SC1(sa, j * 1024, v, srdA, (unsigned)(kt * 4 * 8192)); }
      } else {
#pragma unroll
        for (int j = 0; j < 4; ++j) { const unsigned v = ((j & 1) ? vt1 : vt0) + j * 8 * 128; DMAQ_SC1(sa, j * 1024, v, srdA, 0u); }
      }
    };
    auto issueB = [&](int kt) {
      const unsigned sb = mB + (kt & 7) * C::STAGE, sk = kt * WR_TB;
#pragma unroll
      for (int j = 0; j < 5; ++j) { const unsigned v = vb + j * 1024; DMAQ(sb, j * 1024, v, srdB, sk); }
    };
#pragma unroll
    for (int s = 0; s < 7; ++s) issueB(s);
    if (lane == 0) spin_until(cnt, 32u);
    asm volatile("" ::: "memory");
#pragma unroll
    for (int s = 0; s < 7; ++s) issueA(s);
    WAITV(24); BAR(); issueA(7); issueB(7);
    WAITV(29); BAR(); issueA(8); issueB(8);
    WAITV(34); BAR(); WAITV(30); BAR(); WAITV(26); BAR(); WAITV(22); BAR(); WAITV(18); BAR(); WAITV(9); BAR(); WAITV(0); BAR();
    BAR();
    return;
  }
  {
    const int g = lane >> 4, fr = lane & 15;
    const int tok = row0 + w * 16 + fr;
    const size_t ro = (size_t)tok * D_ + tn * 72 + 4 * g;
    u32x2 r[5];
#pragma unroll
    for (int j = 0; j < 5; ++j) r[j] = *(const u32x2*)(x + ro + ((j < 4 || g < 2) ? 16 * j : 0));
    f32x4 acc[1][5]; float rs[1], rq[1];
    consumer<C, 1, false>(smem, w * 16, lane, acc, rs, rq, ws, 7);
    u16* out = (u16*)(ws + WS_H);
    float s1 = 0.f, s2 = 0.f;
#pragma unroll
    for (int j = 0; j < 5; ++j) {
      if (j < 4 || g < 2) {
        u32x2 v;
        const unsigned p0 = cvtpk(acc[0][j][0], acc[0][j][1]), p1 = cvtpk(acc[0][j][2], acc[0][j][3]);
        v[0] = cvtpk(bflo(r[j][0]) + bflo(p0), bfhi(r[j][0]) + bfhi(p0));
        v[1] = cvtpk(bflo(r[j][1]) + bflo(p1), bfhi(r[j][1]) + bfhi(p1));
        *(u32x2*)(out + ro + 16 * j) = v;
        const float a0 = bflo(v[0]), a1 = bfhi(v[0]), a2 = bflo(v[1]), a3 = bfhi(v[1]);
        s1 += (a0 + a1) + (a2 + a3);
        s2 = fmaf(a0, a0, fmaf(a1, a1, fmaf(a2, a2, fmaf(a3, a3, s2))));
      }
    }
    s1 += xor16(s1); s1 += xor32(s1); s2 += xor16(s2); s2 += xor32(s2);
    if (g == 0) *(f32x2*)(ws + WS_HST + ((size_t)tok * 8 + tn) * 8) = f32x2{s1, s2};
  }
  STAMP_END(2);
}

constexpr int MST = 36864, MNST = 4;
constexpr int M_CSA = MNST * MST, M_CSB = M_CSA + 576, M_RS = M_CSB + 576, M_S2 = M_RS + 1024, M_LDS = M_S2 + 1024;
__global__ __launch_bounds__(448, 2) void k_mix(const u16* __restrict__ wsp, const u16* __restrict__ bsp, char* __restrict__ ws, u16* __restrict__ outp) {
  extern __shared__ __attribute__((aligned(16))) char smem[];
  const int tid = threadIdx.x, w = rfl(tid >> 6);
  const int b = blockIdx.x, xcd = b & 7, idx = b >> 3;
  const int tm = (xcd & 1) * 8 + (idx & 7), tn = (xcd >> 1) * 4 + (idx >> 3);
  char* ws_dbg = ws;
  unsigned* cnt1 = (unsigned*)(ws + WS_CNT + tm * 128);
  unsigned* cnt2 = (unsigned*)(ws + WS_CNT + (16 + tn) * 128);
  unsigned* cnt3 = (unsigned*)(ws + WS_CNT + (40 + tm) * 128);
  using CD = CR<36>;
  const int drow0 = tm * 128 + (tn >> 3) * 64, dtn = tn & 7;
  if (__builtin_expect(w == 4 || w == 5, 1)) {
    const int lane = tid & 63, p = w - 4;
    if (p == 0) STAMPW(3, 4);
    const unsigned ldsb = (unsigned)(size_t)smem;
    const unsigned mA = ldsb + p * 8192, mB = ldsb + 16384 + p * 9216;
    const unsigned sw0 = ((lane & 7) ^ (lane >> 4)) << 4, sw1 = ((lane & 7) ^ (4 + (lane >> 4))) << 4;
    const int g = lane >> 4;
    constexpr int NQ = 17;
    {
      const __amdgpu_buffer_rsrc_t srdA = mksrd((const u16*)(ws + WS_H) + (size_t)tm * 128 * D_, 128 * D_ * 2), srdB = mksrd(ws + WS_WIN, 32 * 9 * WIN_TB);
      const unsigned va0 = (p * 64 + (lane >> 3)) * (D_ * 2) + sw0, va1 = (p * 64 + (lane >> 3)) * (D_ * 2) + sw1, vb = p * 9216 + lane * 16;
      auto issue = [&](int s) {
        const int ph = s >= 9, kt = s - 9 * ph;
        const unsigned so = (s & 3) * MST, sa = mA + so, sb = mB + so;
        const unsigned soA = kt * 128, soB = ((ph ? tn : tn + 16) * 9 + kt) * WIN_TB;
#pragma unroll
        for (int j = 0; j < 8; ++j) { const unsigned v = ((j & 1) ? va1 : va0) + j * 8 * (D_ * 2); DMAQ(sa, j * 1024, v, srdA, soA); }
#pragma unroll
        for (int j = 0; j < 9; ++j) { const unsigned v = vb + j * 1024; DMAQ(sb, j * 1024, v, srdB, soB); }
      };
      issue(0); issue(1); issue(2);
      if (p == 0) STAMPW(3, 5);
      for (int s = 0; s < 15; ++s) { WAITV(2 * NQ); BAR(); issue(s + 3); }
      WAITV(2 * NQ); BAR(); WAITV(NQ); BAR(); WAITV(0); BAR();
    }
    BAR();
    BAR();
    if (p == 0) STAMPW(5, 0);
    {
      const __amdgpu_buffer_rsrc_t srdA = mksrd(wsp + (size_t)tm * 128 * S_, 128 * S_ * 2), srdB = mksrd((const u16*)(ws + WS_Z2N) + (size_t)tn * 144 * S_, 144 * S_ * 2);
      const unsigned va0 = (p * 64 + (lane >> 3)) * (S_ * 2) + sw0, va1 = (p * 64 + (lane >> 3)) * (S_ * 2) + sw1;
      const unsigned vb0 = (p * 72 + (lane >> 3)) * (S_ * 2) + (p ? sw1 : sw0), vb1 = (p * 72 + (lane >> 3)) * (S_ * 2) + (p ? sw0 : sw1);
      auto issueA = [&](int kt) {
        const unsigned sa = mA + (kt & 3) * MST, sk = kt * 128;
#pragma unroll
        for (int j = 0; j < 8; ++j) { const unsigned v = ((j & 1) ? va1 : va0) + j * 8 * (S_ * 2); DMAQ(sa, j * 1024, v, srdA, sk); }
      };
      auto issueB = [&](int kt) {
        const unsigned sb = mB + (kt & 3) * MST, sk = kt * 128;
#pragma unroll
        for (int j = 0; j < 9; ++j) { const unsigned v = ((j & 1) ? vb1 : vb0) + j * 8 * (S_ * 2); DMAQ_SC1(sb, j * 1024, v, srdB, sk); }
      };
      auto issue = [&](int kt) { issueA(kt); issueB(kt); };
      issueA(0); issueA(1); issueA(2);
      if (lane == 0) spin_until(cnt2, 16u);
      asm volatile("" ::: "memory");
      if (p == 0) STAMPW(5, 1);
      if (p == 0) STAMPW(5, 2);
      issueB(0); issueB(1); issueB(2);
      if (p == 0) STAMPW(5, 3);
      WAITV(18); BAR(); issue(3);
      WAITV(9 + NQ); BAR(); issue(4);
      for (int kt = 2; kt < 29; ++kt) { WAITV(2 * NQ); BAR(); issue(kt + 3); }
      WAITV(2 * NQ); BAR(); WAITV(NQ); BAR(); WAITV(0); BAR();
    }
    BAR();
    BAR();
    {
      const __amdgpu_buffer_rsrc_t srdA = mksrd((const u16*)(ws + WS_U) + (size_t)drow0 * FH, 64 * FH * 2), srdB = mksrd(ws + WS_WOUT + (size_t)dtn * 36 * WR_TB, 36 * WR_TB);
      const unsigned mA2 = ldsb + p * 4096, mB2 = ldsb + CD::ABYTES + p * 5120;
      const unsigned va0 = (p * 32 + (lane >> 3)) * (FH * 2) + sw0, va1 = (p * 32 + (lane >> 3)) * (FH * 2) + sw1, vb = p * 5120 + lane * 16;
      auto issueA = [&](int kt) {
        const unsigned sa = mA2 + (kt & 7) * CD::STAGE, sk = kt * 128;
#pragma unroll
        for (int j = 0; j < 4; ++j) { const unsigned v = ((j & 1) ? va1 : va0) + j * 8 * (FH * 2); DMAQ_SC1(sa, j * 1024, v, srdA, sk); }
      };
      auto issueB = [&](int kt) {
        const unsigned sb = mB2 + (kt & 7) * CD::STAGE, sk = kt * WR_TB;
#pragma unroll
        for (int j = 0; j < 5; ++j) { const unsigned v = vb + j * 1024; DMAQ(sb, j * 1024, v, srdB, sk); }
      };
#pragma unroll
      for (int s = 0; s < 7; ++s) issueB(s);
      if (lane == 0) spin_until(cnt3, 16u);
      asm volatile("" ::: "memory");
#pragma unroll
      for (int s = 0; s < 7; ++s) issueA(s);
      WAITV(24); BAR(); issueA(7); issueB(7);
      WAITV(29); BAR(); issueA(8); issueB(8);
      WAITV(34); BAR(); issueA(9); issueB(9);
      WAITV(39); BAR(); issueA(10); issueB(10);
      WAITV(44); BAR(); issueA(11); issueB(11);
      WAITV(49); BAR(); issueA(12); issueB(12);
      for (int kt = 6; kt < 29; ++kt) { WAITV(54); BAR(); issueA(kt + 7); issueB(kt + 7); }
      WAITV(54); BAR(); WAITV(45); BAR(); WAITV(36); BAR(); WAITV(27); BAR(); WAITV(18); BAR(); WAITV(9); BAR(); WAITV(0); BAR();
      BAR();
    }
    return;
  }
  if (w == 6) {
    const int lane = tid & 63;
    f32x2* rst = (f32x2*)(smem + M_RS);
#pragma unroll
    for (int i = 0; i < 2; ++i) {
      const f32x4* q = (const f32x4*)(ws + WS_HST + (size_t)(tm * 128 + 64 * i + lane) * 64);
      const f32x4 a = q[0], bb = q[1], c = q[2], d = q[3];
      const float s1 = (a[0] + a[2]) + (bb[0] + bb[2]) + (c[0] + c[2]) + (d[0] + d[2]), s2 = (a[1] + a[3]) + (bb[1] + bb[3]) + (c[1] + c[3]) + (d[1] + d[3]);
      rst[64 * i + lane] = ln_stats(s1, s2, 1.f / D_);
    }
    if (lane < 36) {
      *(f32x4*)(smem + M_CSA + lane * 16) = *(const f32x4*)(ws + WS_CS + (size_t)((tn + 16) * 144 + lane * 4) * 4);
      *(f32x4*)(smem + M_CSB + lane * 16) = *(const f32x4*)(ws + WS_CS + (size_t)(tn * 144 + lane * 4) * 4);
    }
    WAITL0();
    for (int s = 0; s < 15; ++s) BAR();
    if (lane == 0) spin_until(cnt1, 16u);
    asm volatile("" ::: "memory");
    const __amdgpu_buffer_rsrc_t srdS = mksrd(ws + WS_ST + (size_t)tm * (16 * 128 * 8), 16 * 128 * 8);
    u32x4 pv[16];
#pragma unroll
    for (int q = 0; q < 16; ++q) pv[q] = __builtin_amdgcn_raw_buffer_load_b128(srdS, (q * 128 + 2 * lane) * 8, 0, 16);
    SB(); BAR(); BAR(); SB();
    f32x4 t4 = {0.f, 0.f, 0.f, 0.f};
#pragma unroll
    for (int q = 0; q < 16; ++q) t4 += __builtin_bit_cast(f32x4, pv[q]);
    {
      const f32x2 q0 = ln_stats(t4[0], t4[1], 1.f / FH), q1 = ln_stats(t4[2], t4[3], 1.f / FH);
      *(f32x4*)(smem + M_S2 + lane * 16) = f32x4{q0[1], -q0[0] * q0[1], q1[1], -q1[0] * q1[1]};
    }
    WAITL0();
    BAR();
    BAR();
    return;
  }
  const int lane = tid & 63, g = lane >> 4, fr = lane & 15, f = fr >> 1;
  const int l0 = fr * 128 + ((g ^ f) << 4), l1 = fr * 128 + (((4 + g) ^ f) << 4);
  const int tb = 16384 + (16 * (g >> 1) + 4 * (g & 1) + (fr >> 2)) * 288 + (lane & 3) * 8;
  const int tok0 = tm * 128 + w * 32;
  f32x4 acc[2][9];
  bf16x8 a0[2], b0[9], a1[2], b1[9];
  auto zero_acc = [&]() {
#pragma unroll
    for (int i = 0; i < 2; ++i)
#pragma unroll
      for (int j = 0; j < 9; ++j) acc[i][j] = f32x4{0.f, 0.f, 0.f, 0.f};
  };
  auto mm = [&](bf16x8* a, bf16x8* bb) {
#pragma unroll
    for (int j = 0; j < 9; ++j)
#pragma unroll
      for (int i = 0; i < 2; ++i) acc[i][j] = MFMA16(bb[j], a[i], acc[i][j]);
  };
  auto rdKN = [&](bf16x8* a, bf16x8* bb, int s, int ks) {
    const char* sb = smem + (s & 3) * MST;
#pragma unroll
    for (int i = 0; i < 2; ++i) a[i] = *(const bf16x8*)(sb + w * 4096 + i * 2048 + (ks ? l1 : l0));
#pragma unroll
    for (int j = 0; j < 9; ++j) { const char* q = sb + tb + ks * (32 * 288) + j * 32; bb[j] = cat8(trread(q), trread(q + 8 * 288)); }
  };
  auto rdNK = [&](bf16x8* a, bf16x8* bb, int s, int ks) {
    const char* sb = smem + (s & 3) * MST + (ks ? l1 : l0);
#pragma unroll
    for (int i = 0; i < 2; ++i) a[i] = *(const bf16x8*)(sb + w * 4096 + i * 2048);
#pragma unroll
    for (int j = 0; j < 9; ++j) bb[j] = *(const bf16x8*)(sb + 16384 + j * 2048);
  };
#define SCHED(id, ND, RPG) _Pragma("unroll") for (int q = 0; q < 18; ++q) { __builtin_amdgcn_sched_group_barrier(0x008, 1, id); \
    if (q * RPG < ND) __builtin_amdgcn_sched_group_barrier(0x080, RPG, id); }
  auto gelu = [&](u32x2 (&zp)[2][9], int csoff, f32x2 (&sum)[2]) {
    const float c1 = -2.f * 0.7978845608028654f * 1.4426950408889634f, c2 = c1 * 0.044715f;
    const float* cs = (const float*)(smem + csoff);
    const f32x2* rst = (const f32x2*)(smem + M_RS) + w * 32;
    float nmu[2], k1[2], k3[2], irs[2];
    f32x2 s1[2], s2[2];
#pragma unroll
    for (int i = 0; i < 2; ++i) {
      const f32x2 st = rst[16 * i + fr];
      nmu[i] = -st[0]; k1[i] = c1 * st[1]; k3[i] = c2 * st[1] * st[1] * st[1]; irs[i] = __builtin_amdgcn_rcpf(st[1]);
      s1[i] = f32x2{0.f, 0.f}; s2[i] = f32x2{0.f, 0.f};
    }
    f32x4 c4 = *(const f32x4*)(cs + 4 * g);
#pragma unroll
    for (int j = 0; j < 9; ++j) {
      const f32x4 c4n = *(const f32x4*)(cs + 16 * (j < 8 ? j + 1 : 8) + 4 * g);
      SB();
#pragma unroll
      for (int i = 0; i < 2; ++i)
#pragma unroll
        for (int hh = 0; hh < 2; ++hh) {
          const f32x2 a2 = {acc[i][j][2 * hh], acc[i][j][2 * hh + 1]}, c2v = {c4[2 * hh], c4[2 * hh + 1]};
          const f32x2 t = f32x2{nmu[i], nmu[i]} * c2v + a2;
          const f32x2 y = t * (t * t * f32x2{k3[i], k3[i]} + f32x2{k1[i], k1[i]});
          const f32x2 e = {__builtin_amdgcn_exp2f(y[0]), __builtin_amdgcn_exp2f(y[1])};
          const f32x2 d = e * f32x2{irs[i], irs[i]} + f32x2{irs[i], irs[i]};
          const f32x2 z = t * f32x2{__builtin_amdgcn_rcpf(d[0]), __builtin_amdgcn_rcpf(d[1])};
          s1[i] += z; s2[i] += z * z;
          zp[i][j][hh] = cvtpk(z[0], z[1]);
        }
      VREG(zp[0][j]); VREG(zp[1][j]);
      c4 = c4n;
      SB();
    }
#pragma unroll
    for (int i = 0; i < 2; ++i) sum[i] = f32x2{s1[i][0] + s1[i][1], s2[i][0] + s2[i][1]};
  };
  const __amdgpu_buffer_rsrc_t srdS = mksrd(ws + WS_ST + (size_t)tm * (16 * 128 * 8), 16 * 128 * 8);
  zero_acc();
  STAMP(3, 0);
  BAR();
  STAMP(3, 1);
  rdKN(a0, b0, 0, 0);
  u32x2 z2p[2][9];
  auto stageAB = [&](int s) {
    SB(); rdKN(a1, b1, s, 1); mm(a0, b0); SCHED(0, 20, 2) SB();
    WAITL0(); BAR();
    SB(); rdKN(a0, b0, s + 1, 0); mm(a1, b1); SCHED(1, 20, 2)
  };
  for (int s = 0; s < 9; ++s) stageAB(s);
  MFMA_DRAIN();
  STAMP(5, 6);
  {
    f32x2 sum[2];
    gelu(z2p, M_CSA, sum);
#pragma unroll
    for (int i = 0; i < 2; ++i) {
      f32x2 v = sum[i];
      v[0] += xor16(v[0]); v[0] += xor32(v[0]); v[1] += xor16(v[1]); v[1] += xor32(v[1]);
      if (g == 0) __builtin_amdgcn_raw_buffer_store_b64(__builtin_bit_cast(u32x2, v), srdS, (tn * 128 + w * 32 + 16 * i + fr) * 8, 0, 16);
    }
    asm volatile("s_waitcnt vmcnt(0)" ::: "memory");
    zero_acc();
  }
  SB();
  STAMP(5, 4);
  stageAB(9);
  if (w == 0 && lane == 0) __hip_atomic_fetch_add(cnt1, 1u, __ATOMIC_RELAXED, __HIP_MEMORY_SCOPE_AGENT);
  for (int s = 10; s < 17; ++s) stageAB(s);
  SB(); rdKN(a1, b1, 17, 1); mm(a0, b0); mm(a1, b1); MFMA_DRAIN();
  WAITL0(); BAR();
  STAMP(3, 2);
  {
    const f32x2* st2 = (const f32x2*)(smem + M_S2) + w * 32;
    char* reg = smem + w * (32 * 304);
#pragma unroll
    for (int i = 0; i < 2; ++i) {
      const f32x2 st = st2[16 * i + fr];
#pragma unroll
      for (int j = 0; j < 9; ++j) {
        u32x2 v;
        v[0] = cvtpk(fmaf(bflo(z2p[i][j][0]), st[0], st[1]), fmaf(bfhi(z2p[i][j][0]), st[0], st[1]));
        v[1] = cvtpk(fmaf(bflo(z2p[i][j][1]), st[0], st[1]), fmaf(bfhi(z2p[i][j][1]), st[0], st[1]));
        *(u32x2*)(reg + (16 * i + fr) * 304 + (16 * j + 4 * g) * 2) = v;
      }
    }
    WAITL0();
    const __amdgpu_buffer_rsrc_t so = mksrd((u16*)(ws + WS_Z2N) + (size_t)(tn * 144) * S_ + tok0, 144 * S_ * 2);
    const char* rp = reg + (8 * g + (fr >> 2)) * 304 + (lane & 3) * 8;
#pragma unroll
    for (int j = 0; j < 9; ++j)
      __builtin_amdgcn_raw_buffer_store_b128(__builtin_bit_cast(u32x4, cat8(trread(rp + j * 32), trread(rp + 4 * 304 + j * 32))), so, (j * 16 + fr) * (S_ * 2) + 8 * g * 2, 0, 16);
    asm volatile("s_waitcnt vmcnt(0)" ::: "memory");
    WAITL0(); BAR();
    if (w == 0 && lane == 0) __hip_atomic_fetch_add(cnt2, 1u, __ATOMIC_RELAXED, __HIP_MEMORY_SCOPE_AGENT);
  }
  u32x2 z1p[2][9];
  { f32x2 sum[2]; gelu(z1p, M_CSB, sum); }
  const unsigned bias0 = bsp[tok0 + fr], bias1 = bsp[tok0 + 16 + fr];
  zero_acc();
  STAMP(4, 0);
  BAR();
  STAMP(4, 1);
  rdNK(a0, b0, 0, 0);
  for (int kt = 0; kt < 31; ++kt) {
    SB(); rdNK(a1, b1, kt, 1); mm(a0, b0); SCHED(0, 11, 1) SB();
    WAITL0(); BAR();
    SB(); rdNK(a0, b0, kt + 1, 0); mm(a1, b1); SCHED(1, 11, 1)
  }
  SB(); rdNK(a1, b1, 31, 1); mm(a0, b0); mm(a1, b1); MFMA_DRAIN();
  WAITL0(); BAR();
  SB();
  STAMP(4, 2);
#undef SCHED
  char* reg = smem + w * (32 * 304);
#pragma unroll
  for (int i = 0; i < 2; ++i) {
    const float bi = bflo(i ? bias1 : bias0);
#pragma unroll
    for (int j = 0; j < 9; ++j) {
      u32x2 v;
      const unsigned e0 = cvtpk(acc[i][j][0], acc[i][j][1]), e1 = cvtpk(acc[i][j][2], acc[i][j][3]);
      const unsigned g0 = cvtpk(bflo(e0) + bi, bfhi(e0) + bi), g1 = cvtpk(bflo(e1) + bi, bfhi(e1) + bi);
      v[0] = cvtpk(bflo(z1p[i][j][0]) * bflo(g0), bfhi(z1p[i][j][0]) * bfhi(g0));
      v[1] = cvtpk(bflo(z1p[i][j][1]) * bflo(g1), bfhi(z1p[i][j][1]) * bfhi(g1));
      *(u32x2*)(reg + (16 * i + fr) * 304 + (16 * j + 4 * g) * 2) = v;
    }
  }
  WAITL0();
  const __amdgpu_buffer_rsrc_t so = mksrd((u16*)(ws + WS_U) + (size_t)tok0 * FH + tn * 144, 32 * FH * 2);
#pragma unroll
  for (int i = 0; i < 9; ++i) {
    const int c = i * 64 + lane, row = c / 18, ch = c - row * 18;
    __builtin_amdgcn_raw_buffer_store_b128(*(const u32x4*)(reg + row * 304 + ch * 16), so, row * (FH * 2) + ch * 16, 0, 16);
  }
  asm volatile("s_waitcnt vmcnt(0)" ::: "memory");
  WAITL0(); BAR();
  STAMP(4, 3);
  if (w == 0 && lane == 0) __hip_atomic_fetch_add(cnt3, 1u, __ATOMIC_RELAXED, __HIP_MEMORY_SCOPE_AGENT);
  {
    const int tok = drow0 + w * 16 + fr;
    const size_t ro = (size_t)tok * D_ + dtn * 72 + 4 * g;
    const u16* res = (const u16*)(ws + WS_H);
    u32x2 r[5];
#pragma unroll
    for (int j = 0; j < 5; ++j) r[j] = *(const u32x2*)(res + ro + ((j < 4 || g < 2) ? 16 * j : 0));
    f32x4 acd[1][5]; float rs[1], rq[1];
    consumer<CD, 1, false>(smem, w * 16, lane, acd, rs, rq, ws, 6);
#pragma unroll
    for (int j = 0; j < 5; ++j) {
      if (j < 4 || g < 2) {
        const unsigned p0 = cvtpk(acd[0][j][0], acd[0][j][1]), p1 = cvtpk(acd[0][j][2], acd[0][j][3]);
        u32x2 v;
        v[0] = cvtpk(bflo(r[j][0]) + bflo(p0), bfhi(r[j][0]) + bfhi(p0));
        v[1] = cvtpk(bflo(r[j][1]) + bflo(p1), bfhi(r[j][1]) + bfhi(p1));
        *(u32x2*)(outp + ro + 16 * j) = v;
      }
    }
  }
}

extern "C" void kernel_launch(void* const* d_in, const int* in_sizes, int n_in, void* d_out, int out_size, void* d_ws, size_t ws_size, hipStream_t stream) {
  const u16* x = (const u16*)d_in[0];
  const u16* wqkv = (const u16*)d_in[1];
  const u16* wo = (const u16*)d_in[2];
  const u16* win = (const u16*)d_in[4];
  const u16* wsp = (const u16*)d_in[5];
  const u16* bsp = (const u16*)d_in[6];
  const u16* wout = (const u16*)d_in[7];
  char* ws = (char*)d_ws;
  (void)hipFuncSetAttribute((const void*)k_qkv, hipFuncAttributeMaxDynamicSharedMemorySize, C1::LDS);
  (void)hipFuncSetAttribute((const void*)k_attn, hipFuncAttributeMaxDynamicSharedMemorySize, ATT_LDS);
  (void)hipFuncSetAttribute((const void*)k_mix, hipFuncAttributeMaxDynamicSharedMemorySize, M_LDS);
  hipLaunchKernelGGL(k_qkv, dim3(256), dim3(512), C1::LDS, stream, x, wqkv, win, wout, wo, ws);
  hipLaunchKernelGGL(k_attn, dim3(256), dim3(512), ATT_LDS, stream, ws, wsp, x);
  hipLaunchKernelGGL(k_mix, dim3(256), dim3(448), M_LDS, stream, wsp, bsp, ws, (u16*)d_out);
}
```
